```python
import torch

def _softmax(x, axis=-1):
    return torch.softmax(x, dim=axis)
def _gelu(x):
    return torch.nn.functional.gelu(x, approximate="tanh")
def _silu(x):
    return torch.nn.functional.silu(x)
def _sigmoid(x):
    return torch.sigmoid(x)
def _layernorm(x, axis=-1, eps=1e-5):
    m = x.mean(dim=axis, keepdim=True)
    v = x.var(dim=axis, keepdim=True, unbiased=False)
    return (x - m) / torch.sqrt(v + eps)
def _scatter_add(tgt, idx, src, axis=0):
    return tgt.clone().index_add_(axis, idx, src)
def _segment_sum(src, seg, n):
    out = torch.zeros((n,) + src.shape[1:], dtype=src.dtype)
    return out.index_add_(0, seg, src)
def _pad(x, pw):
    flat = tuple(v for pair in reversed(pw) for v in pair)
    return torch.nn.functional.pad(x, flat)

kernel_name = 'sequential_preLN__mha_block__moe_arch_mod__prompt'

def setup_inputs():

    _w = (lambda a: (lambda t: t.to(torch.bfloat16) if t.is_floating_point() else t)(torch.as_tensor(a)))
    def _A_setup():
        B, S, D, H = 1, 2048, 512, 32
        return {
            "x": _w((lambda *s: torch.randn(*s, dtype=torch.bfloat16) * 0.05)(B, S, D)),
            "wqkv": _w((lambda *s: torch.randn(*s, dtype=torch.bfloat16) * 0.05)(D, 3 * D)),
            "wo": _w((lambda *s: torch.randn(*s, dtype=torch.bfloat16) * 0.05)(D, D)),
        }
    _A_in = _A_setup()

    def _B_setup():
        B, S, D, F, C = 1, 2048, 512, 1376, 8
        return {
            "x": _w((lambda *s: torch.randn(*s, dtype=torch.bfloat16) * 0.05)(B, S, D)),
            "w_r": _w((lambda *s: torch.randn(*s, dtype=torch.bfloat16) * 0.05)(D)),
            "w_gate": _w((lambda *s: torch.randn(*s, dtype=torch.bfloat16) * 0.05)(D, F)),
            "w_up": _w((lambda *s: torch.randn(*s, dtype=torch.bfloat16) * 0.05)(D, F)),
            "w_down": _w((lambda *s: torch.randn(*s, dtype=torch.bfloat16) * 0.05)(F, D)),
        }
    _B_in = _B_setup()

    return {
        "A_x": _A_in["x"],
        "A_wqkv": _A_in["wqkv"],
        "A_wo": _A_in["wo"],
        "B_x": _B_in["x"],
        "B_w_r": _B_in["w_r"],
        "B_w_gate": _B_in["w_gate"],
        "B_w_up": _B_in["w_up"],
        "B_w_down": _B_in["w_down"],
    }

def reference(A_x, A_wqkv, A_wo, B_x, B_w_r, B_w_gate, B_w_up, B_w_down):
    def _A_ref(x, wqkv, wo):
        B, S, D = x.shape
        H = 32
        Dh = D // H
        qkv = (lambda x, *d: x.permute(*d))((x @ wqkv).reshape(B, S, 3, H, Dh), 2, 0, 3, 1, 4)
        q, k, v = qkv[0], qkv[1], qkv[2]
        scores = torch.einsum("bhqd,bhkd->bhqk", q, k) * (Dh ** -0.5)
        p = _softmax(scores, axis=-1)
        o = torch.einsum("bhqk,bhkd->bhqd", p, v)
        return (lambda x, *d: x.permute(*d))(o, 0, 2, 1, 3).reshape(B, S, D) @ wo

    def _B_ref(x, w_r, w_gate, w_up, w_down):
        B, S, D = x.shape
        C = 8
        r = x @ w_r
        idx = torch.argsort(-r, axis=-1)[:, :C]
        idx_e = (lambda x, n, axis: torch.repeat_interleave(x, n, dim=axis))(idx.reshape(B, C, 1), D, axis=2)
        x_sel = (lambda x, i, axis: torch.take_along_dim(x, i, dim=axis))(x, idx_e, axis=1)
        g = _silu(x_sel @ w_gate)
        u = x_sel @ w_up
        y = (g * u) @ w_down
        r_sel = _sigmoid((lambda x, i, axis: torch.take_along_dim(x, i, dim=axis))(r, idx, axis=1))
        y = y * r_sel.reshape(B, C, 1)
        b_off = (torch.arange(B) * S).reshape(B, 1)
        flat_idx = (idx + b_off).reshape(B * C)
        zero = (x * 0.0).reshape(B * S, D)
        upd = _scatter_add(zero, flat_idx, y.reshape(B * C, D), axis=0)
        return x + upd.reshape(B, S, D)

    x = A_x
    h = x + _A_ref(_layernorm(x, axis=-1), A_wqkv, A_wo)
    h2 = h + _B_ref(_layernorm(h, axis=-1), B_w_r, B_w_gate, B_w_up, B_w_down)
    return h2
```

```cpp
#include <hip/hip_runtime.h>
#include <hip/hip_bf16.h>
#include <stdint.h>

typedef short bf16x8 __attribute__((ext_vector_type(8)));
typedef short bf16x4 __attribute__((ext_vector_type(4)));
typedef float f32x16 __attribute__((ext_vector_type(16)));
typedef float f32x4 __attribute__((ext_vector_type(4)));
typedef float f32x2 __attribute__((ext_vector_type(2)));
typedef __bf16 bf16x2_t __attribute__((ext_vector_type(2)));
typedef unsigned u32x4 __attribute__((ext_vector_type(4)));
typedef unsigned u32x2 __attribute__((ext_vector_type(2)));
#define LAS __attribute__((address_space(3)))
#define GAS __attribute__((address_space(1)))
#define DI __device__ __forceinline__
#define MFMA32(a, b, c) __builtin_amdgcn_mfma_f32_32x32x16_bf16((a), (b), (c), 0, 0, 0)
#define MFMA4(a, b, c) __builtin_amdgcn_mfma_f32_4x4x4bf16_1k((a), (b), (c), 0, 0, 0)

constexpr int S = 2048, D = 512, H = 32, F = 1376, TOPC = 8;
constexpr float LN_EPS = 1e-5f;
constexpr float QSCALE = 0.25f * 1.4426950408889634f;

constexpr int KV_TILE = 2080;
constexpr int KV_HEAD = 64 * KV_TILE;
constexpr size_t WS_Q = 0;
constexpr size_t WS_KV = WS_Q + (size_t)H * 64 * 1024;
constexpr size_t WS_O = WS_KV + (size_t)H * KV_HEAD;
constexpr size_t WS_H = WS_O + (size_t)64 * H * 1024;
constexpr size_t WS_LN = WS_H + (size_t)S * D * 2;
constexpr size_t WS_R = WS_LN + (size_t)S * D * 2;
constexpr size_t WS_ACT = WS_R + (size_t)S * 4;
constexpr size_t WS_IDX = WS_ACT + 32768;

DI unsigned pack_bf16(float lo, float hi) {
  f32x2 v = {lo, hi};
  bf16x2_t r = __builtin_convertvector(v, bf16x2_t);
  return __builtin_bit_cast(unsigned, r);
}
DI float bf_lo(unsigned v) { return __uint_as_float(v << 16); }
DI float bf_hi(unsigned v) { return __uint_as_float(v & 0xffff0000u); }
DI float rbf(float x) { return bf_lo(pack_bf16(x, x) & 0xffffu); }
DI float xor32(float v) {
  auto r = __builtin_amdgcn_permlane32_swap(__float_as_uint(v), __float_as_uint(v), false, false);
  return __uint_as_float((threadIdx.x & 32) ? r[0] : r[1]);
}
DI float sum_halves(float v) {
  auto r = __builtin_amdgcn_permlane32_swap(__float_as_uint(v), __float_as_uint(v), false, false);
  return __uint_as_float(r[0]) + __uint_as_float(r[1]);
}
DI float max_halves(float v) {
  auto r = __builtin_amdgcn_permlane32_swap(__float_as_uint(v), __float_as_uint(v), false, false);
  return fmaxf(__uint_as_float(r[0]), __uint_as_float(r[1]));
}
DI void glds16(const void* g, unsigned lds_base) {
  unsigned sv;
  asm volatile("s_mov_b32 %0, m0\n\ts_mov_b32 m0, %2\n\ts_nop 0\n\tglobal_load_lds_dwordx4 %1, off\n\ts_mov_b32 m0, %0"
               : "=&s"(sv) : "v"(g), "s"(lds_base) : "memory");
}
#define WAIT_VM(N) asm volatile("s_waitcnt vmcnt(" #N ")" ::: "memory")
#define WAIT_LGKM0() asm volatile("s_waitcnt lgkmcnt(0)" ::: "memory")
#define BARRIER() asm volatile("s_waitcnt lgkmcnt(0)\n\ts_barrier" ::: "memory")
typedef short v4i16_t __attribute__((ext_vector_type(4)));
DI bf16x4 tr_read(unsigned addr) {
  return __builtin_bit_cast(bf16x4, __builtin_amdgcn_ds_read_tr16_b64_v4i16((LAS v4i16_t*)(uintptr_t)addr));
}
DI bf16x8 lds_ld128(unsigned addr) { return *(const LAS bf16x8*)(uintptr_t)addr; }
DI f32x4 lds_ldf4(unsigned addr) { return *(const LAS f32x4*)(uintptr_t)addr; }
DI void lds_stf(unsigned addr, float v) { *(LAS float*)(uintptr_t)addr = v; }
DI float lds_ldf(unsigned addr) { return *(const LAS float*)(uintptr_t)addr; }
DI bf16x8 cat8(bf16x4 a, bf16x4 b) { return __builtin_shufflevector(a, b, 0, 1, 2, 3, 4, 5, 6, 7); }
DI bf16x4 lo4(bf16x8 a) { return __builtin_shufflevector(a, a, 0, 1, 2, 3); }
DI bf16x4 hi4(bf16x8 a) { return __builtin_shufflevector(a, a, 4, 5, 6, 7); }

constexpr int A_LDS_W = 0, A_LDS_X = 98304, A_LDS_BYTES = 163840;

__global__ __launch_bounds__(256, 1) void k_qkv(const uint16_t* __restrict__ x, const uint16_t* __restrict__ w, char* __restrict__ ws) {
  extern __shared__ __attribute__((aligned(16))) char smem[];
  const unsigned lds0 = (unsigned)(uintptr_t)smem;
  const int lane = threadIdx.x & 63, wid = __builtin_amdgcn_readfirstlane(threadIdx.x >> 6);
  const int b = blockIdx.x;
  const int xcd = b & 7, sub = b >> 3;
  const int hp = xcd * 2 + (sub & 1), rb = sub >> 1;
  const int r32 = lane & 31, hh = lane >> 5;
  const int row0 = rb * 128 + wid * 32;

  const char* xsrc;
  {
    const int rr = lane >> 3, slot = lane & 7;
    xsrc = (const char*)x + (size_t)(row0 + rr) * 1024;
    (void)slot;
  }
  const unsigned xdst = lds0 + A_LDS_X + wid * 16384;
  auto issue_x = [&](int half) {
#pragma unroll
    for (int rg = 0; rg < 4; ++rg) {
      const int f = (rg * 4 + (lane >> 4)) & 7;
      const int chunk = (lane & 7) ^ f;
      const char* p = xsrc + rg * 8192 + half * 512 + chunk * 16;
#pragma unroll
      for (int cb = 0; cb < 4; ++cb) glds16(p + cb * 128, xdst + (rg * 4 + cb) * 1024);
    }
  };
  const char* wsrc = (const char*)w + (size_t)(lane >> 2) * 3072 + hp * 64 + (lane & 3) * 16;
  auto issue_w = [&](int half) {
#pragma unroll
    for (int i = 0; i < 4; ++i) {
      const int piece = half * 16 + i * 4 + wid;
#pragma unroll
      for (int t = 0; t < 3; ++t)
        glds16(wsrc + (size_t)piece * 16 * 3072 + t * 1024, lds0 + A_LDS_W + t * 32768 + piece * 1024);
    }
  };
  issue_x(0);
  issue_w(0);
  issue_w(1);

  const unsigned xrd = xdst + (r32 >> 3) * 4096 + (r32 & 7) * 128;
  const int xf = hh ^ ((r32 >> 1) & 7);
  const int g = lane >> 4, i16 = lane & 15;
  const unsigned wrd = lds0 + A_LDS_W + (8 * (g >> 1) + (i16 >> 2)) * 64 + (g & 1) * 32 + (i16 & 3) * 8;
  const unsigned wcs = wrd + wid * 1024;

  f32x16 accq = {}, acck = {}, accv = {};
  f32x4 rsum = {}, cs0 = {}, cs1 = {}, cs2 = {};
  float ss0 = 0.f, ss1 = 0.f;
  const bf16x4 ones4 = {0x3f80, 0x3f80, 0x3f80, 0x3f80};
  bf16x8 xa[16];

#pragma unroll
  for (int half = 0; half < 2; ++half) {
    if (half == 0) WAIT_VM(24); else WAIT_VM(0);
#pragma unroll
    for (int s = 0; s < 16; ++s) xa[s] = lds_ld128(xrd + (s >> 2) * 1024 + (((2 * (s & 3)) ^ xf) * 16));
    if (half == 0) {
      WAIT_LGKM0();
      issue_x(1);
      WAIT_VM(28);
      BARRIER();
    }
#pragma unroll
    for (int s = 0; s < 16; ++s) {
      const unsigned a = wrd + (half * 16 + s) * 1024;
      const bf16x8 wq = cat8(tr_read(a), tr_read(a + 256));
      const bf16x8 wk = cat8(tr_read(a + 32768), tr_read(a + 32768 + 256));
      const bf16x8 wv = cat8(tr_read(a + 65536), tr_read(a + 65536 + 256));
      accq = MFMA32(wq, xa[s], accq);
      acck = MFMA32(wk, xa[s], acck);
      accv = MFMA32(xa[s], wv, accv);
      rsum = MFMA4(ones4, lo4(xa[s]), rsum);
      rsum = MFMA4(ones4, hi4(xa[s]), rsum);
      const u32x4 u = __builtin_bit_cast(u32x4, xa[s]);
#pragma unroll
      for (int j = 0; j < 4; ++j) { const float lo = bf_lo(u[j]), hi = bf_hi(u[j]); ss0 = fmaf(lo, lo, ss0); ss1 = fmaf(hi, hi, ss1); }
      if ((s & 3) == 0) {
        const unsigned c = wcs + (half * 16 + s) * 1024;
        cs0 = MFMA4(ones4, tr_read(c), cs0); cs0 = MFMA4(ones4, tr_read(c + 256), cs0);
        cs1 = MFMA4(ones4, tr_read(c + 32768), cs1); cs1 = MFMA4(ones4, tr_read(c + 32768 + 256), cs1);
        cs2 = MFMA4(ones4, tr_read(c + 65536), cs2); cs2 = MFMA4(ones4, tr_read(c + 65536 + 256), cs2);
      }
    }
    if (half == 0) { WAIT_VM(16); BARRIER(); }
  }

  const float mean = sum_halves(rsum[0]) * (1.f / D);
  const float var = fmaxf(sum_halves(ss0 + ss1) * (1.f / D) - mean * mean, 0.f);
  const float rstd = rsqrtf(var + LN_EPS);
  {
    const float c0 = sum_halves(cs0[0]), c1 = sum_halves(cs1[0]), c2 = sum_halves(cs2[0]);
    if (lane < 32) {
      lds_stf(xdst + r32 * 4, c0); lds_stf(xdst + 128 + r32 * 4, c1); lds_stf(xdst + 256 + r32 * 4, c2);
      lds_stf(xdst + 512 + r32 * 4, mean); lds_stf(xdst + 640 + r32 * 4, rstd);
    }
  }
  BARRIER();
  const unsigned csf = lds0 + A_LDS_X + 1024;
  if (threadIdx.x < 96) {
    const unsigned a = lds0 + A_LDS_X + threadIdx.x * 4;
    lds_stf(csf + threadIdx.x * 4, (lds_ldf(a) + lds_ldf(a + 16384)) + (lds_ldf(a + 32768) + lds_ldf(a + 49152)));
  }
  BARRIER();

  const int tile = rb * 4 + wid;
  {
    char* qdst = ws + WS_Q + ((size_t)(hp * 2) * 64 + tile) * 1024 + lane * 16;
    char* kdst = ws + WS_KV + (size_t)(hp * 2) * KV_HEAD + tile * KV_TILE + lane * 16;
    const float sq = rstd * QSCALE;
#pragma unroll
    for (int e = 0; e < 2; ++e) {
      u32x4 pq, pk;
#pragma unroll
      for (int gq = 0; gq < 2; ++gq) {
        const f32x4 cq = lds_ldf4(csf + (16 * e + 8 * gq + 4 * hh) * 4);
        const f32x4 ck = lds_ldf4(csf + 128 + (16 * e + 8 * gq + 4 * hh) * 4);
        float vq[4], vk[4];
#pragma unroll
        for (int j = 0; j < 4; ++j) {
          vq[j] = (accq[8 * e + 4 * gq + j] - mean * cq[j]) * sq;
          vk[j] = (acck[8 * e + 4 * gq + j] - mean * ck[j]) * rstd;
        }
        pq[2 * gq] = pack_bf16(vq[0], vq[1]); pq[2 * gq + 1] = pack_bf16(vq[2], vq[3]);
        pk[2 * gq] = pack_bf16(vk[0], vk[1]); pk[2 * gq + 1] = pack_bf16(vk[2], vk[3]);
      }
      *(u32x4*)(qdst + (size_t)e * 65536) = pq;
      *(u32x4*)(kdst + (size_t)e * KV_HEAD) = pk;
    }
  }
  {
    const float cv = lds_ldf(csf + 256 + r32 * 4);
    char* vdst = ws + WS_KV + (size_t)(hp * 2 + (r32 >> 4)) * KV_HEAD + tile * KV_TILE + 1024 + (hh * 16 + (r32 & 15)) * 16;
#pragma unroll
    for (int e = 0; e < 2; ++e) {
      u32x4 pv;
#pragma unroll
      for (int gq = 0; gq < 2; ++gq) {
        const f32x4 mu = lds_ldf4(xdst + 512 + (16 * e + 8 * gq + 4 * hh) * 4);
        const f32x4 rs = lds_ldf4(xdst + 640 + (16 * e + 8 * gq + 4 * hh) * 4);
        float vv[4];
#pragma unroll
        for (int j = 0; j < 4; ++j) vv[j] = (accv[8 * e + 4 * gq + j] - mu[j] * cv) * rs[j];
        pv[2 * gq] = pack_bf16(vv[0], vv[1]); pv[2 * gq + 1] = pack_bf16(vv[2], vv[3]);
      }
      *(u32x4*)(vdst + e * 528) = pv;
    }
    if (lane < 4) {
      const u32x4 one = {0x3f803f80u, 0x3f803f80u, 0x3f803f80u, 0x3f803f80u};
      *(u32x4*)(ws + WS_KV + (size_t)(hp * 2 + (lane >> 1)) * KV_HEAD + tile * KV_TILE + 1024 + (lane & 1) * 528 + 512) = one;
    }
  }
}

constexpr int B_LDS_KV = 0, B_LDS_Q = KV_HEAD, B_LDS_BYTES = KV_HEAD + 8192;

DI void attn_range(unsigned kaddr, unsigned vaddr, int t0, int t1, const bf16x8 q, const f32x16& negm, f32x16& o) {
  unsigned ka = kaddr + t0 * KV_TILE, va = vaddr + t0 * KV_TILE;
  for (int t = t0; t < t1; t += 2) {
#pragma unroll
    for (int u = 0; u < 2; ++u) {
      const bf16x8 kf = lds_ld128(ka + u * KV_TILE);
      const bf16x8 v0 = lds_ld128(va + u * KV_TILE);
      const bf16x8 v1 = lds_ld128(va + u * KV_TILE + 528);
      f32x16 s = MFMA32(kf, q, negm);
      u32x4 p0, p1;
#pragma unroll
      for (int j = 0; j < 4; ++j) {
        p0[j] = pack_bf16(__builtin_amdgcn_exp2f(s[2 * j]), __builtin_amdgcn_exp2f(s[2 * j + 1]));
        p1[j] = pack_bf16(__builtin_amdgcn_exp2f(s[8 + 2 * j]), __builtin_amdgcn_exp2f(s[8 + 2 * j + 1]));
      }
      o = MFMA32(v0, __builtin_bit_cast(bf16x8, p0), o);
      o = MFMA32(v1, __builtin_bit_cast(bf16x8, p1), o);
    }
    ka += 2 * KV_TILE; va += 2 * KV_TILE;
  }
}
DI float tile_max(const f32x16& s) {
  float m = fmaxf(fmaxf(s[0], s[1]), s[2]);
#pragma unroll
  for (int j = 3; j < 15; j += 2) m = fmaxf(fmaxf(m, s[j]), s[j + 1]);
  return fmaxf(m, s[15]);
}

__global__ __launch_bounds__(512, 1) void k_attn(char* __restrict__ ws) {
  extern __shared__ __attribute__((aligned(16))) char smem[];
  const unsigned lds0 = (unsigned)(uintptr_t)smem;
  const int lane = threadIdx.x & 63, wid = __builtin_amdgcn_readfirstlane(threadIdx.x >> 6);
  const int b = blockIdx.x;
  const int xcd = b & 7, sub = b >> 3;
  const int head = xcd * 4 + (sub & 3), qb = sub >> 2;
  const int r32 = lane & 31, hh = lane >> 5;
  const int qtile = qb * 8 + wid;

  const char* kv = ws + WS_KV + (size_t)head * KV_HEAD + lane * 16;
  if (wid < 2) glds16(kv + (128 + wid) * 1024, lds0 + B_LDS_KV + (128 + wid) * 1024);
  glds16(ws + WS_Q + ((size_t)head * 64 + qtile) * 1024 + lane * 16, lds0 + B_LDS_Q + wid * 1024);
#pragma unroll
  for (int i = 0; i < 16; ++i) glds16(kv + (i * 8 + wid) * 1024, lds0 + B_LDS_KV + (i * 8 + wid) * 1024);

  const unsigned kaddr = lds0 + B_LDS_KV + lane * 16;
  const unsigned vaddr = lds0 + B_LDS_KV + 1024 + (r32 < 16 ? (hh * 16 + r32) * 16 : 512);

  WAIT_VM(15);
  BARRIER();
  const bf16x8 q = lds_ld128(lds0 + B_LDS_Q + wid * 1024 + lane * 16);
  f32x16 negm;
  {
    const f32x16 z = {};
    const f32x16 s = MFMA32(lds_ld128(kaddr), q, z);
    const float m = max_halves(tile_max(s));
#pragma unroll
    for (int j = 0; j < 16; ++j) negm[j] = -m;
  }
  f32x16 o = {};
  attn_range(kaddr, vaddr, 0, 2, q, negm, o);
  WAIT_VM(12);
  BARRIER();
  attn_range(kaddr, vaddr, 2, 14, q, negm, o);
  WAIT_VM(0);
  BARRIER();
  attn_range(kaddr, vaddr, 14, 64, q, negm, o);

  {
    float amax = fabsf(o[0]);
#pragma unroll
    for (int j = 1; j < 8; ++j) amax = fmaxf(amax, fabsf(o[j]));
    const bool ok = (o[8] <= 16777216.f) && (amax <= 3.0e38f);
    if (__builtin_expect(!__all(ok), 0)) {
      const f32x16 z = {};
      float m = -INFINITY;
      for (int t = 0; t < 64; ++t) m = fmaxf(m, tile_max(MFMA32(lds_ld128(kaddr + t * KV_TILE), q, z)));
      m = max_halves(m);
#pragma unroll
      for (int j = 0; j < 16; ++j) { negm[j] = -m; o[j] = 0.f; }
      attn_range(kaddr, vaddr, 0, 64, q, negm, o);
    }
  }
  const float inv = 1.f / o[8];
  u32x4 po;
#pragma unroll
  for (int j = 0; j < 4; ++j) po[j] = pack_bf16(o[2 * j] * inv, o[2 * j + 1] * inv);
  *(u32x4*)(ws + WS_O + ((size_t)qtile * H + head) * 1024 + lane * 16) = po;
}

constexpr int C_LDS_BYTES = 131072;
__global__ __launch_bounds__(256, 1) void k_proj(const uint16_t* __restrict__ x, const uint16_t* __restrict__ wo, char* __restrict__ ws) {
  extern __shared__ __attribute__((aligned(16))) char smem[];
  const unsigned lds0 = (unsigned)(uintptr_t)smem;
  const int lane = threadIdx.x & 63, wid = __builtin_amdgcn_readfirstlane(threadIdx.x >> 6);
  const int b = blockIdx.x;
  const int cbk = b & 3, tt = b >> 2;
  const unsigned wdst = lds0 + wid * 32768;
  const char* wsrc = (const char*)wo + (size_t)(wid * 128 + (lane >> 2)) * 1024 + cbk * 256 + (lane & 3) * 16;
  bf16x8 of[8];
#pragma unroll
  for (int i = 0; i < 8; ++i) of[i] = *(const bf16x8*)(ws + WS_O + ((size_t)tt * H + wid * 8 + i) * 1024 + lane * 16);
  asm volatile("" ::: "memory");
#pragma unroll
  for (int i = 0; i < 8; ++i)
#pragma unroll
    for (int ct = 0; ct < 4; ++ct) glds16(wsrc + (size_t)i * 16 * 1024 + ct * 64, wdst + ct * 8192 + i * 1024);
  const int g = lane >> 4, i16 = lane & 15;
  const unsigned wrd = wdst + (4 * (g >> 1) + (i16 >> 2)) * 64 + (g & 1) * 32 + (i16 & 3) * 8;
  f32x16 acc[4] = {};
  WAIT_VM(0);
#pragma unroll
  for (int i = 0; i < 8; ++i)
#pragma unroll
    for (int ct = 0; ct < 4; ++ct) {
      const unsigned a = wrd + ct * 8192 + i * 1024;
      const bf16x8 wf = cat8(tr_read(a), tr_read(a + 512));
      acc[ct] = MFMA32(of[i], wf, acc[ct]);
    }
  WAIT_LGKM0();
  const int r32 = lane & 31, hh = lane >> 5;
#pragma unroll
  for (int ct = 0; ct < 4; ++ct)
#pragma unroll
    for (int j = 0; j < 16; ++j) {
      const int tok = (j & 3) + 8 * (j >> 2) + 4 * hh;
      lds_stf(wdst + tok * 528 + (ct * 32 + r32) * 4, acc[ct][j]);
    }
  BARRIER();
  const int row = threadIdx.x >> 3, cg = threadIdx.x & 7;
  float v[16];
#pragma unroll
  for (int q4 = 0; q4 < 4; ++q4) {
    f32x4 s = lds_ldf4(lds0 + row * 528 + (cg * 16 + q4 * 4) * 4);
#pragma unroll
    for (int w2 = 1; w2 < 4; ++w2) s += lds_ldf4(lds0 + w2 * 32768 + row * 528 + (cg * 16 + q4 * 4) * 4);
#pragma unroll
    for (int j = 0; j < 4; ++j) v[q4 * 4 + j] = s[j];
  }
  const size_t off = ((size_t)(tt * 32 + row) * D + cbk * 128 + cg * 16) * 2;
  const u32x4 x0 = *(const u32x4*)((const char*)x + off), x1 = *(const u32x4*)((const char*)x + off + 16);
  u32x4 h0, h1;
#pragma unroll
  for (int j = 0; j < 4; ++j) {
    h0[j] = pack_bf16(bf_lo(x0[j]) + rbf(v[2 * j]), bf_hi(x0[j]) + rbf(v[2 * j + 1]));
    h1[j] = pack_bf16(bf_lo(x1[j]) + rbf(v[8 + 2 * j]), bf_hi(x1[j]) + rbf(v[8 + 2 * j + 1]));
  }
  *(u32x4*)(ws + WS_H + off) = h0;
  *(u32x4*)(ws + WS_H + off + 16) = h1;
}

DI float wave_sum(float v) {
#pragma unroll
  for (int o = 32; o > 0; o >>= 1) v += __shfl_xor(v, o);
  return v;
}

__global__ __launch_bounds__(256) void k_ln2(const uint16_t* __restrict__ wr, char* __restrict__ ws, uint16_t* __restrict__ out) {
  const int lane = threadIdx.x & 63, wid = threadIdx.x >> 6;
  const int row = blockIdx.x * 4 + wid;
  const size_t off = ((size_t)row * D + lane * 8) * 2;
  const u32x4 hv = *(const u32x4*)(ws + WS_H + off);
  const u32x4 wv = *(const u32x4*)((const char*)wr + lane * 16);
  float h[8];
#pragma unroll
  for (int j = 0; j < 4; ++j) { h[2 * j] = bf_lo(hv[j]); h[2 * j + 1] = bf_hi(hv[j]); }
  float s = 0.f;
#pragma unroll
  for (int j = 0; j < 8; ++j) s += h[j];
  const float mean = wave_sum(s) * (1.f / D);
  float q = 0.f;
#pragma unroll
  for (int j = 0; j < 8; ++j) { const float d = h[j] - mean; q += d * d; }
  const float rstd = rsqrtf(wave_sum(q) * (1.f / D) + LN_EPS);
  u32x4 lv, ov; float rr = 0.f;
#pragma unroll
  for (int j = 0; j < 4; ++j) {
    lv[j] = pack_bf16((h[2 * j] - mean) * rstd, (h[2 * j + 1] - mean) * rstd);
    const float l0 = bf_lo(lv[j]), l1 = bf_hi(lv[j]);
    rr += l0 * bf_lo(wv[j]) + l1 * bf_hi(wv[j]);
    ov[j] = pack_bf16(h[2 * j] + l0, h[2 * j + 1] + l1);
  }
  rr = wave_sum(rr);
  *(u32x4*)(ws + WS_LN + off) = lv;
  *(u32x4*)((char*)out + off) = ov;
  if (lane == 0) ((float*)(ws + WS_R))[row] = rr;
}

DI unsigned sort_key(float r, int idx) {
  unsigned u = __float_as_uint(r);
  u = (u & 0x80000000u) ? ~u : (u | 0x80000000u);
  return (u & ~0x7ffu) | (unsigned)(2047 - idx);
}

__global__ __launch_bounds__(256) void k_ffn1(const uint16_t* __restrict__ wg, const uint16_t* __restrict__ wu, char* __restrict__ ws) {
  __shared__ unsigned s_key[4];
  __shared__ int s_idx[8];
  __shared__ float s_x[8][512];
  __shared__ float s_red[16][2][8][16];
  const int tid = threadIdx.x, lane = tid & 63, wid = tid >> 6;
  unsigned key[8];
#pragma unroll
  for (int j = 0; j < 8; ++j) key[j] = sort_key(((const float*)(ws + WS_R))[tid * 8 + j], tid * 8 + j);
  for (int c = 0; c < 8; ++c) {
    unsigned m = 0;
#pragma unroll
    for (int j = 0; j < 8; ++j) m = max(m, key[j]);
#pragma unroll
    for (int o = 32; o > 0; o >>= 1) m = max(m, (unsigned)__shfl_xor((int)m, o));
    if (lane == 0) s_key[wid] = m;
    __syncthreads();
    m = max(max(s_key[0], s_key[1]), max(s_key[2], s_key[3]));
#pragma unroll
    for (int j = 0; j < 8; ++j) if (key[j] == m) key[j] = 0;
    if (tid == 0) s_idx[c] = 2047 - (int)(m & 0x7ffu);
    __syncthreads();
  }
  if (blockIdx.x == 0 && tid < 8) ((int*)(ws + WS_IDX))[tid] = s_idx[tid];
  for (int i = tid; i < 8 * 512; i += 256) s_x[i >> 9][i & 511] = bf_lo(((const uint16_t*)(ws + WS_LN))[(size_t)s_idx[i >> 9] * D + (i & 511)]);
  __syncthreads();
  const int col = blockIdx.x * 16 + (tid & 15), kg = tid >> 4;
  float ag[8] = {}, au[8] = {};
  for (int k = kg * 32; k < kg * 32 + 32; ++k) {
    const float g = bf_lo(wg[(size_t)k * F + col]), u = bf_lo(wu[(size_t)k * F + col]);
#pragma unroll
    for (int t = 0; t < 8; ++t) { ag[t] += s_x[t][k] * g; au[t] += s_x[t][k] * u; }
  }
#pragma unroll
  for (int t = 0; t < 8; ++t) { s_red[kg][0][t][tid & 15] = ag[t]; s_red[kg][1][t][tid & 15] = au[t]; }
  __syncthreads();
  if (tid < 128) {
    const int t = tid >> 4, c = tid & 15;
    float g = 0.f, u = 0.f;
    for (int i = 0; i < 16; ++i) { g += s_red[i][0][t][c]; u += s_red[i][1][t][c]; }
    g = rbf(g); u = rbf(u);
    const float sg = rbf(g / (1.f + __expf(-g)));
    ((uint16_t*)(ws + WS_ACT))[t * F + blockIdx.x * 16 + c] = (uint16_t)(pack_bf16(sg * u, 0.f) & 0xffffu);
  }
}

__global__ __launch_bounds__(256) void k_ffn2(const uint16_t* __restrict__ wd, char* __restrict__ ws, uint16_t* __restrict__ out) {
  __shared__ float s_a[8][F];
  __shared__ float s_red[16][8][16];
  const int tid = threadIdx.x;
  for (int i = tid; i < 8 * F; i += 256) s_a[i / F][i % F] = bf_lo(((const uint16_t*)(ws + WS_ACT))[i]);
  __syncthreads();
  const int col = blockIdx.x * 16 + (tid & 15), kg = tid >> 4;
  float a[8] = {};
  for (int f = kg * 86; f < kg * 86 + 86; ++f) {
    const float w = bf_lo(wd[(size_t)f * D + col]);
#pragma unroll
    for (int t = 0; t < 8; ++t) a[t] += s_a[t][f] * w;
  }
#pragma unroll
  for (int t = 0; t < 8; ++t) s_red[kg][t][tid & 15] = a[t];
  __syncthreads();
  if (tid < 128) {
    const int t = tid >> 4, c = tid & 15;
    float y = 0.f;
    for (int i = 0; i < 16; ++i) y += s_red[i][t][c];
    const int row = ((const int*)(ws + WS_IDX))[t];
    const float r = rbf(((const float*)(ws + WS_R))[row]);
    const float sig = rbf(1.f / (1.f + __expf(-r)));
    y = rbf(rbf(y) * sig);
    const size_t e = (size_t)row * D + blockIdx.x * 16 + c;
    const float h = bf_lo(((const uint16_t*)(ws + WS_H))[e]), l = bf_lo(((const uint16_t*)(ws + WS_LN))[e]);
    out[e] = (uint16_t)(pack_bf16(h + rbf(l + y), 0.f) & 0xffffu);
  }
}

extern "C" void kernel_launch(void* const* d_in, const int* in_sizes, int n_in, void* d_out, int out_size, void* d_ws, size_t ws_size,
                              hipStream_t stream) {
  const uint16_t* x = (const uint16_t*)d_in[0];
  const uint16_t* wqkv = (const uint16_t*)d_in[1];
  const uint16_t* wo = (const uint16_t*)d_in[2];
  const uint16_t* wr = (const uint16_t*)d_in[4];
  const uint16_t* wg = (const uint16_t*)d_in[5];
  const uint16_t* wu = (const uint16_t*)d_in[6];
  const uint16_t* wd = (const uint16_t*)d_in[7];
  char* ws = (char*)d_ws;
  uint16_t* out = (uint16_t*)d_out;
  (void)hipFuncSetAttribute((const void*)k_qkv, hipFuncAttributeMaxDynamicSharedMemorySize, A_LDS_BYTES);
  (void)hipFuncSetAttribute((const void*)k_attn, hipFuncAttributeMaxDynamicSharedMemorySize, B_LDS_BYTES);
  (void)hipFuncSetAttribute((const void*)k_proj, hipFuncAttributeMaxDynamicSharedMemorySize, C_LDS_BYTES);
  k_qkv<<<256, 256, A_LDS_BYTES, stream>>>(x, wqkv, ws);
  k_attn<<<256, 512, B_LDS_BYTES, stream>>>(ws);
  k_proj<<<256, 256, C_LDS_BYTES, stream>>>(x, wo, ws);
  k_ln2<<<512, 256, 0, stream>>>(wr, ws, out);
  k_ffn1<<<86, 256, 0, stream>>>(wg, wu, ws);
  k_ffn2<<<32, 256, 0, stream>>>(wd, ws, out);
}
```

```cpp
#include <hip/hip_runtime.h>
#include <hip/hip_bf16.h>
#include <stdint.h>

typedef short bf16x8 __attribute__((ext_vector_type(8)));
typedef short bf16x4 __attribute__((ext_vector_type(4)));
typedef float f32x16 __attribute__((ext_vector_type(16)));
typedef float f32x4 __attribute__((ext_vector_type(4)));
typedef float f32x2 __attribute__((ext_vector_type(2)));
typedef __bf16 bf16x2_t __attribute__((ext_vector_type(2)));
typedef unsigned u32x4 __attribute__((ext_vector_type(4)));
typedef unsigned u32x2 __attribute__((ext_vector_type(2)));
#define LAS __attribute__((address_space(3)))
#define GAS __attribute__((address_space(1)))
#define DI __device__ __forceinline__
#define MFMA32(a, b, c) __builtin_amdgcn_mfma_f32_32x32x16_bf16((a), (b), (c), 0, 0, 0)
#define MFMA16(a, b, c) __builtin_amdgcn_mfma_f32_16x16x32_bf16((a), (b), (c), 0, 0, 0)
#define MFMA4(a, b, c) __builtin_amdgcn_mfma_f32_4x4x4bf16_1k((a), (b), (c), 0, 0, 0)
#define SBAR() __builtin_amdgcn_sched_barrier(0)
#define PIN(x) asm volatile("" : "+v"(x))
#define BC8(x) __builtin_bit_cast(bf16x8, x)

constexpr int S = 2048, D = 512, H = 32, F = 1376;
constexpr float LN_EPS = 1e-5f;
constexpr float QSCALE = 0.25f * 1.4426950408889634f;
constexpr unsigned SENT = 0xFFFFFFFFu;
constexpr unsigned SPIN_CAP = 1u << 16;

constexpr int VS = 544;
constexpr int KV_TILE = 1024 + 2 * VS;
constexpr int KV_HEAD = 64 * KV_TILE;
constexpr size_t WS_Q = 0;
constexpr size_t WS_KV = WS_Q + (size_t)H * 64 * 1024;
constexpr size_t WS_O = WS_KV + (size_t)H * KV_HEAD;
constexpr size_t WS_HO = WS_O + (size_t)64 * H * 1024;
constexpr int HO_H = 0;
constexpr int HO_LN = HO_H + S * D * 2;
constexpr int HO_ST = HO_LN + S * D * 2;
constexpr int HO_KEY = HO_ST + 64 * 4 * 32 * 16;
constexpr int HO_ACT = HO_KEY + S * 4;
constexpr int HO_END = HO_ACT + 24576;
constexpr int HO_BYTES = 256 * 17408;
static_assert(HO_BYTES >= HO_END, "reset covers the hand-off region");

#define STAMP_DECL do {} while (0)
#define STAMP(i) do {} while (0)
#define STAMP_OUT(k) do {} while (0)

DI unsigned pack_bf16(float lo, float hi) {
  f32x2 v = {lo, hi};
  bf16x2_t r = __builtin_convertvector(v, bf16x2_t);
  return __builtin_bit_cast(unsigned, r);
}
DI float bf_lo(unsigned v) { return __uint_as_float(v << 16); }
DI float bf_hi(unsigned v) { return __uint_as_float(v & 0xffff0000u); }
DI float rbf(float x) { return bf_lo(pack_bf16(x, x)); }
DI float sum_halves(float v) {
  auto r = __builtin_amdgcn_permlane32_swap(__float_as_uint(v), __float_as_uint(v), false, false);
  return __uint_as_float(r[0]) + __uint_as_float(r[1]);
}
DI float max_halves(float v) {
  auto r = __builtin_amdgcn_permlane32_swap(__float_as_uint(v), __float_as_uint(v), false, false);
  return fmaxf(__uint_as_float(r[0]), __uint_as_float(r[1]));
}
template <int CTRL> DI float dpp_f(float v) { return __uint_as_float(__builtin_amdgcn_update_dpp(0u, __float_as_uint(v), CTRL, 0xf, 0xf, true)); }
template <int CTRL> DI unsigned dpp_u(unsigned v) { return __builtin_amdgcn_update_dpp(0u, v, CTRL, 0xf, 0xf, true); }
constexpr int DPP_X1 = 0xB1, DPP_X2 = 0x4E, DPP_HMIR = 0x141, DPP_MIR = 0x140;
DI float quad_sum(float v) { v += dpp_f<DPP_X1>(v); v += dpp_f<DPP_X2>(v); return v; }
DI float row16_sum(float v) { v = quad_sum(v); v += dpp_f<DPP_HMIR>(v); v += dpp_f<DPP_MIR>(v); return v; }
DI unsigned wave_max_u(unsigned v) {
  v = max(v, dpp_u<DPP_X1>(v)); v = max(v, dpp_u<DPP_X2>(v)); v = max(v, dpp_u<DPP_HMIR>(v)); v = max(v, dpp_u<DPP_MIR>(v));
  v = max(v, (unsigned)__builtin_amdgcn_update_dpp(0u, v, 0x142, 0xa, 0xf, false));
  v = max(v, (unsigned)__builtin_amdgcn_update_dpp(0u, v, 0x143, 0xc, 0xf, false));
  return (unsigned)__builtin_amdgcn_readlane((int)v, 63);
}
DI void glds16(const void* sbase, unsigned voff, unsigned lds_base) {
  unsigned sv;
  asm volatile("s_mov_b32 %0, m0\n\ts_mov_b32 m0, %3\n\ts_nop 0\n\tglobal_load_lds_dwordx4 %1, %2\n\ts_mov_b32 m0, %0"
               : "=&s"(sv) : "v"(voff), "s"(sbase), "s"(lds_base) : "memory");
}
#define WAIT_VM(N) asm volatile("s_waitcnt vmcnt(" #N ")" ::: "memory")
#define WAIT_LGKM0() asm volatile("s_waitcnt lgkmcnt(0)" ::: "memory")
#define BARRIER() asm volatile("s_waitcnt lgkmcnt(0)\n\ts_barrier" ::: "memory")
typedef short v4i16_t __attribute__((ext_vector_type(4)));
DI bf16x4 tr_read(unsigned addr) {
  return __builtin_bit_cast(bf16x4, __builtin_amdgcn_ds_read_tr16_b64_v4i16((LAS v4i16_t*)(uintptr_t)addr));
}
DI bf16x8 lds_ld128(unsigned addr) { return *(const LAS bf16x8*)(uintptr_t)addr; }
DI f32x4 lds_ldf4(unsigned addr) { return *(const LAS f32x4*)(uintptr_t)addr; }
DI u32x4 lds_ldu4(unsigned addr) { return *(const LAS u32x4*)(uintptr_t)addr; }
DI void lds_stf4(unsigned addr, f32x4 v) { *(LAS f32x4*)(uintptr_t)addr = v; }
DI void lds_stu4(unsigned addr, u32x4 v) { *(LAS u32x4*)(uintptr_t)addr = v; }
DI void lds_stf(unsigned addr, float v) { *(LAS float*)(uintptr_t)addr = v; }
DI void lds_stu(unsigned addr, unsigned v) { *(LAS unsigned*)(uintptr_t)addr = v; }
DI float lds_ldf(unsigned addr) { return *(const LAS float*)(uintptr_t)addr; }
DI unsigned lds_ldu(unsigned addr) { return *(const LAS unsigned*)(uintptr_t)addr; }
DI bf16x8 cat8(bf16x4 a, bf16x4 b) { return __builtin_shufflevector(a, b, 0, 1, 2, 3, 4, 5, 6, 7); }
DI bf16x4 lo4(bf16x8 a) { return __builtin_shufflevector(a, a, 0, 1, 2, 3); }
DI bf16x4 hi4(bf16x8 a) { return __builtin_shufflevector(a, a, 4, 5, 6, 7); }
typedef __amdgpu_buffer_rsrc_t rsrc_t;
DI u32x4 ho_ld(rsrc_t rs, int off) { return __builtin_amdgcn_raw_buffer_load_b128(rs, off, 0, 16); }
DI void ho_st(rsrc_t rs, int off, u32x4 v) { __builtin_amdgcn_raw_buffer_store_b128(v, rs, off, 0, 16); }
DI void ho_st1(rsrc_t rs, int off, unsigned v) { __builtin_amdgcn_raw_buffer_store_b32(v, rs, off, 0, 16); }
DI unsigned san(unsigned v) { return min(v, SENT - 1u); }
DI bool pend(u32x4 v) { return max(max(v[0], v[1]), max(v[2], v[3])) == SENT; }

constexpr int A_LDS_BYTES = 131072;
struct AFrag { bf16x8 x, q, k, v; };

__global__ __launch_bounds__(512, 1) void k_qkv(const uint16_t* __restrict__ x, const uint16_t* __restrict__ w, char* __restrict__ ws) {
  extern __shared__ __attribute__((aligned(16))) char smem[];
  const unsigned lds0 = (unsigned)(uintptr_t)smem;
  const int lane = threadIdx.x & 63, wid = __builtin_amdgcn_readfirstlane(threadIdx.x >> 6);
  const int b = blockIdx.x, xcd = b & 7, sub = b >> 3;
  const int rb = (xcd >> 1) * 8 + (sub & 7), hq = (xcd & 1) * 4 + (sub >> 3);
  const int rot = ((sub & 7) + 2 * (sub >> 3)) & 7;
  const int rt = wid & 1, cg = (wid >> 1) & 1, kh = wid >> 2, par = rt ^ cg;
  const int r32 = lane & 31, hh = lane >> 5;
  STAMP_DECL; STAMP(0);

  const unsigned wvo = (8 * wid + (lane >> 3)) * 3072 + hq * 128 + (((lane & 7) ^ (((lane >> 4) & 1) << 2)) * 16);
  const unsigned xvo = (rb * 64 + 8 * wid + (lane >> 3)) * 1024 + (((lane & 7) ^ ((4 * wid + (lane >> 4)) & 7)) * 16);
  auto issue = [&](int c) {
    const int kc = (c + rot) & 7;
    const unsigned dst = lds0 + (c & 3) * 32768 + wid * 1024;
    const char* wb = (const char*)w + (size_t)kc * (64 * 3072);
    glds16((const char*)x + kc * 128, xvo, dst + 24576);
    glds16(wb, wvo, dst);
    glds16(wb + 1024, wvo, dst + 8192);
    glds16(wb + 2048, wvo, dst + 16384);
  };
  issue(0); issue(1); issue(2); issue(3);

  const int g = lane >> 4, i16 = lane & 15;
  const int stA = 2 * kh + par, stB = 2 * kh + (par ^ 1);
  const unsigned xrow = lds0 + 24576 + (rt * 4 + (r32 >> 3)) * 1024 + (r32 & 7) * 128;
  const int xf = (r32 >> 1) & 7;
  const unsigned xA = xrow + (((2 * stA + hh) ^ xf) * 16), xB = xrow + (((2 * stB + hh) ^ xf) * 16);
  const unsigned wl = lds0 + (8 * (g >> 1) + (i16 >> 2)) * 128 + ((cg ^ ((i16 >> 3) & 1)) * 64) + (g & 1) * 32 + (i16 & 3) * 8;
  const unsigned wA = wl + stA * 2048, wB = wl + stB * 2048;
  auto rd = [&](unsigned xa, unsigned wa, int c) {
    const unsigned so = (c & 3) * 32768;
    AFrag f;
    f.x = lds_ld128(xa + so);
    f.q = cat8(tr_read(wa + so), tr_read(wa + so + 512));
    f.k = cat8(tr_read(wa + so + 8192), tr_read(wa + so + 8192 + 512));
    f.v = cat8(tr_read(wa + so + 16384), tr_read(wa + so + 16384 + 512));
    return f;
  };

  f32x16 accq = {}, acck = {}, accv = {};
  f32x4 rsum = {}, rsq = {}, cs0 = {}, cs1 = {}, cs2 = {};
  const bf16x4 ones4 = {0x3f80, 0x3f80, 0x3f80, 0x3f80};
  STAMP(1);
  WAIT_VM(12);
  BARRIER();
  STAMP(2);
  AFrag fa = rd(xA, wA, 0), fb = rd(xB, wB, 0);
#pragma unroll
  for (int c = 0; c < 8; ++c) {
    if (c <= 4) WAIT_VM(8); else if (c == 5) WAIT_VM(4); else if (c == 6) WAIT_VM(0);
    BARRIER();
    SBAR();
    if (c + 4 < 8) issue(c + 4);
    AFrag na, nb;
    if (c + 1 < 8) { na = rd(xA, wA, c + 1); nb = rd(xB, wB, c + 1); }
    accq = MFMA32(fa.q, fa.x, accq);
    acck = MFMA32(fa.k, fa.x, acck);
    accv = MFMA32(fa.x, fa.v, accv);
    rsum = MFMA4(ones4, lo4(fa.x), rsum); rsq = MFMA4(lo4(fa.x), lo4(fa.x), rsq);
    cs0 = MFMA4(ones4, lo4(fa.q), cs0); cs1 = MFMA4(ones4, lo4(fa.k), cs1); cs2 = MFMA4(ones4, lo4(fa.v), cs2);
    rsum = MFMA4(ones4, hi4(fa.x), rsum); rsq = MFMA4(hi4(fa.x), hi4(fa.x), rsq);
    cs0 = MFMA4(ones4, hi4(fa.q), cs0); cs1 = MFMA4(ones4, hi4(fa.k), cs1); cs2 = MFMA4(ones4, hi4(fa.v), cs2);
    accq = MFMA32(fb.q, fb.x, accq);
    acck = MFMA32(fb.k, fb.x, acck);
    accv = MFMA32(fb.x, fb.v, accv);
    SBAR();
    if (c + 1 < 8) { fa = na; fb = nb; }
  }
  STAMP(3);

  constexpr unsigned X_OFF = 0, RS_OFF = 49152, CS_OFF = RS_OFF + 2048, RF_OFF = CS_OFF + 3072, CF_OFF = RF_OFF + 512;
  {
    const unsigned xa = lds0 + X_OFF + wid * 6144 + lane * 16;
    if (kh == 0) {
      lds_stf4(xa, f32x4{accq[8], accq[9], accq[10], accq[11]}); lds_stf4(xa + 1024, f32x4{accq[12], accq[13], accq[14], accq[15]});
      lds_stf4(xa + 2048, f32x4{acck[8], acck[9], acck[10], acck[11]}); lds_stf4(xa + 3072, f32x4{acck[12], acck[13], acck[14], acck[15]});
      lds_stf4(xa + 4096, f32x4{accv[8], accv[9], accv[10], accv[11]}); lds_stf4(xa + 5120, f32x4{accv[12], accv[13], accv[14], accv[15]});
    } else {
      lds_stf4(xa, f32x4{accq[0], accq[1], accq[2], accq[3]}); lds_stf4(xa + 1024, f32x4{accq[4], accq[5], accq[6], accq[7]});
      lds_stf4(xa + 2048, f32x4{acck[0], acck[1], acck[2], acck[3]}); lds_stf4(xa + 3072, f32x4{acck[4], acck[5], acck[6], acck[7]});
      lds_stf4(xa + 4096, f32x4{accv[0], accv[1], accv[2], accv[3]}); lds_stf4(xa + 5120, f32x4{accv[4], accv[5], accv[6], accv[7]});
#pragma unroll
      for (int j = 0; j < 8; ++j) { accq[j] = accq[8 + j]; acck[j] = acck[8 + j]; accv[j] = accv[8 + j]; }
    }
    const int l3 = lane & 3;
    const float dq = l3 == 0 ? rsq[0] : l3 == 1 ? rsq[1] : l3 == 2 ? rsq[2] : rsq[3];
    const float s1 = sum_halves(rsum[0]), s2 = sum_halves(dq);
    const float c0 = sum_halves(cs0[0]), c1 = sum_halves(cs1[0]), c2 = sum_halves(cs2[0]);
    if (lane < 32) {
      const unsigned ra = lds0 + RS_OFF + ((rt * 4 + kh * 2 + cg) * 32 + r32) * 8;
      lds_stf(ra, s1); lds_stf(ra + 4, s2);
      const unsigned ca = lds0 + CS_OFF + ((cg * 4 + kh * 2 + rt) * 96 + r32) * 4;
      lds_stf(ca, c0); lds_stf(ca + 128, c1); lds_stf(ca + 256, c2);
    }
  }
  BARRIER();
  {
    const unsigned xa = lds0 + X_OFF + (wid ^ 4) * 6144 + lane * 16;
    const f32x4 q0 = lds_ldf4(xa), q1 = lds_ldf4(xa + 1024), k0 = lds_ldf4(xa + 2048), k1 = lds_ldf4(xa + 3072), v0 = lds_ldf4(xa + 4096), v1 = lds_ldf4(xa + 5120);
    const int t = threadIdx.x;
    if (t < 192) {
      const int c2 = t / 96, n = t - c2 * 96;
      const unsigned ca = lds0 + CS_OFF + (c2 * 4 * 96 + n) * 4;
      lds_stf(lds0 + CF_OFF + t * 4, (lds_ldf(ca) + lds_ldf(ca + 384)) + (lds_ldf(ca + 768) + lds_ldf(ca + 1152)));
    } else if (t >= 256 && t < 320) {
      const int row = t - 256;
      const unsigned ra = lds0 + RS_OFF + ((row >> 5) * 4 * 32 + (row & 31)) * 8;
      const float s1 = (lds_ldf(ra) + lds_ldf(ra + 256)) + (lds_ldf(ra + 512) + lds_ldf(ra + 768));
      const float s2 = (lds_ldf(ra + 4) + lds_ldf(ra + 260)) + (lds_ldf(ra + 516) + lds_ldf(ra + 772));
      const float mean = s1 * (1.f / D);
      const float var = fmaxf(s2 * (1.f / D) - mean * mean, 0.f);
      lds_stf(lds0 + RF_OFF + row * 4, mean);
      lds_stf(lds0 + RF_OFF + 256 + row * 4, rsqrtf(var + LN_EPS));
    }
#pragma unroll
    for (int j = 0; j < 4; ++j) {
      accq[j] += q0[j]; accq[4 + j] += q1[j]; acck[j] += k0[j]; acck[4 + j] += k1[j]; accv[j] += v0[j]; accv[4 + j] += v1[j];
    }
  }
  BARRIER();
  STAMP(4);
  const int tile = rb * 2 + rt, head0 = hq * 4 + cg * 2;
  {
    const float mean = lds_ldf(lds0 + RF_OFF + (rt * 32 + r32) * 4), rstd = lds_ldf(lds0 + RF_OFF + 256 + (rt * 32 + r32) * 4);
    const float sq = rstd * QSCALE;
    u32x4 pq, pk;
#pragma unroll
    for (int gq = 0; gq < 2; ++gq) {
      const unsigned ca = lds0 + CF_OFF + (cg * 96 + 16 * kh + 8 * gq + 4 * hh) * 4;
      const f32x4 cq = lds_ldf4(ca), ck = lds_ldf4(ca + 128);
      float vq[4], vk[4];
#pragma unroll
      for (int j = 0; j < 4; ++j) { vq[j] = (accq[4 * gq + j] - mean * cq[j]) * sq; vk[j] = (acck[4 * gq + j] - mean * ck[j]) * rstd; }
      pq[2 * gq] = pack_bf16(vq[0], vq[1]); pq[2 * gq + 1] = pack_bf16(vq[2], vq[3]);
      pk[2 * gq] = pack_bf16(vk[0], vk[1]); pk[2 * gq + 1] = pack_bf16(vk[2], vk[3]);
    }
    *(u32x4*)(ws + WS_KV + (size_t)(head0 + kh) * KV_HEAD + tile * KV_TILE + lane * 16) = pk;
    *(u32x4*)(ws + WS_Q + ((size_t)(head0 + kh) * 64 + tile) * 1024 + lane * 16) = pq;
  }
  {
    const float cv = lds_ldf(lds0 + CF_OFF + (cg * 96 + 64 + r32) * 4);
    u32x4 pv;
#pragma unroll
    for (int gq = 0; gq < 2; ++gq) {
      const unsigned ra = lds0 + RF_OFF + (rt * 32 + 16 * kh + 8 * gq + 4 * hh) * 4;
      const f32x4 mu = lds_ldf4(ra), rs = lds_ldf4(ra + 256);
      float vv[4];
#pragma unroll
      for (int j = 0; j < 4; ++j) vv[j] = (accv[4 * gq + j] - mu[j] * cv) * rs[j];
      pv[2 * gq] = pack_bf16(vv[0], vv[1]); pv[2 * gq + 1] = pack_bf16(vv[2], vv[3]);
    }
    char* vb = ws + WS_KV + (size_t)head0 * KV_HEAD + tile * KV_TILE + 1024 + kh * VS;
    *(u32x4*)(vb + (size_t)(r32 >> 4) * KV_HEAD + (hh * 16 + (r32 & 15)) * 16) = pv;
    if (lane < 4) {
      const unsigned o = (lane & 1) ? 0u : 0x3f803f80u;
      *(u32x4*)(vb + (size_t)(lane >> 1) * KV_HEAD + 512 + (lane & 1) * 16) = u32x4{o, o, o, o};
    }
  }
  STAMP(11); STAMP_OUT(0);
}

constexpr int B_LDS_Q = KV_HEAD, B_LDS_BYTES = KV_HEAD + 24576;
#define EX(x) __builtin_amdgcn_exp2f(x)
#define PK(a, b) pack_bf16(a, b)
#define UNIT(SC, SN, KN, QN, NEGN, OPREV, VPREV, OCUR, VCUR0, LOADS) do { \
    float e0 = EX(SC[0]), e1 = EX(SC[1]); SBAR(); \
    OPREV = MFMA32(VPREV, BC8(p1), OPREV); \
    LOADS; \
    SBAR(); \
    float e2 = EX(SC[2]), e3 = EX(SC[3]); SBAR(); \
    p0[0] = PK(e0, e1); SBAR(); float e4 = EX(SC[4]), e5 = EX(SC[5]); SBAR(); \
    p0[1] = PK(e2, e3); SBAR(); float e6 = EX(SC[6]), e7 = EX(SC[7]); SBAR(); \
    SN = MFMA32(KN, QN, NEGN); SBAR(); \
    p0[2] = PK(e4, e5); SBAR(); e0 = EX(SC[8]); e1 = EX(SC[9]); SBAR(); \
    p0[3] = PK(e6, e7); SBAR(); e2 = EX(SC[10]); e3 = EX(SC[11]); SBAR(); \
    OCUR = MFMA32(VCUR0, BC8(p0), OCUR); SBAR(); \
    p1[0] = PK(e0, e1); SBAR(); e4 = EX(SC[12]); e5 = EX(SC[13]); SBAR(); \
    p1[1] = PK(e2, e3); SBAR(); e6 = EX(SC[14]); e7 = EX(SC[15]); SBAR(); \
    p1[2] = PK(e4, e5); SBAR(); \
    p1[3] = PK(e6, e7); SBAR(); \
  } while (0)

DI float tile_max(const f32x16& s) {
  float m = fmaxf(fmaxf(s[0], s[1]), s[2]);
#pragma unroll
  for (int j = 3; j < 15; j += 2) m = fmaxf(fmaxf(m, s[j]), s[j + 1]);
  return fmaxf(m, s[15]);
}

__global__ __launch_bounds__(512, 1) void k_attn(char* __restrict__ ws, const char* __restrict__ wo, const char* __restrict__ wg,
                                                  const char* __restrict__ wu, const char* __restrict__ wd) {
  extern __shared__ __attribute__((aligned(16))) char smem[];
  const unsigned lds0 = (unsigned)(uintptr_t)smem;
  const int lane = threadIdx.x & 63, wid = __builtin_amdgcn_readfirstlane(threadIdx.x >> 6);
  const int b = blockIdx.x, xcd = b & 7, sub = b >> 3;
  const int head = xcd * 4 + (sub & 3), qb = sub >> 2;
  const int r32 = lane & 31, hh = lane >> 5;
  const int qg = wid & 3, kh = wid >> 2;
  STAMP_DECL; STAMP(0);

  {
    const char* kv = ws + WS_KV + (size_t)head * KV_HEAD;
    const unsigned po = (wid & 1) * (KV_HEAD / 2) + (wid >> 1) * 1024;
    if (wid < 4) glds16(kv + po + 16 * 4096, lane * 16, lds0 + po + 16 * 4096);
    glds16(ws + WS_Q + ((size_t)head * 64 + qb * 8 + wid) * 1024, lane * 16, lds0 + B_LDS_Q + wid * 1024);
#pragma unroll
    for (int j = 0; j < 16; ++j) glds16(kv + po + j * 4096, lane * 16, lds0 + po + j * 4096);
  }
  unsigned kaddr = lds0 + kh * (KV_HEAD / 2) + lane * 16;
  unsigned vaddr = lds0 + kh * (KV_HEAD / 2) + 1024 + (r32 < 16 ? (hh * 16 + r32) * 16 : (r32 == 16 ? 512 : 528));
  STAMP(1);
  WAIT_VM(15);
  BARRIER();
  STAMP(2);
  const bf16x8 qa = lds_ld128(lds0 + B_LDS_Q + qg * 2048 + lane * 16), qb2 = lds_ld128(lds0 + B_LDS_Q + qg * 2048 + 1024 + lane * 16);
  float ma, mb;
  {
    const f32x16 z = {};
    const bf16x8 k0 = lds_ld128(kaddr);
    ma = max_halves(tile_max(MFMA32(k0, qa, z)));
    mb = max_halves(tile_max(MFMA32(k0, qb2, z)));
  }
  f32x16 oa, ob;
  for (int attempt = 0;; ++attempt) {
    f32x16 na, nb;
#pragma unroll
    for (int j = 0; j < 16; ++j) { na[j] = -ma; nb[j] = -mb; oa[j] = 0.f; ob[j] = 0.f; }
    PIN(na); PIN(nb);
    unsigned ka = kaddr, va = vaddr;
    bf16x8 k0 = lds_ld128(ka), k1, v00 = lds_ld128(va), v01 = lds_ld128(va + VS), v10, v11 = v00;
    f32x16 sA = MFMA32(k0, qa, na), sB;
    u32x4 p0, p1 = {0, 0, 0, 0};
    asm volatile("s_nop 7\n\ts_nop 7" : "+v"(sA));
    for (int it = 0; it < 8; ++it) {
      if (attempt == 0 && it < 3) {
        if (it == 0) WAIT_VM(13); else if (it == 1) WAIT_VM(11); else WAIT_VM(0);
        asm volatile("s_barrier" ::: "memory");
      }
#pragma unroll
      for (int u = 0; u < 4; u += 2) {
        UNIT(sA, sB, k0, qb2, nb, ob, v11, oa, v00, k1 = lds_ld128(ka + (u + 1) * KV_TILE); v10 = lds_ld128(va + (u + 1) * KV_TILE));
        UNIT(sB, sA, k1, qa, na, oa, v01, ob, v00, v11 = lds_ld128(va + (u + 1) * KV_TILE + VS));
        UNIT(sA, sB, k1, qb2, nb, ob, v01, oa, v10, k0 = lds_ld128(ka + (u + 2) * KV_TILE); v00 = lds_ld128(va + (u + 2) * KV_TILE));
        UNIT(sB, sA, k0, qa, na, oa, v11, ob, v10, v01 = lds_ld128(va + (u + 2) * KV_TILE + VS));
      }
      ka += 4 * KV_TILE; va += 4 * KV_TILE;
      if (attempt == 0 && it == 3) {
        const rsrc_t rs = __builtin_amdgcn_make_buffer_rsrc(ws + WS_HO, 0, HO_BYTES, 0x00020000);
        const u32x4 sv = {SENT, SENT, SENT, SENT};
        const int o = b * 17408 + threadIdx.x * 16;
        ho_st(rs, o, sv); ho_st(rs, o + 8192, sv);
        if (wid == 0) ho_st(rs, o + 16384, sv);
      }
    }
    ob = MFMA32(v11, BC8(p1), ob);
    float ta = oa[8], tb = ob[8];
    bool ok = (ta <= 16777216.f) && (tb <= 16777216.f);
#pragma unroll
    for (int j = 0; j < 8; ++j) { ta += fabsf(oa[j]); tb += fabsf(ob[j]); }
    ok = ok && (ta + tb <= 3.0e38f);
    if (__builtin_expect(__all(ok) || attempt == 1, 1)) break;
    const f32x16 z = {};
    ma = -INFINITY; mb = -INFINITY;
    for (int t = 0; t < 32; ++t) {
      const bf16x8 kf = lds_ld128(kaddr + t * KV_TILE);
      ma = fmaxf(ma, tile_max(MFMA32(kf, qa, z)));
      mb = fmaxf(mb, tile_max(MFMA32(kf, qb2, z)));
    }
    ma = max_halves(ma); mb = max_halves(mb);
  }
  STAMP(6);
  {
    const unsigned xa = lds0 + B_LDS_Q + wid * 3072 + lane * 16;
    if (kh == 0) {
      lds_stf4(xa, f32x4{ob[0], ob[1], ob[2], ob[3]}); lds_stf4(xa + 1024, f32x4{ob[4], ob[5], ob[6], ob[7]});
      lds_stf4(xa + 2048, f32x4{sum_halves(ob[8]), mb, 0.f, 0.f});
    } else {
      lds_stf4(xa, f32x4{oa[0], oa[1], oa[2], oa[3]}); lds_stf4(xa + 1024, f32x4{oa[4], oa[5], oa[6], oa[7]});
      lds_stf4(xa + 2048, f32x4{sum_halves(oa[8]), ma, 0.f, 0.f});
#pragma unroll
      for (int j = 0; j < 9; ++j) oa[j] = ob[j];
      ma = mb;
    }
    const float l = sum_halves(oa[8]);
    BARRIER();
    const unsigned ya = lds0 + B_LDS_Q + (wid ^ 4) * 3072 + lane * 16;
    const f32x4 y0 = lds_ldf4(ya), y1 = lds_ldf4(ya + 1024), y2 = lds_ldf4(ya + 2048);
    const float M = fmaxf(ma, y2[1]);
    float f1 = EX(ma - M), f2 = EX(y2[1] - M);
    const float inv = 1.f / (l * f1 + y2[0] * f2);
    f1 *= inv; f2 *= inv;
    u32x4 po;
    po[0] = pack_bf16(oa[0] * f1 + y0[0] * f2, oa[1] * f1 + y0[1] * f2);
    po[1] = pack_bf16(oa[2] * f1 + y0[2] * f2, oa[3] * f1 + y0[3] * f2);
    po[2] = pack_bf16(oa[4] * f1 + y1[0] * f2, oa[5] * f1 + y1[1] * f2);
    po[3] = pack_bf16(oa[6] * f1 + y1[2] * f2, oa[7] * f1 + y1[3] * f2);
    *(u32x4*)(ws + WS_O + ((size_t)(qb * 8 + qg * 2 + kh) * H + head) * 1024 + lane * 16) = po;
  }
  STAMP(11); STAMP_OUT(1);
}

constexpr int C_LDS_O = 131072, C_LDS_BYTES = 163840;
constexpr int N_D1 = 86, N_D2 = 32;

DI unsigned sort_key(float r, int idx) {
  unsigned u = pack_bf16(r, r) & 0xffffu;
  if ((u & 0x7fffu) > 0x7f80u) u = 0xffffu;
  u = (u & 0x8000u) ? (~u & 0xffffu) : (u | 0x8000u);
  return (u << 16) | (unsigned)(2047 - idx);
}
DI float key_score(unsigned key) {
  unsigned u = key >> 16;
  u = (u & 0x8000u) ? (u & 0x7fffu) : (~u & 0xffffu);
  return __uint_as_float(u << 16);
}

__global__ __launch_bounds__(512, 1) void k_tail(const uint16_t* __restrict__ x, const uint16_t* __restrict__ wo, const uint16_t* __restrict__ wr,
                                                  const uint16_t* __restrict__ wg, const uint16_t* __restrict__ wu, const uint16_t* __restrict__ wd,
                                                  char* __restrict__ ws, uint16_t* __restrict__ out) {
  extern __shared__ __attribute__((aligned(16))) char smem[];
  const unsigned lds0 = (unsigned)(uintptr_t)smem;
  const int tid = threadIdx.x, lane = tid & 63, wid = __builtin_amdgcn_readfirstlane(tid >> 6);
  const int b = blockIdx.x, xcd = b & 7, sub = b >> 3;
  const int tt = xcd * 8 + (sub >> 2), cbk = sub & 3;
  const int ct = wid & 3, kh = wid >> 2;
  const int r32 = lane & 31, hh = lane >> 5;
  const rsrc_t rs = __builtin_amdgcn_make_buffer_rsrc(ws + WS_HO, 0, HO_BYTES, 0x00020000);
  STAMP_DECL; STAMP(0);

  const int row = tid >> 4, c8 = tid & 15;
  const int goff = ((tt * 32 + row) * D + cbk * 128 + c8 * 8) * 2;
  {
    const char* ob = ws + WS_O + (size_t)tt * H * 1024;
#pragma unroll
    for (int j = 0; j < 4; ++j) glds16(ob + (j * 8 + wid) * 1024, lane * 16, lds0 + C_LDS_O + (j * 8 + wid) * 1024);
    const unsigned wvo = (lane >> 4) * 1024 + cbk * 256 + (((lane & 15) ^ ((lane >> 4) << 2)) * 16);
#pragma unroll
    for (int st = 0; st < 2; ++st)
#pragma unroll
      for (int j = 0; j < 8; ++j) {
        const int piece = (j >> 2) * 64 + st * 32 + (j & 3) * 8 + wid;
        glds16((const char*)wo + (size_t)piece * 4096, wvo, lds0 + piece * 1024);
      }
  }
  u32x4 xv, wrv, wra, fw[6];
  {
    const char* f0; const char* f1; unsigned vo, st;
    if (b < N_D1) { f0 = (const char*)wg; f1 = (const char*)wu; vo = (wid * 64 + (lane >> 1)) * (F * 2) + b * 32 + (lane & 1) * 16; st = 32 * F * 2; }
    else { f0 = (const char*)wd; f1 = f0; vo = (wid * 32 + (lane >> 1)) * (D * 2) + ((b - N_D1) & 31) * 32 + (lane & 1) * 16; st = 256 * D * 2; }
    const unsigned o1 = vo + st, o2 = (b < N_D1) ? vo : vo + 2 * st, o3 = o2 + st;
    const bool isd = b < N_D1 + N_D2;
    const unsigned z = lane * 16;
    const unsigned o4 = (b >= N_D1 && isd) ? vo + 4 * st : z, o5 = (b >= N_D1 && isd && wid < 3) ? vo + 5 * st : z;
    asm volatile("global_load_dwordx4 %0, %9, %10\n\tglobal_load_dwordx4 %1, %11, %12\n\tglobal_load_dwordx4 %2, %13, %12\n\t"
                 "global_load_dwordx4 %3, %14, %20\n\tglobal_load_dwordx4 %4, %15, %20\n\tglobal_load_dwordx4 %5, %16, %21\n\t"
                 "global_load_dwordx4 %6, %17, %21\n\tglobal_load_dwordx4 %7, %18, %21\n\tglobal_load_dwordx4 %8, %19, %21"
                 : "=&v"(xv), "=&v"(wrv), "=&v"(wra), "=&v"(fw[0]), "=&v"(fw[1]), "=&v"(fw[2]), "=&v"(fw[3]), "=&v"(fw[4]), "=&v"(fw[5])
                 : "v"(goff), "s"(x), "v"((cbk * 128 + c8 * 8) * 2), "s"(wr), "v"(z),
                   "v"(isd ? vo : z), "v"(isd ? o1 : z), "v"(isd ? o2 : z), "v"(isd ? o3 : z), "v"(o4), "v"(o5), "s"(f0), "s"(f1)
                 : "memory");
  }

  const int g = lane >> 4, i16 = lane & 15;
  const unsigned wl = lds0 + kh * 65536 + (4 * (g >> 1) + (i16 >> 2)) * 256 + ((ct ^ (i16 >> 2)) * 64) + (g & 1) * 32 + (i16 & 3) * 8;
  const unsigned ol = lds0 + C_LDS_O + kh * 16384 + lane * 16;
  f32x16 acc = {};
  STAMP(1);
  WAIT_VM(17);
  BARRIER();
  STAMP(2);
#pragma unroll
  for (int i = 0; i < 8; ++i) acc = MFMA32(cat8(tr_read(wl + i * 4096), tr_read(wl + i * 4096 + 2048)), lds_ld128(ol + i * 1024), acc);
  WAIT_VM(9);
  BARRIER();
#pragma unroll
  for (int i = 8; i < 16; ++i) acc = MFMA32(cat8(tr_read(wl + i * 4096), tr_read(wl + i * 4096 + 2048)), lds_ld128(ol + i * 1024), acc);
  STAMP(3);
  {
    const unsigned pa = lds0 + kh * 65536 + r32 * 528 + (ct * 32 + 4 * hh) * 4;
#pragma unroll
    for (int gq = 0; gq < 4; ++gq) lds_stf4(pa + gq * 32, f32x4{acc[4 * gq], acc[4 * gq + 1], acc[4 * gq + 2], acc[4 * gq + 3]});
  }
  asm volatile("s_waitcnt vmcnt(6)" : "+v"(xv), "+v"(wrv), "+v"(wra) :: "memory");
  float swr = 0.f;
#pragma unroll
  for (int j = 0; j < 4; ++j) swr += bf_lo(wra[j]) + bf_hi(wra[j]);
  swr = row16_sum(swr);
  swr += __shfl_xor(swr, 16); swr = sum_halves(swr);
  BARRIER();
  constexpr unsigned F_OFF = 98304;
  float h[8];
  u32x4 hv;
  {
    const unsigned pa = lds0 + row * 528 + c8 * 32;
    const f32x4 a0 = lds_ldf4(pa), a1 = lds_ldf4(pa + 16), b0 = lds_ldf4(pa + 65536), b1 = lds_ldf4(pa + 65536 + 16);
    const unsigned t0 = pack_bf16(a0[0] + b0[0], a0[1] + b0[1]), t1 = pack_bf16(a0[2] + b0[2], a0[3] + b0[3]);
    const unsigned t2 = pack_bf16(a1[0] + b1[0], a1[1] + b1[1]), t3 = pack_bf16(a1[2] + b1[2], a1[3] + b1[3]);
    hv[0] = pack_bf16(bf_lo(xv[0]) + bf_lo(t0), bf_hi(xv[0]) + bf_hi(t0)); hv[1] = pack_bf16(bf_lo(xv[1]) + bf_lo(t1), bf_hi(xv[1]) + bf_hi(t1));
    hv[2] = pack_bf16(bf_lo(xv[2]) + bf_lo(t2), bf_hi(xv[2]) + bf_hi(t2)); hv[3] = pack_bf16(bf_lo(xv[3]) + bf_lo(t3), bf_hi(xv[3]) + bf_hi(t3));
#pragma unroll
    for (int j = 0; j < 4; ++j) { h[2 * j] = bf_lo(hv[j]); h[2 * j + 1] = bf_hi(hv[j]); }
  }
  float s1 = 0.f, s2 = 0.f, s3 = 0.f;
#pragma unroll
  for (int j = 0; j < 4; ++j) {
    s1 += h[2 * j] + h[2 * j + 1];
    s2 = fmaf(h[2 * j], h[2 * j], s2); s2 = fmaf(h[2 * j + 1], h[2 * j + 1], s2);
    s3 = fmaf(h[2 * j], bf_lo(wrv[j]), s3); s3 = fmaf(h[2 * j + 1], bf_hi(wrv[j]), s3);
  }
  s1 = row16_sum(s1); s2 = row16_sum(s2); s3 = row16_sum(s3);
  if (c8 == 0) ho_st(rs, HO_ST + ((tt * 4 + cbk) * 32 + row) * 16, u32x4{san(__float_as_uint(s1)), san(__float_as_uint(s2)), san(__float_as_uint(s3)), 0u});
  ho_st(rs, HO_H + goff, u32x4{san(hv[0]), san(hv[1]), san(hv[2]), san(hv[3])});
  STAMP(4);
  asm volatile("s_waitcnt vmcnt(2)" : "+v"(fw[0]), "+v"(fw[1]), "+v"(fw[2]), "+v"(fw[3]), "+v"(fw[4]), "+v"(fw[5]) :: "memory");
  if (b < N_D1) {
    const unsigned fa = lds0 + F_OFF + wid * 2048 + lane * 16;
    lds_stu4(fa, fw[0]); lds_stu4(fa + 1024, fw[1]); lds_stu4(fa + 16384, fw[2]); lds_stu4(fa + 16384 + 1024, fw[3]);
  } else if (b < N_D1 + N_D2) {
    const unsigned fa = lds0 + F_OFF + wid * 1024 + lane * 16;
#pragma unroll
    for (int j = 0; j < 6; ++j) if (wid + 8 * j < 43) lds_stu4(fa + j * 8192, fw[j]);
  }
  {
    u32x4 sv;
    for (unsigned spin = 0;; ++spin) {
      sv = ho_ld(rs, HO_ST + ((tt * 4 + (c8 & 3)) * 32 + row) * 16);
      if (!__any(pend(sv)) || spin > SPIN_CAP) break;
      __builtin_amdgcn_s_sleep(1);
    }
    s1 = quad_sum(__uint_as_float(sv[0])); s2 = quad_sum(__uint_as_float(sv[1])); s3 = quad_sum(__uint_as_float(sv[2]));
  }
  STAMP(5);
  u32x4 lv, ov;
  {
    const float mean = s1 * (1.f / D);
    const float var = fmaxf(s2 * (1.f / D) - mean * mean, 0.f);
    const float mb = rbf(mean);
    const float rden = 1.f / rbf(sqrtf(rbf(rbf(var) + LN_EPS)));
    const unsigned key = sort_key((s3 - mb * swr) * rden, tt * 32 + row);
    if (c8 == 0 && (row >> 3) == cbk) ho_st1(rs, HO_KEY + (tt * 32 + row) * 4, key);
#pragma unroll
    for (int j = 0; j < 4; ++j) {
      const unsigned d = pack_bf16(h[2 * j] - mb, h[2 * j + 1] - mb);
      lv[j] = pack_bf16(bf_lo(d) * rden, bf_hi(d) * rden);
      ov[j] = pack_bf16(h[2 * j] + bf_lo(lv[j]), h[2 * j + 1] + bf_hi(lv[j]));
    }
    ho_st(rs, HO_LN + goff, u32x4{san(lv[0]), san(lv[1]), san(lv[2]), san(lv[3])});
  }
  constexpr unsigned T_OFF = 40960;
  {
    u32x4 kv;
    for (unsigned spin = 0;; ++spin) {
      kv = ho_ld(rs, HO_KEY + tid * 16);
      if (!__any(pend(kv)) || spin > SPIN_CAP) break;
      __builtin_amdgcn_s_sleep(1);
    }
    STAMP(6);
    unsigned k0 = kv[0], k1 = kv[1], k2 = kv[2], k3 = kv[3], t;
    t = max(k0, k1); k1 = min(k0, k1); k0 = t;  t = max(k2, k3); k3 = min(k2, k3); k2 = t;
    t = max(k0, k2); k2 = min(k0, k2); k0 = t;  t = max(k1, k3); k3 = min(k1, k3); k1 = t;
    t = max(k1, k2); k2 = min(k1, k2); k1 = t;
    unsigned mine = 0;
#pragma unroll
    for (int c = 0; c < 8; ++c) {
      const unsigned m = wave_max_u(k0);
      if (lane == c) mine = m;
      if (k0 == m) { k0 = k1; k1 = k2; k2 = k3; k3 = 0; }
    }
    if (lane < 8) lds_stu(lds0 + T_OFF + (wid * 8 + lane) * 4, mine);
    BARRIER();
    const unsigned cand = lds_ldu(lds0 + T_OFF + (tid >> 3) * 4);
    const u32x4 o0 = lds_ldu4(lds0 + T_OFF + (tid & 7) * 32), o1 = lds_ldu4(lds0 + T_OFF + (tid & 7) * 32 + 16);
    unsigned rank = 0;
#pragma unroll
    for (int j = 0; j < 4; ++j) { rank += (o0[j] > cand) ? 1u : 0u; rank += (o1[j] > cand) ? 1u : 0u; }
    rank += dpp_u<DPP_X1>(rank); rank += dpp_u<DPP_X2>(rank); rank += dpp_u<DPP_HMIR>(rank);
    if ((tid & 7) == 0 && rank < 8) lds_stu(lds0 + T_OFF + 256 + rank * 4, cand);
    BARRIER();
  }
  STAMP(7);
  const unsigned mykey = lds_ldu(lds0 + T_OFF + 256 + (lane & 7) * 4);
  const int myrow = 2047 - (int)(mykey & 0x7ffu);
  if (b < N_D1) {
    u32x4 a0, a1;
    const int ao = HO_LN + (myrow * D + wid * 64 + g * 8) * 2;
    for (unsigned spin = 0;; ++spin) {
      a0 = ho_ld(rs, ao); a1 = ho_ld(rs, ao + 64);
      if (!__any(pend(a0) || pend(a1)) || spin > SPIN_CAP) break;
      __builtin_amdgcn_s_sleep(1);
    }
    STAMP(8);
    const unsigned fl = lds0 + F_OFF + (wid * 64 + 8 * g + (i16 >> 2)) * 32 + (i16 & 3) * 8;
    f32x4 ag = {}, au = {};
    ag = MFMA16(BC8(a0), cat8(tr_read(fl), tr_read(fl + 128)), ag);
    au = MFMA16(BC8(a0), cat8(tr_read(fl + 16384), tr_read(fl + 16384 + 128)), au);
    ag = MFMA16(BC8(a1), cat8(tr_read(fl + 1024), tr_read(fl + 1024 + 128)), ag);
    au = MFMA16(BC8(a1), cat8(tr_read(fl + 16384 + 1024), tr_read(fl + 16384 + 1024 + 128)), au);
    constexpr unsigned R_OFF = 49152;
    if (lane < 32) { lds_stf4(lds0 + R_OFF + wid * 1024 + lane * 16, ag); lds_stf4(lds0 + R_OFF + wid * 1024 + 512 + lane * 16, au); }
    BARRIER();
    if (tid < 128) {
      float gs = 0.f, us = 0.f;
#pragma unroll
      for (int w2 = 0; w2 < 8; ++w2) { gs += lds_ldf(lds0 + R_OFF + w2 * 1024 + tid * 4); us += lds_ldf(lds0 + R_OFF + w2 * 1024 + 512 + tid * 4); }
      gs = rbf(gs); us = rbf(us);
      const float sg = rbf(gs / (1.f + __expf(-gs)));
      const float av = sg * us;
      const float nb = __shfl_xor(av, 4);
      const int tok = 4 * (tid >> 6) + (tid & 3), col = (tid >> 2) & 15;
      if (!(col & 1)) ho_st1(rs, HO_ACT + (tok * F + b * 16 + col) * 2, san(pack_bf16(av, nb)));
    }
    STAMP(9);
  }
  {
    const int grow = tt * 32 + row;
    bool sel = false;
#pragma unroll
    for (int c = 0; c < 8; ++c) sel = sel || (__builtin_amdgcn_readlane(myrow, c) == grow);
    if (!sel) *(u32x4*)((char*)out + goff) = ov;
  }
  if (b >= N_D1 && b < N_D1 + N_D2) {
    const int n0 = (b - N_D1) * 16;
    unsigned hw = 0, lw = 0; unsigned fkey = 0;
    const int ftok = 4 * ((tid >> 6) & 1) + (tid & 3), fcol = (tid >> 2) & 15;
    if (tid < 128) {
      fkey = lds_ldu(lds0 + T_OFF + 256 + ftok * 4);
      const int eo = ((2047 - (int)(fkey & 0x7ffu)) * D + n0 + (fcol & ~1)) * 2;
      for (unsigned spin = 0;; ++spin) {
        hw = __builtin_amdgcn_raw_buffer_load_b32(rs, HO_H + eo, 0, 16); lw = __builtin_amdgcn_raw_buffer_load_b32(rs, HO_LN + eo, 0, 16);
        if (!__any(hw == SENT || lw == SENT) || spin > SPIN_CAP) break;
        __builtin_amdgcn_s_sleep(1);
      }
    }
    u32x4 av[6];
    const int ao = HO_ACT + ((lane & 7) * F + wid * 32 + g * 8) * 2;
    for (unsigned spin = 0;; ++spin) {
      bool p = false;
#pragma unroll
      for (int j = 0; j < 6; ++j) {
        av[j] = (wid + 8 * j < 43) ? ho_ld(rs, ao + j * 512) : u32x4{0u, 0u, 0u, 0u};
        p = p || pend(av[j]);
      }
      if (!__any(p) || spin > SPIN_CAP) break;
      __builtin_amdgcn_s_sleep(1);
    }
    STAMP(8);
    const unsigned fl = lds0 + F_OFF + (wid * 32 + 8 * g + (i16 >> 2)) * 32 + (i16 & 3) * 8;
    f32x4 y = {};
#pragma unroll
    for (int j = 0; j < 6; ++j) if (wid + 8 * j < 43) y = MFMA16(BC8(av[j]), cat8(tr_read(fl + j * 8192), tr_read(fl + j * 8192 + 128)), y);
    constexpr unsigned R_OFF = 49152;
    if (lane < 32) lds_stf4(lds0 + R_OFF + wid * 512 + lane * 16, y);
    BARRIER();
    if (tid < 128) {
      float ys = 0.f;
#pragma unroll
      for (int w2 = 0; w2 < 8; ++w2) ys += lds_ldf(lds0 + R_OFF + w2 * 512 + tid * 4);
      const float sig = rbf(1.f / (1.f + __expf(-key_score(fkey))));
      ys = rbf(rbf(ys) * sig);
      const float hh2 = (fcol & 1) ? bf_hi(hw) : bf_lo(hw), ll = (fcol & 1) ? bf_hi(lw) : bf_lo(lw);
      out[(size_t)(2047 - (int)(fkey & 0x7ffu)) * D + n0 + fcol] = (uint16_t)(pack_bf16(hh2 + rbf(ll + ys), 0.f) & 0xffffu);
    }
    STAMP(10);
  }
  STAMP(11); STAMP_OUT(2);
}

extern "C" void kernel_launch(void* const* d_in, const int* in_sizes, int n_in, void* d_out, int out_size, void* d_ws, size_t ws_size,
                              hipStream_t stream) {
  const uint16_t* x = (const uint16_t*)d_in[0];
  const uint16_t* wqkv = (const uint16_t*)d_in[1];
  const uint16_t* wo = (const uint16_t*)d_in[2];
  const uint16_t* wr = (const uint16_t*)d_in[4];
  const uint16_t* wg = (const uint16_t*)d_in[5];
  const uint16_t* wu = (const uint16_t*)d_in[6];
  const uint16_t* wd = (const uint16_t*)d_in[7];
  char* ws = (char*)d_ws;
  uint16_t* out = (uint16_t*)d_out;
  (void)hipFuncSetAttribute((const void*)k_qkv, hipFuncAttributeMaxDynamicSharedMemorySize, A_LDS_BYTES);
  (void)hipFuncSetAttribute((const void*)k_attn, hipFuncAttributeMaxDynamicSharedMemorySize, B_LDS_BYTES);
  (void)hipFuncSetAttribute((const void*)k_tail, hipFuncAttributeMaxDynamicSharedMemorySize, C_LDS_BYTES);
  k_qkv<<<256, 512, A_LDS_BYTES, stream>>>(x, wqkv, ws);
  k_attn<<<256, 512, B_LDS_BYTES, stream>>>(ws, (const char*)wo, (const char*)wg, (const char*)wu, (const char*)wd);
  k_tail<<<256, 512, C_LDS_BYTES, stream>>>(x, wo, wr, wg, wu, wd, ws, out);
}
```

```cpp
#include <hip/hip_runtime.h>
#include <hip/hip_bf16.h>
#include <stdint.h>

typedef short bf16x8 __attribute__((ext_vector_type(8)));
typedef short bf16x4 __attribute__((ext_vector_type(4)));
typedef float f32x16 __attribute__((ext_vector_type(16)));
typedef float f32x4 __attribute__((ext_vector_type(4)));
typedef float f32x2 __attribute__((ext_vector_type(2)));
typedef __bf16 bf16x2_t __attribute__((ext_vector_type(2)));
typedef unsigned u32x4 __attribute__((ext_vector_type(4)));
typedef unsigned u32x2 __attribute__((ext_vector_type(2)));
#define LAS __attribute__((address_space(3)))
#define GAS __attribute__((address_space(1)))
#define DI __device__ __forceinline__
#define MFMA32(a, b, c) __builtin_amdgcn_mfma_f32_32x32x16_bf16((a), (b), (c), 0, 0, 0)
#define MFMA16(a, b, c) __builtin_amdgcn_mfma_f32_16x16x32_bf16((a), (b), (c), 0, 0, 0)
#define MFMA4(a, b, c) __builtin_amdgcn_mfma_f32_4x4x4bf16_1k((a), (b), (c), 0, 0, 0)
#define SBAR() __builtin_amdgcn_sched_barrier(0)
#define PIN(x) asm volatile("" : "+v"(x))
#define BC8(x) __builtin_bit_cast(bf16x8, x)

constexpr int S = 2048, D = 512, H = 32, F = 1376;
constexpr float LN_EPS = 1e-5f;
constexpr float QSCALE = 0.25f * 1.4426950408889634f;
constexpr unsigned SENT = 0xFFFFFFFFu;
constexpr unsigned SPIN_CAP = 1u << 16;

constexpr int WS_Q = 0;
constexpr int WS_KV = WS_Q + H * 64 * 1024;
constexpr int WS_FA = WS_KV + H * 64 * 2048;
constexpr int WS_FR = WS_FA + 256 * 128;
constexpr int WS_EP = WS_FR + 4096;
constexpr int WS_HO = WS_EP + 4096;
constexpr int HO_O = WS_HO;
constexpr int HO_LN = HO_O + 64 * H * 1024;
constexpr int HO_ST = HO_LN + S * D * 2;
constexpr int HO_KEY = HO_ST + 64 * 4 * 512;
constexpr int HO_ACT = HO_KEY + 8 * S * 4;
constexpr int HO_HS = HO_ACT + 24576;
constexpr int HO_END = HO_HS + 8192;
constexpr int HO_PER_WG = 17 * 1024;
static_assert(WS_HO + 256 * HO_PER_WG >= HO_END, "reset covers the hand-off words");
constexpr int WS_BYTES = WS_HO + 256 * HO_PER_WG;

#define STAMP_DECL do {} while (0)
#define STAMP(i) do {} while (0)
#define STAMP_OUT(k) do {} while (0)

DI unsigned pack_bf16(float lo, float hi) {
  f32x2 v = {lo, hi};
  bf16x2_t r = __builtin_convertvector(v, bf16x2_t);
  return __builtin_bit_cast(unsigned, r);
}
DI float bf_lo(unsigned v) { return __uint_as_float(v << 16); }
DI float bf_hi(unsigned v) { return __uint_as_float(v & 0xffff0000u); }
DI float rbf(float x) { return bf_lo(pack_bf16(x, x)); }
DI float sum_halves(float v) {
  auto r = __builtin_amdgcn_permlane32_swap(__float_as_uint(v), __float_as_uint(v), false, false);
  return __uint_as_float(r[0]) + __uint_as_float(r[1]);
}
DI float sum_rows(float v) {
  auto r = __builtin_amdgcn_permlane16_swap(__float_as_uint(v), __float_as_uint(v), false, false);
  return sum_halves(__uint_as_float(r[0]) + __uint_as_float(r[1]));
}
DI float max_halves(float v) {
  auto r = __builtin_amdgcn_permlane32_swap(__float_as_uint(v), __float_as_uint(v), false, false);
  return fmaxf(__uint_as_float(r[0]), __uint_as_float(r[1]));
}
template <int CTRL> DI float dpp_f(float v) { return __uint_as_float(__builtin_amdgcn_update_dpp(0u, __float_as_uint(v), CTRL, 0xf, 0xf, true)); }
template <int CTRL> DI unsigned dpp_u(unsigned v) { return __builtin_amdgcn_update_dpp(0u, v, CTRL, 0xf, 0xf, true); }
constexpr int DPP_X1 = 0xB1, DPP_X2 = 0x4E, DPP_HMIR = 0x141, DPP_MIR = 0x140;
DI float quad_sum(float v) { v += dpp_f<DPP_X1>(v); v += dpp_f<DPP_X2>(v); return v; }
DI float row16_sum(float v) { v = quad_sum(v); v += dpp_f<DPP_HMIR>(v); v += dpp_f<DPP_MIR>(v); return v; }
DI unsigned wave_max_u(unsigned v) {
  v = max(v, dpp_u<DPP_X1>(v)); v = max(v, dpp_u<DPP_X2>(v)); v = max(v, dpp_u<DPP_HMIR>(v)); v = max(v, dpp_u<DPP_MIR>(v));
  v = max(v, (unsigned)__builtin_amdgcn_update_dpp(0u, v, 0x142, 0xa, 0xf, false));
  v = max(v, (unsigned)__builtin_amdgcn_update_dpp(0u, v, 0x143, 0xc, 0xf, false));
  return (unsigned)__builtin_amdgcn_readlane((int)v, 63);
}
DI void glds16(const void* sbase, unsigned voff, unsigned lds_base) {
  unsigned sv;
  asm volatile("s_mov_b32 %0, m0\n\ts_mov_b32 m0, %3\n\ts_nop 0\n\tglobal_load_lds_dwordx4 %1, %2\n\ts_mov_b32 m0, %0"
               : "=&s"(sv) : "v"(voff), "s"(sbase), "s"(lds_base) : "memory");
}
DI void glds16s(const void* sbase, unsigned voff, unsigned lds_base) {
  unsigned sv;
  asm volatile("s_mov_b32 %0, m0\n\ts_mov_b32 m0, %3\n\ts_nop 0\n\tglobal_load_lds_dwordx4 %1, %2 sc1\n\ts_mov_b32 m0, %0"
               : "=&s"(sv) : "v"(voff), "s"(sbase), "s"(lds_base) : "memory");
}
#define WAIT_VM(N) asm volatile("s_waitcnt vmcnt(" #N ")" ::: "memory")
#define WAIT_LGKM0() asm volatile("s_waitcnt lgkmcnt(0)" ::: "memory")
#define BARRIER() asm volatile("s_waitcnt lgkmcnt(0)\n\ts_barrier" ::: "memory")
typedef short v4i16_t __attribute__((ext_vector_type(4)));
DI bf16x4 tr_read(unsigned addr) {
  return __builtin_bit_cast(bf16x4, __builtin_amdgcn_ds_read_tr16_b64_v4i16((LAS v4i16_t*)(uintptr_t)addr));
}
DI bf16x8 lds_ld128(unsigned addr) { return *(const LAS bf16x8*)(uintptr_t)addr; }
DI f32x4 lds_ldf4(unsigned addr) { return *(const LAS f32x4*)(uintptr_t)addr; }
DI u32x4 lds_ldu4(unsigned addr) { return *(const LAS u32x4*)(uintptr_t)addr; }
DI void lds_stf4(unsigned addr, f32x4 v) { *(LAS f32x4*)(uintptr_t)addr = v; }
DI void lds_stu4(unsigned addr, u32x4 v) { *(LAS u32x4*)(uintptr_t)addr = v; }
DI void lds_stf(unsigned addr, float v) { *(LAS float*)(uintptr_t)addr = v; }
DI void lds_stu(unsigned addr, unsigned v) { *(LAS unsigned*)(uintptr_t)addr = v; }
DI float lds_ldf(unsigned addr) { return *(const LAS float*)(uintptr_t)addr; }
DI unsigned lds_ldu(unsigned addr) { return *(const LAS unsigned*)(uintptr_t)addr; }
DI bf16x8 cat8(bf16x4 a, bf16x4 b) { return __builtin_shufflevector(a, b, 0, 1, 2, 3, 4, 5, 6, 7); }
DI bf16x4 lo4(bf16x8 a) { return __builtin_shufflevector(a, a, 0, 1, 2, 3); }
DI bf16x4 hi4(bf16x8 a) { return __builtin_shufflevector(a, a, 4, 5, 6, 7); }
typedef __amdgpu_buffer_rsrc_t rsrc_t;
DI u32x4 ho_ld(rsrc_t rs, int off) { return __builtin_amdgcn_raw_buffer_load_b128(rs, off, 0, 16); }
DI void ho_st(rsrc_t rs, int off, u32x4 v) { __builtin_amdgcn_raw_buffer_store_b128(v, rs, off, 0, 16); }
DI void ho_st1(rsrc_t rs, int off, unsigned v) { __builtin_amdgcn_raw_buffer_store_b32(v, rs, off, 0, 16); }
DI unsigned san(unsigned v) { return min(v, SENT - 1u); }
DI bool pend(u32x4 v) { return max(max(v[0], v[1]), max(v[2], v[3])) == SENT; }

constexpr int LDS_BYTES = 163840;
constexpr int KVT = 2080;
constexpr int L_PRIV = 64 * KVT, PRIV = 3840;
constexpr int N_D1 = 86;
struct AFrag { bf16x8 x0, x1, q, k, v; };

#define EX(x) __builtin_amdgcn_exp2f(x)
#define PK(a, b) pack_bf16(a, b)
#define UNIT(SC, SN, KN, QN, NEGN, OPREV, VPREV, OCUR, VCUR0, LOADS) do { \
    float e0 = EX(SC[0]), e1 = EX(SC[1]); SBAR(); \
    OPREV = MFMA32(VPREV, BC8(p1), OPREV); \
    LOADS; \
    SBAR(); \
    float e2 = EX(SC[2]), e3 = EX(SC[3]); SBAR(); \
    p0[0] = PK(e0, e1); SBAR(); float e4 = EX(SC[4]), e5 = EX(SC[5]); SBAR(); \
    p0[1] = PK(e2, e3); SBAR(); float e6 = EX(SC[6]), e7 = EX(SC[7]); SBAR(); \
    SN = MFMA32(KN, QN, NEGN); SBAR(); \
    p0[2] = PK(e4, e5); SBAR(); e0 = EX(SC[8]); e1 = EX(SC[9]); SBAR(); \
    p0[3] = PK(e6, e7); SBAR(); e2 = EX(SC[10]); e3 = EX(SC[11]); SBAR(); \
    OCUR = MFMA32(VCUR0, BC8(p0), OCUR); SBAR(); \
    p1[0] = PK(e0, e1); SBAR(); e4 = EX(SC[12]); e5 = EX(SC[13]); SBAR(); \
    p1[1] = PK(e2, e3); SBAR(); e6 = EX(SC[14]); e7 = EX(SC[15]); SBAR(); \
    p1[2] = PK(e4, e5); SBAR(); \
    p1[3] = PK(e6, e7); SBAR(); \
  } while (0)
#define TWO_TILES(U) do { \
    UNIT(sA, sB, k0, qb2, ng, ob, v11, oa, v00, k1 = lds_ld128(ka + (U + 1) * KVT); v10 = lds_ld128(va0 + (U + 1) * KVT)); \
    UNIT(sB, sA, k1, qa, ng, oa, v01, ob, v00, v11 = lds_ld128(va1 + (U + 1) * KVT)); \
    UNIT(sA, sB, k1, qb2, ng, ob, v01, oa, v10, k0 = lds_ld128(ka + (U + 2) * KVT); v00 = lds_ld128(va0 + (U + 2) * KVT)); \
    UNIT(sB, sA, k0, qa, ng, oa, v11, ob, v10, v01 = lds_ld128(va1 + (U + 2) * KVT)); \
  } while (0)

DI float tile_max(const f32x16& s) {
  float m = fmaxf(fmaxf(s[0], s[1]), s[2]);
#pragma unroll
  for (int j = 3; j < 15; j += 2) m = fmaxf(fmaxf(m, s[j]), s[j + 1]);
  return fmaxf(m, s[15]);
}
DI unsigned sort_key(float r, int idx) {
  unsigned u = pack_bf16(r, r) & 0xffffu;
  if ((u & 0x7fffu) > 0x7f80u) u = 0xffffu;
  u = (u & 0x8000u) ? (~u & 0xffffu) : (u | 0x8000u);
  return (u << 16) | (unsigned)(2047 - idx);
}
DI float key_score(unsigned key) {
  unsigned u = key >> 16;
  u = (u & 0x8000u) ? (u & 0x7fffu) : (~u & 0xffffu);
  return __uint_as_float(u << 16);
}
DI void ln_stats(float s1, float s2, float& mb, float& rden) {
  const float mean = s1 * (1.f / D);
  const float var = fmaxf(s2 * (1.f / D) - mean * mean, 0.f);
  mb = rbf(mean);
  rden = 1.f / rbf(sqrtf(rbf(rbf(var) + LN_EPS)));
}

__global__ __launch_bounds__(512, 1) void k_fused(const uint16_t* __restrict__ x, const uint16_t* __restrict__ wqkv, const uint16_t* __restrict__ wo,
                                                   const uint16_t* __restrict__ wr, const uint16_t* __restrict__ wg, const uint16_t* __restrict__ wu,
                                                   const uint16_t* __restrict__ wd, char* __restrict__ ws, uint16_t* __restrict__ out) {
  extern __shared__ __attribute__((aligned(16))) char smem[];
  const unsigned lds0 = (unsigned)(uintptr_t)smem;
  const int tid = threadIdx.x, lane = tid & 63, wid = __builtin_amdgcn_readfirstlane(tid >> 6);
  const int b = blockIdx.x, xcd = b & 7, sub = b >> 3;
  const int r32 = lane & 31, hh = lane >> 5, g = lane >> 4, i16 = lane & 15;
  const rsrc_t rs = __builtin_amdgcn_make_buffer_rsrc(ws, 0, WS_BYTES, 0x00020000);
  STAMP_DECL; STAMP(0);

  unsigned tag;
  {
    const int rb = (xcd >> 1) * 8 + (sub & 7), hq = (xcd & 1) * 4 + (sub >> 3);
    const int rot = ((sub & 7) + 2 * (sub >> 3)) & 7;
    const int rt = wid & 1, hd = wid >> 1, sw = hd >> 1, par = rt ^ (hd & 1);
    const unsigned wvo = (8 * wid + (lane >> 3)) * 3072 + hq * 128 + (((lane & 7) ^ (((g & 1) | ((wid & 1) << 1)) << 1)) * 16);
    const unsigned xvo = (rb * 64 + 8 * wid + (lane >> 3)) * 1024 + (((lane & 7) ^ ((4 * wid + g) & 7)) * 16);
    auto issue = [&](int c) {
      const int kc = (c + rot) & 7;
      const unsigned dst = lds0 + (c & 3) * 32768 + wid * 1024;
      const char* wb = (const char*)wqkv + (size_t)kc * (64 * 3072);
      glds16((const char*)x + kc * 128, xvo, dst + 24576);
      glds16(wb, wvo, dst);
      glds16(wb + 1024, wvo, dst + 8192);
      glds16(wb + 2048, wvo, dst + 16384);
    };
    issue(0); issue(1); issue(2); issue(3);
    const int xr0 = rt * 32 + sw * 16 + i16, xr1 = xr0 ^ 16;
    const int xf = (i16 >> 1) & 7;
    const unsigned xb0 = lds0 + 24576 + (xr0 >> 3) * 1024 + (xr0 & 7) * 128, xb1 = lds0 + 24576 + (xr1 >> 3) * 1024 + (xr1 & 7) * 128;
    const unsigned xoA = ((4 * par + g) ^ xf) * 16, xoB = ((4 * (par ^ 1) + g) ^ xf) * 16;
    const unsigned wl = lds0 + (8 * g + (i16 >> 2)) * 128 + ((hd ^ (((i16 >> 3) & 1) | ((g & 1) << 1))) * 32) + (i16 & 3) * 8;
    const unsigned wA = wl + par * 4096, wB = wl + (par ^ 1) * 4096;
    auto rd = [&](unsigned xo, unsigned wa, int c) {
      const unsigned so = (c & 3) * 32768;
      AFrag f;
      f.x0 = lds_ld128(xb0 + xo + so); f.x1 = lds_ld128(xb1 + xo + so);
      f.q = cat8(tr_read(wa + so), tr_read(wa + so + 512));
      f.k = cat8(tr_read(wa + so + 8192), tr_read(wa + so + 8192 + 512));
      f.v = cat8(tr_read(wa + so + 16384), tr_read(wa + so + 16384 + 512));
      return f;
    };
    f32x4 aq0 = {}, aq1 = {}, ak0 = {}, ak1 = {}, av0 = {}, av1 = {};
    f32x4 rsum = {}, rsq = {}, cs0 = {}, cs1 = {}, cs2 = {};
    const bf16x4 ones4 = {0x3f80, 0x3f80, 0x3f80, 0x3f80};
    WAIT_VM(12);
    BARRIER();
    STAMP(1);
    AFrag fa = rd(xoA, wA, 0), fb = rd(xoB, wB, 0);
#pragma unroll
    for (int c = 0; c < 8; ++c) {
      if (c <= 4) WAIT_VM(8); else if (c == 5) WAIT_VM(4); else if (c == 6) WAIT_VM(0);
      BARRIER();
      SBAR();
      if (c + 4 < 8) issue(c + 4);
      AFrag na, nb;
      if (c + 1 < 8) { na = rd(xoA, wA, c + 1); nb = rd(xoB, wB, c + 1); }
      aq0 = MFMA16(fa.q, fa.x0, aq0); aq1 = MFMA16(fa.q, fa.x1, aq1);
      ak0 = MFMA16(fa.k, fa.x0, ak0); ak1 = MFMA16(fa.k, fa.x1, ak1);
      av0 = MFMA16(fa.x0, fa.v, av0); av1 = MFMA16(fa.x1, fa.v, av1);
      rsum = MFMA4(ones4, lo4(fa.x0), rsum); rsq = MFMA4(lo4(fa.x0), lo4(fa.x0), rsq);
      cs0 = MFMA4(ones4, lo4(fa.q), cs0); cs1 = MFMA4(ones4, lo4(fa.k), cs1); cs2 = MFMA4(ones4, lo4(fa.v), cs2);
      rsum = MFMA4(ones4, hi4(fa.x0), rsum); rsq = MFMA4(hi4(fa.x0), hi4(fa.x0), rsq);
      cs0 = MFMA4(ones4, hi4(fa.q), cs0); cs1 = MFMA4(ones4, hi4(fa.k), cs1); cs2 = MFMA4(ones4, hi4(fa.v), cs2);
      aq0 = MFMA16(fb.q, fb.x0, aq0); aq1 = MFMA16(fb.q, fb.x1, aq1);
      ak0 = MFMA16(fb.k, fb.x0, ak0); ak1 = MFMA16(fb.k, fb.x1, ak1);
      av0 = MFMA16(fb.x0, fb.v, av0); av1 = MFMA16(fb.x1, fb.v, av1);
      SBAR();
      if (c + 1 < 8) { fa = na; fb = nb; }
      if (c == 6) tag = __builtin_amdgcn_raw_buffer_load_b32(rs, WS_EP, 0, 16);
    }
    STAMP(2);
    const unsigned RS = lds0 + L_PRIV + 2048, CS = RS + PRIV, MR = lds0 + L_PRIV + wid * PRIV;
    {
      const int l3 = lane & 3;
      const float dq = l3 == 0 ? rsq[0] : l3 == 1 ? rsq[1] : l3 == 2 ? rsq[2] : rsq[3];
      const float s1 = sum_rows(rsum[0]), s2 = sum_rows(dq);
      const float c0 = sum_rows(cs0[0]), c1 = sum_rows(cs1[0]), c2 = sum_rows(cs2[0]);
      if (lane < 16) {
        const unsigned ra = RS + ((rt * 4 + hd) * 16 + lane) * 8;
        lds_stf(ra, s1); lds_stf(ra + 4, s2);
        const unsigned ca = CS + ((hd * 2 + rt) * 48 + lane) * 4;
        lds_stf(ca, c0); lds_stf(ca + 64, c1); lds_stf(ca + 128, c2);
      }
      if (tid < 128) {
        const unsigned o = (tid & 1) ? 0u : 0x3f803f80u;
        lds_stu4(lds0 + (tid >> 1) * KVT + 2048 + (tid & 1) * 16, u32x4{o, o, o, o});
      }
    }
    BARRIER();
    {
      const unsigned ra = RS + ((rt * 4 + (g & 1) * 2) * 16 + i16) * 8;
      float mb, rden;
      ln_stats(lds_ldf(ra) + lds_ldf(ra + 128), lds_ldf(ra + 4) + lds_ldf(ra + 132), mb, rden);
      if (lane < 32) { lds_stf(MR + r32 * 4, mb); lds_stf(MR + 128 + r32 * 4, rden); }
      const unsigned ca = CS + hd * 2 * 48 * 4;
      const f32x4 cq = lds_ldf4(ca + g * 16) + lds_ldf4(ca + 192 + g * 16), ck = lds_ldf4(ca + 64 + g * 16) + lds_ldf4(ca + 192 + 64 + g * 16);
      const float cv = lds_ldf(ca + 128 + i16 * 4) + lds_ldf(ca + 192 + 128 + i16 * 4);
      WAIT_LGKM0();
      const int t0 = sw * 16, t1 = t0 ^ 16;
      const float m0 = lds_ldf(MR + (t0 + i16) * 4), m1 = lds_ldf(MR + (t1 + i16) * 4), d0 = lds_ldf(MR + 128 + (t0 + i16) * 4), d1 = lds_ldf(MR + 128 + (t1 + i16) * 4);
      const f32x4 mv0 = lds_ldf4(MR + (t0 + 4 * g) * 4), mv1 = lds_ldf4(MR + (t1 + 4 * g) * 4), dv0 = lds_ldf4(MR + 128 + (t0 + 4 * g) * 4), dv1 = lds_ldf4(MR + 128 + (t1 + 4 * g) * 4);
      const int head = hq * 4 + hd, tile = rb * 2 + rt;
      unsigned a0, a1, b0, b1;
      a0 = pack_bf16((ak0[0] - m0 * ck[0]) * d0, (ak0[1] - m0 * ck[1]) * d0); a1 = pack_bf16((ak0[2] - m0 * ck[2]) * d0, (ak0[3] - m0 * ck[3]) * d0);
      b0 = pack_bf16((ak1[0] - m1 * ck[0]) * d1, (ak1[1] - m1 * ck[1]) * d1); b1 = pack_bf16((ak1[2] - m1 * ck[2]) * d1, (ak1[3] - m1 * ck[3]) * d1);
      { auto r0 = __builtin_amdgcn_permlane16_swap(a0, b0, false, false); auto r1 = __builtin_amdgcn_permlane16_swap(a1, b1, false, false);
        ho_st(rs, WS_KV + (head * 64 + tile) * 2048 + (lane ^ t0) * 16, u32x4{r0[0], r1[0], r0[1], r1[1]}); }
      a0 = pack_bf16((av0[0] - mv0[0] * cv) * dv0[0], (av0[1] - mv0[1] * cv) * dv0[1]); a1 = pack_bf16((av0[2] - mv0[2] * cv) * dv0[2], (av0[3] - mv0[3] * cv) * dv0[3]);
      b0 = pack_bf16((av1[0] - mv1[0] * cv) * dv1[0], (av1[1] - mv1[1] * cv) * dv1[1]); b1 = pack_bf16((av1[2] - mv1[2] * cv) * dv1[2], (av1[3] - mv1[3] * cv) * dv1[3]);
      { auto r0 = __builtin_amdgcn_permlane32_swap(a0, b0, false, false); auto r1 = __builtin_amdgcn_permlane32_swap(a1, b1, false, false);
        ho_st(rs, WS_KV + (head * 64 + tile) * 2048 + 1024 + (lane ^ (sw * 32)) * 16, u32x4{r0[0], r1[0], r0[1], r1[1]}); }
      const float e0 = d0 * QSCALE, e1 = d1 * QSCALE;
      a0 = pack_bf16((aq0[0] - m0 * cq[0]) * e0, (aq0[1] - m0 * cq[1]) * e0); a1 = pack_bf16((aq0[2] - m0 * cq[2]) * e0, (aq0[3] - m0 * cq[3]) * e0);
      b0 = pack_bf16((aq1[0] - m1 * cq[0]) * e1, (aq1[1] - m1 * cq[1]) * e1); b1 = pack_bf16((aq1[2] - m1 * cq[2]) * e1, (aq1[3] - m1 * cq[3]) * e1);
      { auto r0 = __builtin_amdgcn_permlane16_swap(a0, b0, false, false); auto r1 = __builtin_amdgcn_permlane16_swap(a1, b1, false, false);
        ho_st(rs, WS_Q + (head * 64 + tile) * 1024 + (lane ^ t0) * 16, u32x4{r0[0], r1[0], r0[1], r1[1]}); }
    }
    tag = __builtin_amdgcn_readfirstlane(tag) + 1u;
    STAMP(3);
    WAIT_VM(0);
    BARRIER();
    if (tid == 0) __builtin_amdgcn_raw_buffer_store_b64(u32x2{tag, ~tag}, rs, WS_FA + (hq * 32 + rb) * 128, 0, 16);
    STAMP(4);
  }

  const int head = xcd * 4 + (sub & 3), qb = sub >> 2;
  const int qg = wid & 3, kh = wid >> 2;
  const int tt = xcd * 8 + (sub >> 2), cbk = sub & 3;
  const int d1 = (xcd < 7 && sub < 13) ? xcd * 13 + sub : 999;
  const bool isd1 = d1 < N_D1, isd2 = xcd == 7;
  const unsigned priv = lds0 + L_PRIV + wid * PRIV;
  if (wid == 0) {
    for (unsigned spin = 0;; ++spin) {
      const u32x2 f = __builtin_amdgcn_raw_buffer_load_b64(rs, WS_FA + (xcd * 32 + r32) * 128, 0, 16);
      if (__all(f[0] == tag && f[1] == ~tag) || spin > SPIN_CAP) break;
      __builtin_amdgcn_s_sleep(1);
    }
  }
  BARRIER();
  STAMP(5);
  const char* kvg = ws + WS_KV + (size_t)head * (64 * 2048) + ((wid & 1) * 32 + (wid >> 1)) * 2048;
  const unsigned kvl = lds0 + ((wid & 1) * 32 + (wid >> 1)) * KVT;
  auto kv_round = [&](int j) { glds16s(kvg, lane * 16 + j * 8192, kvl + j * 4 * KVT); glds16s(kvg, lane * 16 + j * 8192 + 1024, kvl + j * 4 * KVT + 1024); };
  {
    const char* qsrc = ws + WS_Q + (size_t)(head * 64 + qb * 8 + qg * 2) * 1024;
    glds16s(qsrc, lane * 16, priv); glds16s(qsrc + 1024, lane * 16, priv + 1024);
    kv_round(0); kv_round(1);
  }
  const unsigned kaddr = lds0 + kh * 32 * KVT + lane * 16;
  const unsigned vaddr0 = lds0 + kh * 32 * KVT + (r32 < 16 ? 1024 + (hh * 16 + r32) * 16 : (r32 == 16 ? 2048 : 2064));
  const unsigned vaddr1 = vaddr0 + (r32 < 16 ? 512 : 0);
  bf16x4 wl_[16], wh_[16];
#pragma unroll
  for (int i = 0; i < 16; ++i) { wl_[i] = bf16x4{0, 0, 0, 0}; wh_[i] = bf16x4{0, 0, 0, 0}; }
  const unsigned wovo = (kh * 256 + (lane >> 2)) * 1024 + cbk * 256 + qg * 64 + (lane & 3) * 16;
  const unsigned wor = priv + (4 * (g >> 1) + (i16 >> 2)) * 64 + (g & 1) * 32 + (i16 & 3) * 8;
#define WO_STEP(K, F, F1, F2, N, N1, N2) do { unsigned sv_, tv_; \
    asm volatile("s_cmp_lg_u32 %[it], " #K "\n\ts_cbranch_scc1 .Lwo%=\n\ts_waitcnt vmcnt(0)\n\t" \
      "ds_read_b64_tr_b16 %[a0], %[wor]\n\tds_read_b64_tr_b16 %[a1], %[wor] offset:512\n\t" \
      "ds_read_b64_tr_b16 %[a2], %[wor] offset:1024\n\tds_read_b64_tr_b16 %[a3], %[wor] offset:1536\n\t" \
      "ds_read_b64_tr_b16 %[a4], %[wor] offset:2048\n\tds_read_b64_tr_b16 %[a5], %[wor] offset:2560\n\t" \
      "s_mov_b32 %[sv], m0\n\ts_mov_b32 m0, %[priv]\n\tv_add_u32 %[tv], " #N "*0x4000, %[wovo]\n\ts_waitcnt lgkmcnt(0)\n\t" \
      "global_load_lds_dwordx4 %[tv], %[wo]\n\ts_add_u32 m0, m0, 0x400\n\tv_add_u32 %[tv], " #N1 "*0x4000, %[wovo]\n\t" \
      "global_load_lds_dwordx4 %[tv], %[wo]\n\ts_add_u32 m0, m0, 0x400\n\tv_add_u32 %[tv], " #N2 "*0x4000, %[wovo]\n\t" \
      "global_load_lds_dwordx4 %[tv], %[wo]\n\ts_mov_b32 m0, %[sv]\n.Lwo%=:" \
      : [a0] "+v"(wl_[F]), [a1] "+v"(wh_[F]), [a2] "+v"(wl_[F1]), [a3] "+v"(wh_[F1]), [a4] "+v"(wl_[F2]), [a5] "+v"(wh_[F2]), [sv] "=&s"(sv_), [tv] "=&v"(tv_) \
      : [it] "s"(it), [wor] "v"(wor), [priv] "s"(priv), [wovo] "v"(wovo), [wo] "s"(wo) : "memory", "scc"); } while (0)
#define WO_LAST(K) \
    asm volatile("s_cmp_lg_u32 %[it], " #K "\n\ts_cbranch_scc1 .Lwo%=\n\ts_waitcnt vmcnt(0)\n\t" \
      "ds_read_b64_tr_b16 %[a0], %[wor]\n\tds_read_b64_tr_b16 %[a1], %[wor] offset:512\n\ts_waitcnt lgkmcnt(0)\n.Lwo%=:" \
      : [a0] "+v"(wl_[15]), [a1] "+v"(wh_[15]) : [it] "s"(it), [wor] "v"(wor) : "memory", "scc")
  auto ff_piece = [&](int p, unsigned dst) {
    if (isd1) { if (p < 32) glds16(p < 16 ? wg : wu, ((p & 15) * 32 + (lane >> 1)) * (F * 2) + d1 * 32 + (lane & 1) * 16, dst); }
    else if (isd2) { if (p < 43) glds16(wd, (p * 32 + (lane >> 1)) * (D * 2) + sub * 32 + (lane & 1) * 16, dst); }
  };
  WAIT_VM(2);
  BARRIER();
  STAMP(6);
  const bf16x8 qa = lds_ld128(priv + lane * 16), qb2 = lds_ld128(priv + 1024 + lane * 16);
  float mref;
  {
    const f32x16 z = {};
    const bf16x8 k0 = lds_ld128(kaddr);
    mref = max_halves(fmaxf(tile_max(MFMA32(k0, qa, z)), tile_max(MFMA32(k0, qb2, z))));
  }
  f32x16 oa, ob;
  float ma = mref, mb = mref;
  {
    f32x16 ng;
#pragma unroll
    for (int j = 0; j < 16; ++j) { ng[j] = -mref; oa[j] = 0.f; ob[j] = 0.f; }
    PIN(ng);
    unsigned ka = kaddr, va0 = vaddr0, va1 = vaddr1;
    bf16x8 k0 = lds_ld128(ka), k1, v00 = lds_ld128(va0), v01 = lds_ld128(va1), v10, v11 = v00;
    f32x16 sA = MFMA32(k0, qa, ng), sB;
    u32x4 p0, p1 = {0, 0, 0, 0};
    asm volatile("s_nop 7\n\ts_nop 7" : "+v"(sA));
    for (int it = 0; it < 8; ++it) {
      if (it <= 1) { kv_round(4 * it + 2); kv_round(4 * it + 3); }
      WO_STEP(2, 0, 1, 2, 3, 4, 5);
      WO_STEP(3, 6, 7, 8, 9, 10, 11);
      WO_STEP(4, 12, 13, 14, 15, 15, 15);
      SBAR();
      TWO_TILES(0);
      if (it <= 1) {
        if (it == 0) { WAIT_VM(4); asm volatile("s_barrier" ::: "memory"); kv_round(4); kv_round(5); }
        else {
          WAIT_VM(0); asm volatile("s_barrier" ::: "memory");
          glds16(wo, wovo, priv); glds16(wo, wovo + 0x4000, priv + 1024); glds16(wo, wovo + 0x8000, priv + 2048);
          if (wid == 3) {
            const u32x4 sv = {SENT, SENT, SENT, SENT};
            for (int j = 0; j < 17; ++j) ho_st(rs, WS_HO + b * HO_PER_WG + j * 1024 + lane * 16, sv);
          }
        }
      }
      WO_STEP(2, 3, 4, 5, 6, 7, 8);
      WO_STEP(3, 9, 10, 11, 12, 13, 14);
      WO_LAST(4);
      if (it == 4) {
        if (wid == 3 && lane == 0) __builtin_amdgcn_raw_buffer_store_b64(u32x2{tag, ~tag}, rs, WS_FR + b * 8, 0, 16);
        for (int j = 0; j < 6; ++j) ff_piece(wid + 8 * j, priv);
      }
      SBAR();
      TWO_TILES(2);
      ka += 4 * KVT; va0 += 4 * KVT; va1 += 4 * KVT;
    }
    ob = MFMA32(v11, BC8(p1), ob);
  }
  {
    const float la = sum_halves(oa[8]), lb = sum_halves(ob[8]);
    float t = 0.f;
#pragma unroll
    for (int j = 0; j < 8; ++j) t += fabsf(oa[j]) + fabsf(ob[j]);
    const bool ok = (la <= 16777216.f) && (lb <= 16777216.f) && (la >= 5.9604645e-8f) && (lb >= 5.9604645e-8f) && (t <= 3.0e38f);
    if (__builtin_expect(!__all(ok), 0)) {
      const f32x16 z = {};
      ma = -INFINITY; mb = -INFINITY;
      for (int t2 = 0; t2 < 32; ++t2) {
        const bf16x8 kf = lds_ld128(kaddr + t2 * KVT);
        ma = fmaxf(ma, tile_max(MFMA32(kf, qa, z)));
        mb = fmaxf(mb, tile_max(MFMA32(kf, qb2, z)));
      }
      ma = max_halves(ma); mb = max_halves(mb);
      oa = z; ob = z;
      for (int t2 = 0; t2 < 32; ++t2) {
        const bf16x8 kf = lds_ld128(kaddr + t2 * KVT), v0 = lds_ld128(vaddr0 + t2 * KVT), v1 = lds_ld128(vaddr1 + t2 * KVT);
        const f32x16 sa = MFMA32(kf, qa, z), sb = MFMA32(kf, qb2, z);
        u32x4 pa0, pa1, pb0, pb1;
#pragma unroll
        for (int j = 0; j < 4; ++j) {
          pa0[j] = PK(EX(sa[2 * j] - ma), EX(sa[2 * j + 1] - ma)); pa1[j] = PK(EX(sa[8 + 2 * j] - ma), EX(sa[9 + 2 * j] - ma));
          pb0[j] = PK(EX(sb[2 * j] - mb), EX(sb[2 * j + 1] - mb)); pb1[j] = PK(EX(sb[8 + 2 * j] - mb), EX(sb[9 + 2 * j] - mb));
        }
        oa = MFMA32(v0, BC8(pa0), oa); oa = MFMA32(v1, BC8(pa1), oa);
        ob = MFMA32(v0, BC8(pb0), ob); ob = MFMA32(v1, BC8(pb1), ob);
      }
    }
  }
  STAMP(7);
  WAIT_VM(0);
  const int row = tid >> 4, c8 = tid & 15;
  const int goff = ((tt * 32 + row) * D + cbk * 128 + c8 * 8) * 2;
  const u32x4 xv = *(const u32x4*)((const char*)x + goff);
  const u32x4 wrv = *(const u32x4*)((const char*)wr + (cbk * 128 + c8 * 8) * 2);
  const u32x4 wra = *(const u32x4*)((const char*)wr + lane * 16);
  {
    const unsigned xa = priv + lane * 16;
    if (kh == 0) {
      lds_stf4(xa, f32x4{ob[0], ob[1], ob[2], ob[3]}); lds_stf4(xa + 1024, f32x4{ob[4], ob[5], ob[6], ob[7]});
      lds_stf4(xa + 2048, f32x4{sum_halves(ob[8]), mb, 0.f, 0.f});
    } else {
      lds_stf4(xa, f32x4{oa[0], oa[1], oa[2], oa[3]}); lds_stf4(xa + 1024, f32x4{oa[4], oa[5], oa[6], oa[7]});
      lds_stf4(xa + 2048, f32x4{sum_halves(oa[8]), ma, 0.f, 0.f});
#pragma unroll
      for (int j = 0; j < 9; ++j) oa[j] = ob[j];
      ma = mb;
    }
    const float l = sum_halves(oa[8]);
    if (wid == 0) {
      for (unsigned spin = 0;; ++spin) {
        const u32x4 f0 = ho_ld(rs, WS_FR + lane * 16), f1 = ho_ld(rs, WS_FR + 1024 + lane * 16);
        const bool okf = f0[0] == tag && f0[1] == ~tag && f0[2] == tag && f0[3] == ~tag && f1[0] == tag && f1[1] == ~tag && f1[2] == tag && f1[3] == ~tag;
        if (__all(okf) || spin > SPIN_CAP) break;
        __builtin_amdgcn_s_sleep(1);
      }
    }
    BARRIER();
    const unsigned ya = lds0 + L_PRIV + (wid ^ 4) * PRIV + lane * 16;
    const f32x4 y0 = lds_ldf4(ya), y1 = lds_ldf4(ya + 1024), y2 = lds_ldf4(ya + 2048);
    const float M = fmaxf(ma, y2[1]);
    float f1 = EX(ma - M), f2 = EX(y2[1] - M);
    const float inv = 1.f / (l * f1 + y2[0] * f2);
    f1 *= inv; f2 *= inv;
    u32x4 po;
    po[0] = san(pack_bf16(oa[0] * f1 + y0[0] * f2, oa[1] * f1 + y0[1] * f2));
    po[1] = san(pack_bf16(oa[2] * f1 + y0[2] * f2, oa[3] * f1 + y0[3] * f2));
    po[2] = san(pack_bf16(oa[4] * f1 + y1[0] * f2, oa[5] * f1 + y1[1] * f2));
    po[3] = san(pack_bf16(oa[6] * f1 + y1[2] * f2, oa[7] * f1 + y1[3] * f2));
    ho_st(rs, HO_O + ((qb * 8 + qg * 2 + kh) * H + head) * 1024 + lane * 16, po);
  }
  STAMP(8);

  constexpr unsigned P_OFF = 32768, P_SZ = 16896, S_OFF = 69632, T_OFF = 71680, R_OFF = 73728, F_OFF = 81920;
  float swr = 0.f;
#pragma unroll
  for (int j = 0; j < 4; ++j) swr += bf_lo(wra[j]) + bf_hi(wra[j]);
  swr = sum_rows(row16_sum(swr));
  {
    u32x4 o0 = {SENT, SENT, SENT, SENT}, o1 = o0, o2 = o0, o3 = o0;
    const int oo = HO_O + (tt * H + wid * 4) * 1024 + lane * 16;
    for (unsigned spin = 0;; ++spin) {
      if (__any(pend(o0))) o0 = ho_ld(rs, oo);
      if (__any(pend(o1))) o1 = ho_ld(rs, oo + 1024);
      if (__any(pend(o2))) o2 = ho_ld(rs, oo + 2048);
      if (__any(pend(o3))) o3 = ho_ld(rs, oo + 3072);
      if (!__any(pend(o0) || pend(o1) || pend(o2) || pend(o3)) || spin > SPIN_CAP) break;
      __builtin_amdgcn_s_sleep(1);
    }
    const unsigned oa2 = lds0 + wid * 4096 + lane * 16;
    lds_stu4(oa2, o0); lds_stu4(oa2 + 1024, o1); lds_stu4(oa2 + 2048, o2); lds_stu4(oa2 + 3072, o3);
    if (wid > 0) for (int p = wid - 1; p < 43; p += 7) ff_piece(p, lds0 + F_OFF + p * 1024);
  }
  BARRIER();
  STAMP(9);
  {
    f32x16 acc = {};
    const unsigned ol = lds0 + kh * 16384 + lane * 16;
#pragma unroll
    for (int i = 0; i < 16; ++i) acc = MFMA32(cat8(wl_[i], wh_[i]), lds_ld128(ol + i * 1024), acc);
    const unsigned pa = lds0 + P_OFF + kh * P_SZ + r32 * 528 + (qg * 32 + 4 * hh) * 4;
#pragma unroll
    for (int gq = 0; gq < 4; ++gq) lds_stf4(pa + gq * 32, f32x4{acc[4 * gq], acc[4 * gq + 1], acc[4 * gq + 2], acc[4 * gq + 3]});
  }
  BARRIER();
  float h[8];
  u32x4 hv;
  {
    const unsigned pa = lds0 + P_OFF + row * 528 + c8 * 32;
    const f32x4 a0 = lds_ldf4(pa), a1 = lds_ldf4(pa + 16), b0 = lds_ldf4(pa + P_SZ), b1 = lds_ldf4(pa + P_SZ + 16);
    const unsigned t0 = pack_bf16(a0[0] + b0[0], a0[1] + b0[1]), t1 = pack_bf16(a0[2] + b0[2], a0[3] + b0[3]);
    const unsigned t2 = pack_bf16(a1[0] + b1[0], a1[1] + b1[1]), t3 = pack_bf16(a1[2] + b1[2], a1[3] + b1[3]);
    hv[0] = pack_bf16(bf_lo(xv[0]) + bf_lo(t0), bf_hi(xv[0]) + bf_hi(t0)); hv[1] = pack_bf16(bf_lo(xv[1]) + bf_lo(t1), bf_hi(xv[1]) + bf_hi(t1));
    hv[2] = pack_bf16(bf_lo(xv[2]) + bf_lo(t2), bf_hi(xv[2]) + bf_hi(t2)); hv[3] = pack_bf16(bf_lo(xv[3]) + bf_lo(t3), bf_hi(xv[3]) + bf_hi(t3));
#pragma unroll
    for (int j = 0; j < 4; ++j) { h[2 * j] = bf_lo(hv[j]); h[2 * j + 1] = bf_hi(hv[j]); }
    float s1 = 0.f, s2 = 0.f, s3 = 0.f;
#pragma unroll
    for (int j = 0; j < 4; ++j) {
      s1 += h[2 * j] + h[2 * j + 1];
      s2 = fmaf(h[2 * j], h[2 * j], s2); s2 = fmaf(h[2 * j + 1], h[2 * j + 1], s2);
      s3 = fmaf(h[2 * j], bf_lo(wrv[j]), s3); s3 = fmaf(h[2 * j + 1], bf_hi(wrv[j]), s3);
    }
    s1 = row16_sum(s1); s2 = row16_sum(s2); s3 = row16_sum(s3);
    if (c8 == 0) lds_stu4(lds0 + S_OFF + row * 16, u32x4{san(__float_as_uint(s1)), san(__float_as_uint(s2)), san(__float_as_uint(s3)), 0u});
  }
  BARRIER();
  STAMP(10);
  if (wid == 0) {
    const u32x4 mine = lds_ldu4(lds0 + S_OFF + r32 * 16);
    if (lane < 32) ho_st(rs, HO_ST + (tt * 4 + cbk) * 512 + lane * 16, mine);
    u32x4 sa = {SENT, SENT, SENT, SENT}, sb = sa;
    if (2 * hh == cbk) sa = mine;
    if (2 * hh + 1 == cbk) sb = mine;
    const int so = HO_ST + (tt * 4 + 2 * hh) * 512 + r32 * 16;
    for (unsigned spin = 0;; ++spin) {
      if (pend(sa)) sa = ho_ld(rs, so);
      if (pend(sb)) sb = ho_ld(rs, so + 512);
      if (!__any(pend(sa) || pend(sb)) || spin > SPIN_CAP) break;
      __builtin_amdgcn_s_sleep(1);
    }
    const float s1 = sum_halves(__uint_as_float(sa[0]) + __uint_as_float(sb[0]));
    const float s2 = sum_halves(__uint_as_float(sa[1]) + __uint_as_float(sb[1]));
    const float s3 = sum_halves(__uint_as_float(sa[2]) + __uint_as_float(sb[2]));
    float mbv, rden;
    ln_stats(s1, s2, mbv, rden);
    ho_st1(rs, HO_KEY + (2 * cbk + hh) * (S * 4) + (tt * 32 + r32) * 4, sort_key((s3 - mbv * swr) * rden, tt * 32 + r32));
    if (lane < 32) { lds_stf(lds0 + S_OFF + 512 + lane * 8, mbv); lds_stf(lds0 + S_OFF + 516 + lane * 8, rden); }
  }
  BARRIER();
  STAMP(11);
  u32x4 lv, ov;
  {
    const float mbv = lds_ldf(lds0 + S_OFF + 512 + row * 8), rden = lds_ldf(lds0 + S_OFF + 516 + row * 8);
#pragma unroll
    for (int j = 0; j < 4; ++j) {
      const unsigned d = pack_bf16(h[2 * j] - mbv, h[2 * j + 1] - mbv);
      lv[j] = pack_bf16(bf_lo(d) * rden, bf_hi(d) * rden);
      ov[j] = pack_bf16(h[2 * j] + bf_lo(lv[j]), h[2 * j + 1] + bf_hi(lv[j]));
    }
    ho_st(rs, HO_LN + goff, u32x4{san(lv[0]), san(lv[1]), san(lv[2]), san(lv[3])});
  }
  {
    u32x4 kv = {SENT, SENT, SENT, SENT};
    for (unsigned spin = 0;; ++spin) {
      if (pend(kv)) kv = ho_ld(rs, HO_KEY + xcd * (S * 4) + tid * 16);
      if (!__any(pend(kv)) || spin > SPIN_CAP) break;
      __builtin_amdgcn_s_sleep(1);
    }
    STAMP(12);
    unsigned k0 = kv[0], k1 = kv[1], k2 = kv[2], k3 = kv[3], t;
    t = max(k0, k1); k1 = min(k0, k1); k0 = t;  t = max(k2, k3); k3 = min(k2, k3); k2 = t;
    t = max(k0, k2); k2 = min(k0, k2); k0 = t;  t = max(k1, k3); k3 = min(k1, k3); k1 = t;
    t = max(k1, k2); k2 = min(k1, k2); k1 = t;
    unsigned mine = 0;
#pragma unroll
    for (int c = 0; c < 8; ++c) {
      const unsigned m = wave_max_u(k0);
      if (lane == c) mine = m;
      if (k0 == m) { k0 = k1; k1 = k2; k2 = k3; k3 = 0; }
    }
    if (lane < 8) lds_stu(lds0 + T_OFF + (wid * 8 + lane) * 4, mine);
    WAIT_VM(0);
    BARRIER();
    const unsigned cand = lds_ldu(lds0 + T_OFF + (tid >> 3) * 4);
    const u32x4 o0 = lds_ldu4(lds0 + T_OFF + (tid & 7) * 32), o1 = lds_ldu4(lds0 + T_OFF + (tid & 7) * 32 + 16);
    unsigned rank = 0;
#pragma unroll
    for (int j = 0; j < 4; ++j) { rank += (o0[j] > cand) ? 1u : 0u; rank += (o1[j] > cand) ? 1u : 0u; }
    rank += dpp_u<DPP_X1>(rank); rank += dpp_u<DPP_X2>(rank); rank += dpp_u<DPP_HMIR>(rank);
    if ((tid & 7) == 0 && rank < 8) lds_stu(lds0 + T_OFF + 256 + rank * 4, cand);
    BARRIER();
  }
  STAMP(13);
  const unsigned mykey = lds_ldu(lds0 + T_OFF + 256 + (lane & 7) * 4);
  const int myrow = 2047 - (int)(mykey & 0x7ffu);
  if (isd1) {
    u32x4 a0, a1;
    const unsigned fl = lds0 + F_OFF + wid * 2048 + (8 * g + (i16 >> 2)) * 32 + (i16 & 3) * 8;
    const bf16x8 f0 = cat8(tr_read(fl), tr_read(fl + 128)), f1 = cat8(tr_read(fl + 1024), tr_read(fl + 1024 + 128));
    const bf16x8 f2 = cat8(tr_read(fl + 16384), tr_read(fl + 16384 + 128)), f3 = cat8(tr_read(fl + 17408), tr_read(fl + 17408 + 128));
    const int ao = HO_LN + (myrow * D + wid * 64 + g * 8) * 2;
    for (unsigned spin = 0;; ++spin) {
      a0 = ho_ld(rs, ao); a1 = ho_ld(rs, ao + 64);
      if (!__any(pend(a0) || pend(a1)) || spin > SPIN_CAP) break;
      __builtin_amdgcn_s_sleep(1);
    }
    f32x4 ag = {}, au = {};
    ag = MFMA16(BC8(a0), f0, ag); au = MFMA16(BC8(a0), f2, au);
    ag = MFMA16(BC8(a1), f1, ag); au = MFMA16(BC8(a1), f3, au);
    if (lane < 32) { lds_stf4(lds0 + R_OFF + wid * 1024 + lane * 16, ag); lds_stf4(lds0 + R_OFF + wid * 1024 + 512 + lane * 16, au); }
    BARRIER();
    if (wid == 0) {
      const int tok = lane >> 3, cp = lane & 7;
      const unsigned ra = lds0 + R_OFF + (((tok >> 2) * 16 + cp * 2) * 4 + (tok & 3)) * 4;
      float g0 = 0.f, g1 = 0.f, u0 = 0.f, u1 = 0.f;
#pragma unroll
      for (int w2 = 0; w2 < 8; ++w2) {
        g0 += lds_ldf(ra + w2 * 1024); g1 += lds_ldf(ra + w2 * 1024 + 16); u0 += lds_ldf(ra + w2 * 1024 + 512); u1 += lds_ldf(ra + w2 * 1024 + 528);
      }
      g0 = rbf(g0); g1 = rbf(g1); u0 = rbf(u0); u1 = rbf(u1);
      const float s0 = rbf(g0 / (1.f + __expf(-g0))), s1 = rbf(g1 / (1.f + __expf(-g1)));
      ho_st1(rs, HO_ACT + d1 * 256 + lane * 4, san(pack_bf16(s0 * u0, s1 * u1)));
    }
    STAMP(14);
  }
  {
    const int grow = tt * 32 + row;
    int sel = -1;
#pragma unroll
    for (int c = 0; c < 8; ++c) if (__builtin_amdgcn_readlane(myrow, c) == grow) sel = c;
    if (sel < 0) *(u32x4*)((char*)out + goff) = ov;
    else ho_st(rs, HO_HS + (sel * D + cbk * 128 + c8 * 8) * 2, u32x4{san(hv[0]), san(hv[1]), san(hv[2]), san(hv[3])});
    if (b == 0 && tid == 0) __builtin_amdgcn_raw_buffer_store_b32(tag, rs, WS_EP, 0, 16);
  }
  if (isd2) {
    const int n0 = sub * 16;
    unsigned hw = SENT, lw = SENT, fkey = 0;
    const int tok = lane >> 3, cp = lane & 7;
    if (wid == 0) {
      fkey = lds_ldu(lds0 + T_OFF + 256 + tok * 4);
      const int frow = 2047 - (int)(fkey & 0x7ffu);
      for (unsigned spin = 0;; ++spin) {
        if (hw == SENT) hw = __builtin_amdgcn_raw_buffer_load_b32(rs, HO_HS + (tok * D + n0 + cp * 2) * 2, 0, 16);
        if (lw == SENT) lw = __builtin_amdgcn_raw_buffer_load_b32(rs, HO_LN + (frow * D + n0 + cp * 2) * 2, 0, 16);
        if (!__any(hw == SENT || lw == SENT) || spin > SPIN_CAP) break;
        __builtin_amdgcn_s_sleep(1);
      }
    }
    bf16x8 ff[6];
    const unsigned fl = lds0 + F_OFF + wid * 1024 + (8 * g + (i16 >> 2)) * 32 + (i16 & 3) * 8;
#pragma unroll
    for (int j = 0; j < 6; ++j) ff[j] = cat8(tr_read(fl + j * 8192), tr_read(fl + j * 8192 + 128));
    u32x4 av[6];
#pragma unroll
    for (int j = 0; j < 6; ++j) av[j] = (wid + 8 * j < 43) ? u32x4{SENT, SENT, SENT, SENT} : u32x4{0u, 0u, 0u, 0u};
    const int ao = HO_ACT + ((wid * 2 + (g >> 1)) * 8 + (lane & 7)) * 32 + (g & 1) * 16;
    for (unsigned spin = 0;; ++spin) {
      bool p = false;
#pragma unroll
      for (int j = 0; j < 6; ++j) if (__any(pend(av[j]))) av[j] = ho_ld(rs, ao + j * 4096);
#pragma unroll
      for (int j = 0; j < 6; ++j) p = p || pend(av[j]);
      if (!__any(p) || spin > SPIN_CAP) break;
      __builtin_amdgcn_s_sleep(1);
    }
    STAMP(14);
    f32x4 y = {};
#pragma unroll
    for (int j = 0; j < 6; ++j) if (wid + 8 * j < 43) y = MFMA16(BC8(av[j]), ff[j], y);
    if (lane < 32) lds_stf4(lds0 + R_OFF + wid * 512 + lane * 16, y);
    BARRIER();
    if (wid == 0) {
      const unsigned ra = lds0 + R_OFF + (((tok >> 2) * 16 + cp * 2) * 4 + (tok & 3)) * 4;
      float y0 = 0.f, y1 = 0.f;
#pragma unroll
      for (int w2 = 0; w2 < 8; ++w2) { y0 += lds_ldf(ra + w2 * 512); y1 += lds_ldf(ra + w2 * 512 + 16); }
      const float sig = rbf(1.f / (1.f + __expf(-key_score(fkey))));
      y0 = rbf(rbf(y0) * sig); y1 = rbf(rbf(y1) * sig);
      *(unsigned*)((char*)out + ((size_t)(2047 - (int)(fkey & 0x7ffu)) * D + n0 + cp * 2) * 2) =
          pack_bf16(bf_lo(hw) + rbf(bf_lo(lw) + y0), bf_hi(hw) + rbf(bf_hi(lw) + y1));
    }
  }
  STAMP(15); STAMP_OUT(0);
}

extern "C" void kernel_launch(void* const* d_in, const int* in_sizes, int n_in, void* d_out, int out_size, void* d_ws, size_t ws_size,
                              hipStream_t stream) {
  (void)hipFuncSetAttribute((const void*)k_fused, hipFuncAttributeMaxDynamicSharedMemorySize, LDS_BYTES);
  k_fused<<<256, 512, LDS_BYTES, stream>>>((const uint16_t*)d_in[0], (const uint16_t*)d_in[1], (const uint16_t*)d_in[2], (const uint16_t*)d_in[4],
                                           (const uint16_t*)d_in[5], (const uint16_t*)d_in[6], (const uint16_t*)d_in[7], (char*)d_ws, (uint16_t*)d_out);
}
```

```cpp
#include <hip/hip_runtime.h>
#include <hip/hip_bf16.h>
#include <stdint.h>

typedef short bf16x8 __attribute__((ext_vector_type(8)));
typedef short bf16x4 __attribute__((ext_vector_type(4)));
typedef float f32x16 __attribute__((ext_vector_type(16)));
typedef float f32x4 __attribute__((ext_vector_type(4)));
typedef float f32x2 __attribute__((ext_vector_type(2)));
typedef __bf16 bf16x2_t __attribute__((ext_vector_type(2)));
typedef unsigned u32x4 __attribute__((ext_vector_type(4)));
typedef unsigned u32x2 __attribute__((ext_vector_type(2)));
#define LAS __attribute__((address_space(3)))
#define GAS __attribute__((address_space(1)))
#define DI __device__ __forceinline__
#define MFMA32(a, b, c) __builtin_amdgcn_mfma_f32_32x32x16_bf16((a), (b), (c), 0, 0, 0)
#define MFMA16(a, b, c) __builtin_amdgcn_mfma_f32_16x16x32_bf16((a), (b), (c), 0, 0, 0)
#define MFMA4(a, b, c) __builtin_amdgcn_mfma_f32_4x4x4bf16_1k((a), (b), (c), 0, 0, 0)
#define SBAR() __builtin_amdgcn_sched_barrier(0)
#define PIN(x) asm volatile("" : "+v"(x))
#define BC8(x) __builtin_bit_cast(bf16x8, x)

constexpr int S = 2048, D = 512, H = 32, F = 1376;
constexpr float LN_EPS = 1e-5f;
constexpr float QSCALE = 0.25f * 1.4426950408889634f;
constexpr unsigned SENT = 0xFFFFFFFFu;
constexpr unsigned SPIN_CAP = 1u << 16;

constexpr int WS_Q = 0;
constexpr int WS_KV = WS_Q + H * 64 * 1024;
constexpr int WS_FA = WS_KV + H * 64 * 2048;
constexpr int WS_FR = WS_FA + 256 * 128;
constexpr int WS_EP = WS_FR + 4096;
constexpr int WS_HO = WS_EP + 4096;
constexpr int HO_O = WS_HO;
constexpr int HO_LN = HO_O + 64 * H * 1024;
constexpr int HO_ST = HO_LN + S * D * 2;
constexpr int HO_KEY = HO_ST + 64 * 16 * 512;
constexpr int HO_ACT = HO_KEY + 8 * S * 4;
constexpr int HO_HS = HO_ACT + 24576;
constexpr int HO_END = HO_HS + 8192;
constexpr int HO_PER_WG = 19 * 1024;
static_assert(WS_HO + 256 * HO_PER_WG >= HO_END, "reset covers the hand-off words");
constexpr int WS_BYTES = WS_HO + 256 * HO_PER_WG;

#define STAMP_DECL do {} while (0)
#define STAMP(i) do {} while (0)
#define STAMP_OUT(k) do {} while (0)

#define FSTAMP(i) do {} while (0)

DI unsigned pack_bf16(float lo, float hi) {
  f32x2 v = {lo, hi};
  bf16x2_t r = __builtin_convertvector(v, bf16x2_t);
  return __builtin_bit_cast(unsigned, r);
}
DI float bf_lo(unsigned v) { return __uint_as_float(v << 16); }
DI float bf_hi(unsigned v) { return __uint_as_float(v & 0xffff0000u); }
DI float rbf(float x) { return bf_lo(pack_bf16(x, x)); }
DI float sum_halves(float v) {
  auto r = __builtin_amdgcn_permlane32_swap(__float_as_uint(v), __float_as_uint(v), false, false);
  return __uint_as_float(r[0]) + __uint_as_float(r[1]);
}
DI float sum_rows(float v) {
  auto r = __builtin_amdgcn_permlane16_swap(__float_as_uint(v), __float_as_uint(v), false, false);
  return sum_halves(__uint_as_float(r[0]) + __uint_as_float(r[1]));
}
DI float max_halves(float v) {
  auto r = __builtin_amdgcn_permlane32_swap(__float_as_uint(v), __float_as_uint(v), false, false);
  return fmaxf(__uint_as_float(r[0]), __uint_as_float(r[1]));
}
template <int CTRL> DI float dpp_f(float v) { return __uint_as_float(__builtin_amdgcn_update_dpp(0u, __float_as_uint(v), CTRL, 0xf, 0xf, true)); }
template <int CTRL> DI unsigned dpp_u(unsigned v) { return __builtin_amdgcn_update_dpp(0u, v, CTRL, 0xf, 0xf, true); }
constexpr int DPP_X1 = 0xB1, DPP_X2 = 0x4E, DPP_HMIR = 0x141, DPP_MIR = 0x140;
DI float quad_sum(float v) { v += dpp_f<DPP_X1>(v); v += dpp_f<DPP_X2>(v); return v; }
DI float row16_sum(float v) { v = quad_sum(v); v += dpp_f<DPP_HMIR>(v); v += dpp_f<DPP_MIR>(v); return v; }
DI unsigned wave_max_u(unsigned v) {
  v = max(v, dpp_u<DPP_X1>(v)); v = max(v, dpp_u<DPP_X2>(v)); v = max(v, dpp_u<DPP_HMIR>(v)); v = max(v, dpp_u<DPP_MIR>(v));
  v = max(v, (unsigned)__builtin_amdgcn_update_dpp(0u, v, 0x142, 0xa, 0xf, false));
  v = max(v, (unsigned)__builtin_amdgcn_update_dpp(0u, v, 0x143, 0xc, 0xf, false));
  return (unsigned)__builtin_amdgcn_readlane((int)v, 63);
}
DI void glds16(const void* sbase, unsigned voff, unsigned lds_base) {
  unsigned sv;
  asm volatile("s_mov_b32 %0, m0\n\ts_mov_b32 m0, %3\n\ts_nop 0\n\tglobal_load_lds_dwordx4 %1, %2\n\ts_mov_b32 m0, %0"
               : "=&s"(sv) : "v"(voff), "s"(sbase), "s"(lds_base) : "memory");
}
DI void glds16s(const void* sbase, unsigned voff, unsigned lds_base) {
  unsigned sv;
  asm volatile("s_mov_b32 %0, m0\n\ts_mov_b32 m0, %3\n\ts_nop 0\n\tglobal_load_lds_dwordx4 %1, %2 sc1\n\ts_mov_b32 m0, %0"
               : "=&s"(sv) : "v"(voff), "s"(sbase), "s"(lds_base) : "memory");
}
#define WAIT_VM(N) asm volatile("s_waitcnt vmcnt(" #N ")" ::: "memory")
#define WAIT_LGKM0() asm volatile("s_waitcnt lgkmcnt(0)" ::: "memory")
#define BARRIER() asm volatile("s_waitcnt lgkmcnt(0)\n\ts_barrier" ::: "memory")
typedef short v4i16_t __attribute__((ext_vector_type(4)));
DI bf16x4 tr_read(unsigned addr) {
  return __builtin_bit_cast(bf16x4, __builtin_amdgcn_ds_read_tr16_b64_v4i16((LAS v4i16_t*)(uintptr_t)addr));
}
DI bf16x8 lds_ld128(unsigned addr) { return *(const LAS bf16x8*)(uintptr_t)addr; }
DI f32x4 lds_ldf4(unsigned addr) { return *(const LAS f32x4*)(uintptr_t)addr; }
DI u32x4 lds_ldu4(unsigned addr) { return *(const LAS u32x4*)(uintptr_t)addr; }
DI void lds_stf4(unsigned addr, f32x4 v) { *(LAS f32x4*)(uintptr_t)addr = v; }
DI void lds_stu4(unsigned addr, u32x4 v) { *(LAS u32x4*)(uintptr_t)addr = v; }
DI void lds_stf(unsigned addr, float v) { *(LAS float*)(uintptr_t)addr = v; }
DI void lds_stu(unsigned addr, unsigned v) { *(LAS unsigned*)(uintptr_t)addr = v; }
DI float lds_ldf(unsigned addr) { return *(const LAS float*)(uintptr_t)addr; }
DI unsigned lds_ldu(unsigned addr) { return *(const LAS unsigned*)(uintptr_t)addr; }
DI bf16x8 cat8(bf16x4 a, bf16x4 b) { return __builtin_shufflevector(a, b, 0, 1, 2, 3, 4, 5, 6, 7); }
DI bf16x4 lo4(bf16x8 a) { return __builtin_shufflevector(a, a, 0, 1, 2, 3); }
DI bf16x4 hi4(bf16x8 a) { return __builtin_shufflevector(a, a, 4, 5, 6, 7); }
typedef __amdgpu_buffer_rsrc_t rsrc_t;
DI u32x4 ho_ld(rsrc_t rs, int off) { return __builtin_amdgcn_raw_buffer_load_b128(rs, off, 0, 16); }
DI void ho_st(rsrc_t rs, int off, u32x4 v) { __builtin_amdgcn_raw_buffer_store_b128(v, rs, off, 0, 16); }
DI void ho_st1(rsrc_t rs, int off, unsigned v) { __builtin_amdgcn_raw_buffer_store_b32(v, rs, off, 0, 16); }
DI unsigned san(unsigned v) { return min(v, SENT - 1u); }
DI bool pend(u32x4 v) { return max(max(v[0], v[1]), max(v[2], v[3])) == SENT; }

constexpr int LDS_BYTES = 163840;
constexpr int KVT = 2080;
constexpr int L_PRIV = 64 * KVT, PRIV = 2560;
constexpr int L_STG = L_PRIV + 8 * PRIV;
constexpr int L_RDY = L_STG + 4 * 2048;
constexpr int L_RS = L_RDY + 64, L_CS = L_RS + 1024;
static_assert(L_CS + 768 <= LDS_BYTES, "lds map");
constexpr int N_D1 = 86;
constexpr int CODE_PF_BYTES = 27 * 1024;
struct AFrag { bf16x8 x0, x1, q, k, v; };

#define EX(x) __builtin_amdgcn_exp2f(x)
#define PK(a, b) pack_bf16(a, b)
#define RCP(x) __builtin_amdgcn_rcpf(x)
#define UNIT(SC, SN, KN, QN, NEGN, OPREV, VPREV, OCUR, VCUR0, LOADS) do { \
    float e0 = EX(SC[0]), e1 = EX(SC[1]); SBAR(); \
    OPREV = MFMA32(VPREV, BC8(p1), OPREV); \
    LOADS; \
    SBAR(); \
    float e2 = EX(SC[2]), e3 = EX(SC[3]); SBAR(); \
    p0[0] = PK(e0, e1); SBAR(); float e4 = EX(SC[4]), e5 = EX(SC[5]); SBAR(); \
    p0[1] = PK(e2, e3); SBAR(); float e6 = EX(SC[6]), e7 = EX(SC[7]); SBAR(); \
    SN = MFMA32(KN, QN, NEGN); SBAR(); \
    p0[2] = PK(e4, e5); SBAR(); e0 = EX(SC[8]); e1 = EX(SC[9]); SBAR(); \
    p0[3] = PK(e6, e7); SBAR(); e2 = EX(SC[10]); e3 = EX(SC[11]); SBAR(); \
    OCUR = MFMA32(VCUR0, BC8(p0), OCUR); SBAR(); \
    p1[0] = PK(e0, e1); SBAR(); e4 = EX(SC[12]); e5 = EX(SC[13]); SBAR(); \
    p1[1] = PK(e2, e3); SBAR(); e6 = EX(SC[14]); e7 = EX(SC[15]); SBAR(); \
    p1[2] = PK(e4, e5); SBAR(); \
    p1[3] = PK(e6, e7); SBAR(); \
  } while (0)
#define TWO_TILES(U) do { \
    UNIT(sA, sB, k0, qb2, ng, ob, v11, oa, v00, k1 = lds_ld128(ka + (U + 1) * KVT); v10 = lds_ld128(va0 + (U + 1) * KVT)); \
    UNIT(sB, sA, k1, qa, ng, oa, v01, ob, v00, v11 = lds_ld128(va1 + (U + 1) * KVT)); \
    UNIT(sA, sB, k1, qb2, ng, ob, v01, oa, v10, k0 = lds_ld128(ka + (U + 2) * KVT); v00 = lds_ld128(va0 + (U + 2) * KVT)); \
    UNIT(sB, sA, k0, qa, ng, oa, v11, ob, v10, v01 = lds_ld128(va1 + (U + 2) * KVT)); \
  } while (0)

DI int fresh_lane() {
  int l;
  asm volatile("v_mbcnt_lo_u32_b32 %0, -1, 0\n\tv_mbcnt_hi_u32_b32 %0, -1, %0" : "=v"(l));
  return l;
}
#define RELANE() const int lane = fresh_lane(), tid = wid * 64 + lane, r32 = lane & 31, hh = lane >> 5, g = lane >> 4, i16 = lane & 15; \
  (void)tid; (void)r32; (void)hh; (void)g; (void)i16
DI float tile_max(const f32x16& s) {
  float m = fmaxf(fmaxf(s[0], s[1]), s[2]);
#pragma unroll
  for (int j = 3; j < 15; j += 2) m = fmaxf(fmaxf(m, s[j]), s[j + 1]);
  return fmaxf(m, s[15]);
}
DI unsigned sort_key(float r, int idx) {
  unsigned u = pack_bf16(r, r) & 0xffffu;
  if ((u & 0x7fffu) > 0x7f80u) u = 0xffffu;
  u = (u & 0x8000u) ? (~u & 0xffffu) : (u | 0x8000u);
  return (u << 16) | (unsigned)(2047 - idx);
}
DI float key_score(unsigned key) {
  unsigned u = key >> 16;
  u = (u & 0x8000u) ? (u & 0x7fffu) : (~u & 0xffffu);
  return __uint_as_float(u << 16);
}
DI void ln_stats(float s1, float s2, float& mb, float& rden) {
  const float mean = s1 * (1.f / D);
  const float var = fmaxf(s2 * (1.f / D) - mean * mean, 0.f);
  mb = rbf(mean);
  rden = RCP(rbf(__builtin_amdgcn_sqrtf(rbf(rbf(var) + LN_EPS))));
}
DI float sigmoid(float v) { return RCP(1.f + EX(v * -1.4426950408889634f)); }
DI void wait_rdy(unsigned addr, unsigned need) {
  for (unsigned spin = 0;; ++spin) {
    const u32x4 r = *(volatile const LAS u32x4*)(uintptr_t)addr;
    const unsigned m = min(min(r[0], r[1]), min(r[2], r[3]));
    if (__builtin_amdgcn_readfirstlane(m) >= need || spin > (SPIN_CAP << 4)) break;
    __builtin_amdgcn_s_sleep(1);
  }
  asm volatile("" ::: "memory");
}

__global__ __launch_bounds__(768, 1) void k_fused(const uint16_t* __restrict__ x, const uint16_t* __restrict__ wqkv, const uint16_t* __restrict__ wo,
                                                   const uint16_t* __restrict__ wr, const uint16_t* __restrict__ wg, const uint16_t* __restrict__ wu,
                                                   const uint16_t* __restrict__ wd, char* __restrict__ ws, uint16_t* __restrict__ out) {
  extern __shared__ __attribute__((aligned(16))) char smem[];
  constexpr unsigned lds0 = 0;
  asm volatile("" :: "s"((unsigned)(uintptr_t)smem));
  const int tid = threadIdx.x, lane = tid & 63, wid = __builtin_amdgcn_readfirstlane(tid >> 6);
  const int b = blockIdx.x, xcd = b & 7, sub = b >> 3;
  const int r32 = lane & 31, hh = lane >> 5, g = lane >> 4, i16 = lane & 15;
  const rsrc_t rs = __builtin_amdgcn_make_buffer_rsrc(ws, 0, WS_BYTES, 0x00020000);
  const int rb = (xcd >> 1) * 8 + (sub & 7), hq = (xcd & 1) * 4 + (sub >> 3);
  const int head = xcd * 4 + (sub & 3), qb = sub >> 2;
  const int tt = xcd * 8 + (sub >> 2), cbk = sub & 3;
  const int d1 = (xcd < 7 && sub < 13) ? xcd * 13 + sub : 999;
  const bool isd1 = d1 < N_D1, isd2 = xcd == 7;
  constexpr unsigned X_OFF = 32768, S_OFF = 53248, T_OFF = 55296, R_OFF = 57344, F_OFF = 81920;
  STAMP_DECL; STAMP(0);
  unsigned code_pf = 0;
  if (wid == 7) {
    const unsigned long long pc = __builtin_amdgcn_s_getpc() & ~0xFFull;
    const unsigned off = min((unsigned)((sub & 3) * 8192 + lane * 128), (unsigned)(CODE_PF_BYTES - 128));
    asm volatile("global_load_dword %0, %1, %2" : "=v"(code_pf) : "v"(off), "s"(pc) : "memory");
  }

  if (__builtin_expect(wid >= 8, 1)) {
    const int hw = wid - 8;
    __builtin_amdgcn_s_setprio(3);
    {
      const int rot = ((sub & 7) + 2 * (sub >> 3)) & 7;
      const char* base; unsigned vo0, vo1, cstr, pstr;
      if (hw < 3) {
        base = (const char*)wqkv + hw * 1024 + hq * 128; cstr = 64 * 3072; pstr = 8 * 3072;
        vo0 = (lane >> 3) * 3072 + (((lane & 7) ^ ((g & 1) << 1)) * 16); vo1 = (lane >> 3) * 3072 + (((lane & 7) ^ (((g & 1) | 2) << 1)) * 16);
      } else {
        base = (const char*)x + (size_t)rb * 65536; cstr = 128; pstr = 8192;
        vo0 = (lane >> 3) * 1024 + (((lane & 7) ^ g) * 16); vo1 = (lane >> 3) * 1024 + (((lane & 7) ^ (4 + g)) * 16);
      }
      auto issue = [&](int c) {
        const char* src = base + (size_t)((c + rot) & 7) * cstr;
        const unsigned dst = lds0 + (c & 3) * 32768 + hw * 8192;
#pragma unroll
        for (int p = 0; p < 8; ++p) glds16(src + (size_t)p * pstr, (p & 1) ? vo1 : vo0, dst + p * 1024);
      };
      { const char* bb = base; asm volatile("" : "+s"(bb)); STAMP(1); }
      issue(0);
      STAMP(2);
      issue(1);
      WAIT_VM(8);
      STAMP(3);
      BARRIER();
      issue(2);
#pragma unroll 1
      for (int c = 0; c < 7; ++c) {
        if (c <= 5) { WAIT_VM(8); } else { WAIT_VM(0); }
        BARRIER();
        if (c + 3 < 8) issue(c + 3);
      }
    }
    unsigned tag = __builtin_amdgcn_raw_buffer_load_b32(rs, WS_EP, 0, 16);
    tag = __builtin_amdgcn_readfirstlane(tag) + 1u;
    BARRIER();
    BARRIER();
    STAMP(4);
    if (lane < 32) {
      const unsigned o = (lane & 1) ? 0u : 0x3f803f80u;
      lds_stu4(lds0 + (hw * 16 + (lane >> 1)) * KVT + 2048 + (lane & 1) * 16, u32x4{o, o, o, o});
    }
    for (unsigned spin = 0;; ++spin) {
      const u32x2 f = __builtin_amdgcn_raw_buffer_load_b64(rs, WS_FA + (xcd * 32 + (lane & 7) * 4 + hw) * 128, 0, 16);
      if (__all(f[0] == tag && f[1] == ~tag) || spin > SPIN_CAP) break;
      __builtin_amdgcn_s_sleep(1);
    }
    STAMP(5);
    {
      const char* kvg = ws + WS_KV + (size_t)head * (64 * 2048) + hw * 4096;
      const unsigned kvl = lds0 + hw * KVT;
      const char* qsrc = ws + WS_Q + (size_t)(head * 64 + qb * 8 + hw * 2) * 1024;
      auto kv_step = [&](int st) {
#pragma unroll
        for (int hf = 0; hf < 2; ++hf)
#pragma unroll
          for (int p = 0; p < 2; ++p)
            glds16s(kvg + (st >> 1) * 16384 + (st & 1) * 2048 + hf * 65536 + p * 1024, lane * 16, kvl + (st * 4 + hf * 32) * KVT + p * 1024);
      };
      glds16s(qsrc, lane * 16, lds0 + L_STG + hw * 2048); glds16s(qsrc + 1024, lane * 16, lds0 + L_STG + hw * 2048 + 1024);
      kv_step(0); kv_step(1);
      asm volatile("s_waitcnt vmcnt(4) lgkmcnt(0)" ::: "memory");
      if (lane == 0) lds_stu(lds0 + L_RDY + hw * 4, 1u);
      kv_step(2); kv_step(3);
      WAIT_VM(8);
      if (lane == 0) lds_stu(lds0 + L_RDY + hw * 4, 2u);
      kv_step(4); kv_step(5);
      WAIT_VM(8);
      if (lane == 0) lds_stu(lds0 + L_RDY + hw * 4, 4u);
      kv_step(6); kv_step(7);
      WAIT_VM(0);
      if (lane == 0) lds_stu(lds0 + L_RDY + hw * 4, 8u);
    }
    STAMP(6);
    {
      const u32x4 sv = {SENT, SENT, SENT, SENT};
      if (hw == 0) {
#pragma unroll 1
        for (int j = 0; j < 19; ++j) __builtin_amdgcn_raw_buffer_store_b128(sv, rs, lane * 16, WS_HO + b * HO_PER_WG + j * 1024, 16);
        WAIT_VM(0);
        if (lane == 0) __builtin_amdgcn_raw_buffer_store_b64(u32x2{tag, ~tag}, rs, WS_FR + b * 8, 0, 16);
      }
    }
    bf16x8 wf[32];
    {
      const char* wsrc = (const char*)wo + (lane >> 2) * 1024 + cbk * 256 + hw * 64 + (lane & 3) * 16;
#pragma unroll
      for (int i = 0; i < 32; ++i) wf[i] = *(const bf16x8*)(wsrc + i * 16384);
      const unsigned stg = lds0 + L_STG + hw * 2048;
      const unsigned wor = stg + (4 * (g >> 1) + (i16 >> 2)) * 64 + (g & 1) * 32 + (i16 & 3) * 8;
      for (unsigned spin = 0;; ++spin) {
        const unsigned r = *(volatile const LAS unsigned*)(uintptr_t)(lds0 + L_RDY + 16 + hw * 4);
        if (__builtin_amdgcn_readfirstlane(r) >= 2u || spin > (SPIN_CAP << 4)) break;
        __builtin_amdgcn_s_sleep(4);
      }
      asm volatile("" ::: "memory");
#pragma unroll
      for (int i = 0; i < 32; ++i) {
        *(LAS bf16x8*)(uintptr_t)(stg + (i & 1) * 1024 + lane * 16) = wf[i];
        WAIT_LGKM0();
        wf[i] = cat8(tr_read(wor + (i & 1) * 1024), tr_read(wor + (i & 1) * 1024 + 512));
      }
    }
    STAMP(7);
    if (hw == 0) {
      for (unsigned spin = 0;; ++spin) {
        const u32x4 f0 = ho_ld(rs, WS_FR + lane * 16), f1 = ho_ld(rs, WS_FR + 1024 + lane * 16);
        const bool okf = f0[0] == tag && f0[1] == ~tag && f0[2] == tag && f0[3] == ~tag && f1[0] == tag && f1[1] == ~tag && f1[2] == tag && f1[3] == ~tag;
        if (__all(okf) || spin > SPIN_CAP) break;
        __builtin_amdgcn_s_sleep(8);
      }
    }
    WAIT_VM(0);
    STAMP(8);
    BARRIER();
    BARRIER();
    STAMP(9);
    {
      RELANE();
      f32x16 acc = {};
      const unsigned ol = lds0 + lane * 16;
      bf16x8 of[3];
#pragma unroll
      for (int i = 0; i < 3; ++i) of[i] = lds_ld128(ol + i * 1024);
#pragma unroll
      for (int i = 0; i < 32; ++i) {
        acc = MFMA32(wf[i], of[i % 3], acc);
        if (i + 3 < 32) of[i % 3] = lds_ld128(ol + (i + 3) * 1024);
        SBAR();
      }
      const unsigned xa = lds0 + X_OFF + r32 * 272 + (hw * 32 + 4 * hh) * 2, wa = lds0 + X_OFF + 32 * 272 + (hw * 32 + 4 * hh) * 2;
      float s1 = 0.f, s2 = 0.f, s3 = 0.f;
      u32x2 hp[4];
#pragma unroll
      for (int gq = 0; gq < 4; ++gq) {
        const u32x2 xw = *(const LAS u32x2*)(uintptr_t)(xa + gq * 16), ww = *(const LAS u32x2*)(uintptr_t)(wa + gq * 16);
#pragma unroll
        for (int e = 0; e < 2; ++e) {
          const unsigned t = pack_bf16(acc[4 * gq + 2 * e], acc[4 * gq + 2 * e + 1]);
          const unsigned hw2 = pack_bf16(bf_lo(xw[e]) + bf_lo(t), bf_hi(xw[e]) + bf_hi(t));
          hp[gq][e] = hw2;
          const float h0 = bf_lo(hw2), h1 = bf_hi(hw2);
          s1 += h0 + h1;
          s2 = fmaf(h0, h0, s2); s2 = fmaf(h1, h1, s2);
          s3 = fmaf(h0, bf_lo(ww[e]), s3); s3 = fmaf(h1, bf_hi(ww[e]), s3);
        }
      }
      s1 = sum_halves(s1); s2 = sum_halves(s2); s3 = sum_halves(s3);
      if (lane < 32) ho_st(rs, HO_ST + (tt * 16 + cbk * 4 + hw) * 512 + lane * 16, u32x4{san(__float_as_uint(s1)), san(__float_as_uint(s2)), san(__float_as_uint(s3)), 0u});
#pragma unroll
      for (int gq = 0; gq < 4; ++gq) *(LAS u32x2*)(uintptr_t)(xa + gq * 16) = hp[gq];
    }
    BARRIER();
    STAMP(15); STAMP_OUT(0);
    return;
  }

  unsigned tag;
  {
    const int rt = wid & 1, hd = wid >> 1, sw = hd >> 1, par = rt ^ (hd & 1);
    tag = __builtin_amdgcn_raw_buffer_load_b32(rs, WS_EP, 0, 16);
    const int xr0 = rt * 32 + sw * 16 + i16, xr1 = xr0 ^ 16;
    const int xf = (i16 >> 1) & 7;
    const unsigned xb0 = lds0 + 24576 + (xr0 >> 3) * 1024 + (xr0 & 7) * 128, xb1 = lds0 + 24576 + (xr1 >> 3) * 1024 + (xr1 & 7) * 128;
    const unsigned xoA = ((4 * par + g) ^ xf) * 16, xoB = ((4 * (par ^ 1) + g) ^ xf) * 16;
    const unsigned wl = lds0 + (8 * g + (i16 >> 2)) * 128 + ((hd ^ (((i16 >> 3) & 1) | ((g & 1) << 1))) * 32) + (i16 & 3) * 8;
    const unsigned wA = wl + par * 4096, wB = wl + (par ^ 1) * 4096;
    unsigned x0A = xb0 + xoA, x1A = xb1 + xoA, x0B = xb0 + xoB, x1B = xb1 + xoB, wAa = wA, wBa = wB;
#define RDX(a, so) lds_ld128((a) + (so))
#define RDW(a, so, t) cat8(tr_read((a) + (so) + (t) * 8192), tr_read((a) + (so) + (t) * 8192 + 512))
#define SMFMA(d, a, b) d = MFMA16(a, b, d)
#define ARD(stmt) stmt
    f32x4 aq0 = {}, aq1 = {}, ak0 = {}, ak1 = {}, av0 = {}, av1 = {};
    f32x4 rsum = {}, rsq = {}, cs0 = {}, cs1 = {}, cs2 = {};
    bf16x8 ones8 = {0x3f80, 0x3f80, 0x3f80, 0x3f80, 0x3f80, 0x3f80, 0x3f80, 0x3f80};
    PIN(ones8);
    if (tid < 8) lds_stu(lds0 + L_RDY + tid * 4, 0u);
    BARRIER();
    STAMP(1);
    AFrag fa, fb;
    fa.q = RDW(wAa, 0, 0); fa.x0 = RDX(x0A, 0); fa.x1 = RDX(x1A, 0); fa.k = RDW(wAa, 0, 1); fa.v = RDW(wAa, 0, 2);
    fb.q = RDW(wBa, 0, 0); fb.x0 = RDX(x0B, 0); fb.x1 = RDX(x1B, 0); fb.k = RDW(wBa, 0, 1); fb.v = RDW(wBa, 0, 2);
    const unsigned RS = lds0 + L_RS, CS = lds0 + L_STG, MR = lds0 + L_PRIV + wid * PRIV + 2048, DUMP = lds0 + L_PRIV + wid * PRIV + lane * 16;
    unsigned ad_rs = (g == (i16 >> 2)) ? RS + ((rt * 4 + hd) * 16 + i16) * 8 : DUMP;
    unsigned ad_cq = (i16 == 0) ? CS + wid * 192 + g * 16 : DUMP, ad_cv = CS + wid * 192 + 128 + i16 * 4;
    unsigned rd_rs = RS + ((rt * 4 + (g & 1) * 2) * 16 + i16) * 8, rd_cq = CS + (wid ^ 1) * 192 + g * 16, rd_cv = CS + (wid ^ 1) * 192 + 128 + i16 * 4;
    unsigned ad_mr = MR + r32 * 4, rd_mr = MR + (sw * 16 + 4 * g) * 4;
    const int ahead = hq * 4 + hd, tile = rb * 2 + rt;
    unsigned go_k = WS_KV + (ahead * 64 + tile) * 2048 + (lane ^ (sw * 16)) * 16, go_q = WS_Q + (ahead * 64 + tile) * 1024 + (lane ^ (sw * 16)) * 16;
    unsigned go_v = WS_KV + (ahead * 64 + tile) * 2048 + 1024 + (lane ^ (sw * 32)) * 16;
    PIN(ad_rs); PIN(ad_cq); PIN(ad_cv); PIN(rd_rs); PIN(rd_cq); PIN(rd_cv); PIN(ad_mr); PIN(rd_mr); PIN(go_k); PIN(go_q); PIN(go_v);
#pragma unroll
    for (int c = 0; c < 7; ++c) {
      asm volatile("s_barrier" ::: "memory");
      FSTAMP(16 + c);
      if (((c + 1) & 1) == 0) {
        const unsigned d = ((c + 1) & 2) ? 65536u : (unsigned)-65536;
        x0A += d; x1A += d; x0B += d; x1B += d; wAa += d; wBa += d;
        PIN(x0A); PIN(x1A); PIN(x0B); PIN(x1B); PIN(wAa); PIN(wBa);
      }
      const unsigned so = ((c + 1) & 1) * 32768;
      SBAR();
      aq0 = MFMA16(fa.q, fa.x0, aq0); aq1 = MFMA16(fa.q, fa.x1, aq1);
      SMFMA(cs0, fa.q, ones8);
      SBAR(); ARD(fa.q = RDW(wAa, so, 0)); SBAR();
      ak0 = MFMA16(fa.k, fa.x0, ak0); ak1 = MFMA16(fa.k, fa.x1, ak1);
      SMFMA(cs1, fa.k, ones8);
      SBAR(); ARD(fa.k = RDW(wAa, so, 1)); SBAR();
      av0 = MFMA16(fa.x0, fa.v, av0); av1 = MFMA16(fa.x1, fa.v, av1);
      SBAR(); ARD(fa.x1 = RDX(x1A, so)); SBAR();
      SMFMA(cs2, ones8, fa.v); SMFMA(rsum, ones8, fa.x0); SMFMA(rsq, fa.x0, fa.x0);
      SBAR(); ARD(fa.x0 = RDX(x0A, so)); ARD(fa.v = RDW(wAa, so, 2)); SBAR();
      aq0 = MFMA16(fb.q, fb.x0, aq0); aq1 = MFMA16(fb.q, fb.x1, aq1);
      SBAR(); ARD(fb.q = RDW(wBa, so, 0)); SBAR();
      ak0 = MFMA16(fb.k, fb.x0, ak0); ak1 = MFMA16(fb.k, fb.x1, ak1);
      SBAR(); ARD(fb.k = RDW(wBa, so, 1)); SBAR();
      av0 = MFMA16(fb.x0, fb.v, av0); av1 = MFMA16(fb.x1, fb.v, av1);
      SBAR(); ARD(fb.x0 = RDX(x0B, so)); ARD(fb.x1 = RDX(x1B, so)); ARD(fb.v = RDW(wBa, so, 2)); SBAR();
    }
    rsum = MFMA16(ones8, fa.x0, rsum); rsq = MFMA16(fa.x0, fa.x0, rsq);
    cs0 = MFMA16(fa.q, ones8, cs0); cs1 = MFMA16(fa.k, ones8, cs1); cs2 = MFMA16(ones8, fa.v, cs2);
    SBAR();
    ak0 = MFMA16(fa.k, fa.x0, ak0); ak1 = MFMA16(fa.k, fa.x1, ak1);
    SBAR();
    {
      const bool b0 = lane & 1, b1 = lane & 2;
      const float dlo = b0 ? rsq[1] : rsq[0], dhi = b0 ? rsq[3] : rsq[2];
      *(LAS f32x2*)(uintptr_t)ad_rs = f32x2{rsum[0], b1 ? dhi : dlo};
      lds_stf4(ad_cq, cs0); lds_stf4(ad_cq + 64, cs1); lds_stf(ad_cv, cs2[0]);
    }
    STAMP(2);
    BARRIER();
    {
      const f32x2 pa = *(const LAS f32x2*)(uintptr_t)rd_rs, pb = *(const LAS f32x2*)(uintptr_t)(rd_rs + 128);
      const f32x4 oq = lds_ldf4(rd_cq), ok = lds_ldf4(rd_cq + 64);
      const float ov = lds_ldf(rd_cv);
      SBAR();
      ak0 = MFMA16(fb.k, fb.x0, ak0); ak1 = MFMA16(fb.k, fb.x1, ak1);
      aq0 = MFMA16(fa.q, fa.x0, aq0); aq1 = MFMA16(fa.q, fa.x1, aq1);
      SBAR();
      float mb, rden;
      ln_stats(pa[0] + pb[0], pa[1] + pb[1], mb, rden);
      aq0 = MFMA16(fb.q, fb.x0, aq0); aq1 = MFMA16(fb.q, fb.x1, aq1);
      av0 = MFMA16(fa.x0, fa.v, av0); av1 = MFMA16(fa.x1, fa.v, av1);
      av0 = MFMA16(fb.x0, fb.v, av0); av1 = MFMA16(fb.x1, fb.v, av1);
      lds_stf(ad_mr, mb); lds_stf(ad_mr + 128, rden);
      const f32x4 cq = cs0 + oq, ck = cs1 + ok;
      const float cv = cs2[0] + ov;
      const auto sm = __builtin_amdgcn_permlane16_swap(__float_as_uint(mb), __float_as_uint(mb), false, false);
      const auto sd = __builtin_amdgcn_permlane16_swap(__float_as_uint(rden), __float_as_uint(rden), false, false);
      const float m0 = __uint_as_float(sw ? sm[1] : sm[0]), m1 = __uint_as_float(sw ? sm[0] : sm[1]);
      const float d0 = __uint_as_float(sw ? sd[1] : sd[0]), d1v = __uint_as_float(sw ? sd[0] : sd[1]);
      WAIT_LGKM0();
      const f32x4 mv0 = lds_ldf4(rd_mr), mv1 = lds_ldf4(rd_mr ^ 64), dv0 = lds_ldf4(rd_mr + 128), dv1 = lds_ldf4((rd_mr ^ 64) + 128);
      unsigned a0, a1, b0, b1;
      a0 = pack_bf16((ak0[0] - m0 * ck[0]) * d0, (ak0[1] - m0 * ck[1]) * d0); a1 = pack_bf16((ak0[2] - m0 * ck[2]) * d0, (ak0[3] - m0 * ck[3]) * d0);
      b0 = pack_bf16((ak1[0] - m1 * ck[0]) * d1v, (ak1[1] - m1 * ck[1]) * d1v); b1 = pack_bf16((ak1[2] - m1 * ck[2]) * d1v, (ak1[3] - m1 * ck[3]) * d1v);
      { auto r0 = __builtin_amdgcn_permlane16_swap(a0, b0, false, false); auto r1 = __builtin_amdgcn_permlane16_swap(a1, b1, false, false);
        ho_st(rs, go_k, u32x4{r0[0], r1[0], r0[1], r1[1]}); }
      const float e0 = d0 * QSCALE, e1 = d1v * QSCALE;
      a0 = pack_bf16((aq0[0] - m0 * cq[0]) * e0, (aq0[1] - m0 * cq[1]) * e0); a1 = pack_bf16((aq0[2] - m0 * cq[2]) * e0, (aq0[3] - m0 * cq[3]) * e0);
      b0 = pack_bf16((aq1[0] - m1 * cq[0]) * e1, (aq1[1] - m1 * cq[1]) * e1); b1 = pack_bf16((aq1[2] - m1 * cq[2]) * e1, (aq1[3] - m1 * cq[3]) * e1);
      { auto r0 = __builtin_amdgcn_permlane16_swap(a0, b0, false, false); auto r1 = __builtin_amdgcn_permlane16_swap(a1, b1, false, false);
        ho_st(rs, go_q, u32x4{r0[0], r1[0], r0[1], r1[1]}); }
      a0 = pack_bf16((av0[0] - mv0[0] * cv) * dv0[0], (av0[1] - mv0[1] * cv) * dv0[1]); a1 = pack_bf16((av0[2] - mv0[2] * cv) * dv0[2], (av0[3] - mv0[3] * cv) * dv0[3]);
      b0 = pack_bf16((av1[0] - mv1[0] * cv) * dv1[0], (av1[1] - mv1[1] * cv) * dv1[1]); b1 = pack_bf16((av1[2] - mv1[2] * cv) * dv1[2], (av1[3] - mv1[3] * cv) * dv1[3]);
      { auto r0 = __builtin_amdgcn_permlane32_swap(a0, b0, false, false); auto r1 = __builtin_amdgcn_permlane32_swap(a1, b1, false, false);
        ho_st(rs, go_v, u32x4{r0[0], r1[0], r0[1], r1[1]}); }
    }
    tag = __builtin_amdgcn_readfirstlane(tag) + 1u;
    STAMP(3);
    asm volatile("s_waitcnt vmcnt(0)" : "+v"(code_pf) :: "memory");
    BARRIER();
    if (tid == 0) __builtin_amdgcn_raw_buffer_store_b64(u32x2{tag, ~tag}, rs, WS_FA + (hq * 32 + rb) * 128, 0, 16);
    STAMP(4);
  }

  const int qg = wid & 3, kh = wid >> 2;
  const unsigned priv = lds0 + L_PRIV + wid * PRIV;
  const int row = tid >> 4, c8 = tid & 15;
  const int goff = ((tt * 32 + row) * D + cbk * 128 + c8 * 8) * 2;
  const u32x4 xv = *(const u32x4*)((const char*)x + goff);
  const u32x4 wrv = *(const u32x4*)((const char*)wr + (cbk * 128 + c8 * 8) * 2);
  float swr = 0.f;
  {
    const u32x4 wra = *(const u32x4*)((const char*)wr + lane * 16);
#pragma unroll
    for (int j = 0; j < 4; ++j) swr += bf_lo(wra[j]) + bf_hi(wra[j]);
    swr = sum_rows(row16_sum(swr));
  }
  const unsigned kaddr = lds0 + kh * 32 * KVT + lane * 16;
  const unsigned vaddr0 = lds0 + kh * 32 * KVT + (r32 < 16 ? 1024 + (hh * 16 + r32) * 16 : (r32 == 16 ? 2048 : 2064));
  const unsigned vaddr1 = vaddr0 + (r32 < 16 ? 512 : 0);
  u32x4 fw0 = {}, fw1 = {}, fw2 = {}, fw3 = {}, fw4 = {}, fw5 = {};
  const char* fb0 = isd1 ? (const char*)wg + wid * (64 * F * 2) + d1 * 32 : (const char*)wd + wid * (32 * D * 2) + sub * 32;
  const char* fb2 = isd1 ? (const char*)wu + wid * (64 * F * 2) + d1 * 32 : fb0 + 2 * (256 * D * 2);
  const unsigned fst = isd1 ? 32 * F * 2 : 256 * D * 2;
  const unsigned fs4 = isd1 ? 0u : 2 * fst, fs5 = (isd1 || wid >= 3) ? 0u : 3 * fst;
  const unsigned fvo = (lane >> 1) * (isd1 ? F * 2 : D * 2) + (lane & 1) * 16;
  const int fit = (isd1 || isd2) ? 2 : -1;
  __builtin_amdgcn_s_setprio(2);
  wait_rdy(lds0 + L_RDY, 1u);
  STAMP(6);
  const bf16x8 qa = lds_ld128(lds0 + L_STG + qg * 2048 + lane * 16), qb2 = lds_ld128(lds0 + L_STG + qg * 2048 + 1024 + lane * 16);
  WAIT_LGKM0();
  if (lane == 0) __hip_atomic_fetch_add((LAS unsigned*)(uintptr_t)(lds0 + L_RDY + 16 + qg * 4), 1u, __ATOMIC_RELAXED, __HIP_MEMORY_SCOPE_WORKGROUP);
  float mref;
  f32x16 oa, ob;
  {
    unsigned ka = kaddr, va0 = vaddr0, va1 = vaddr1;
    bf16x8 k0 = lds_ld128(ka), k1, v00 = lds_ld128(va0), v01 = lds_ld128(va1), v10, v11 = v00;
    f32x16 ng, sA, sB;
    {
      const f32x16 z = {};
      sA = MFMA32(k0, qa, z); sB = MFMA32(k0, qb2, z);
      mref = max_halves(fmaxf(tile_max(sA), tile_max(sB)));
    }
#pragma unroll
    for (int j = 0; j < 16; ++j) { ng[j] = -mref; sA[j] -= mref; oa[j] = 0.f; ob[j] = 0.f; }
    PIN(ng); PIN(sA);
    u32x4 p0, p1 = {0, 0, 0, 0};
    for (int it = 0; it < 8; ++it) {
      FSTAMP(32 + 2 * it);
      if (it == 0) wait_rdy(lds0 + L_RDY, 2u);
      if (it == 1) wait_rdy(lds0 + L_RDY, 4u);
      if (it == 3) wait_rdy(lds0 + L_RDY, 8u);
      { unsigned tv_;
        asm volatile("s_cmp_lg_u32 %[it], %[fit]\n\ts_cbranch_scc1 .Lff%=\n\t"
          "global_load_dwordx4 %[a0], %[vo], %[b0]\n\tglobal_load_dwordx4 %[a2], %[vo], %[b2]\n\tv_add_u32 %[tv], %[st], %[vo]\n\t"
          "global_load_dwordx4 %[a1], %[tv], %[b0]\n\tglobal_load_dwordx4 %[a3], %[tv], %[b2]\n\tv_add_u32 %[tv], %[s4], %[vo]\n\t"
          "global_load_dwordx4 %[a4], %[tv], %[b2]\n\tv_add_u32 %[tv], %[s5], %[vo]\n\t"
          "global_load_dwordx4 %[a5], %[tv], %[b2]\n.Lff%=:"
          : [a0] "+v"(fw0), [a1] "+v"(fw1), [a2] "+v"(fw2), [a3] "+v"(fw3), [a4] "+v"(fw4), [a5] "+v"(fw5), [tv] "=&v"(tv_)
          : [it] "s"(it), [fit] "s"(fit), [vo] "v"(fvo), [b0] "s"(fb0), [b2] "s"(fb2), [st] "s"(fst), [s4] "s"(fs4), [s5] "s"(fs5) : "memory", "scc"); }
      if (it == 5) __builtin_amdgcn_s_setprio(1);
      if (it == 7) __builtin_amdgcn_s_setprio(0);
      SBAR();
      TWO_TILES(0);
      TWO_TILES(2);
      ka += 4 * KVT; va0 += 4 * KVT; va1 += 4 * KVT;
    }
    ob = MFMA32(v11, BC8(p1), ob);
  }
  asm volatile("s_waitcnt vmcnt(0)" : "+v"(fw0), "+v"(fw1), "+v"(fw2), "+v"(fw3), "+v"(fw4), "+v"(fw5) :: "memory");
  float ma = mref, mb = mref;
  float la = sum_halves(oa[8]), lb = sum_halves(ob[8]);
  {
    float t = 0.f;
#pragma unroll
    for (int j = 0; j < 8; ++j) t += fabsf(oa[j]) + fabsf(ob[j]);
    const bool ok = (la <= 16777216.f) && (lb <= 16777216.f) && (la >= 5.9604645e-8f) && (lb >= 5.9604645e-8f) && (t <= 3.0e38f);
    if (__builtin_expect(!__all(ok), 0)) {
      const f32x16 z = {};
      ma = -INFINITY; mb = -INFINITY;
      for (int t2 = 0; t2 < 32; ++t2) {
        const bf16x8 kf = lds_ld128(kaddr + t2 * KVT);
        ma = fmaxf(ma, tile_max(MFMA32(kf, qa, z)));
        mb = fmaxf(mb, tile_max(MFMA32(kf, qb2, z)));
      }
      ma = max_halves(ma); mb = max_halves(mb);
      oa = z; ob = z;
      for (int t2 = 0; t2 < 32; ++t2) {
        const bf16x8 kf = lds_ld128(kaddr + t2 * KVT), v0 = lds_ld128(vaddr0 + t2 * KVT), v1 = lds_ld128(vaddr1 + t2 * KVT);
        const f32x16 sa = MFMA32(kf, qa, z), sb = MFMA32(kf, qb2, z);
        u32x4 pa0, pa1, pb0, pb1;
#pragma unroll
        for (int j = 0; j < 4; ++j) {
          pa0[j] = PK(EX(sa[2 * j] - ma), EX(sa[2 * j + 1] - ma)); pa1[j] = PK(EX(sa[8 + 2 * j] - ma), EX(sa[9 + 2 * j] - ma));
          pb0[j] = PK(EX(sb[2 * j] - mb), EX(sb[2 * j + 1] - mb)); pb1[j] = PK(EX(sb[8 + 2 * j] - mb), EX(sb[9 + 2 * j] - mb));
        }
        oa = MFMA32(v0, BC8(pa0), oa); oa = MFMA32(v1, BC8(pa1), oa);
        ob = MFMA32(v0, BC8(pb0), ob); ob = MFMA32(v1, BC8(pb1), ob);
      }
      la = sum_halves(oa[8]); lb = sum_halves(ob[8]);
    }
  }
  STAMP(7);
  {
  RELANE();
  const int row = tid >> 4, c8 = tid & 15;
  const int goff = ((tt * 32 + row) * D + cbk * 128 + c8 * 8) * 2;
  {
    const unsigned xa = priv + lane * 16;
    if (kh == 0) {
      lds_stf4(xa, f32x4{ob[0], ob[1], ob[2], ob[3]}); lds_stf4(xa + 1024, f32x4{ob[4], ob[5], ob[6], ob[7]});
      *(LAS f32x2*)(uintptr_t)(priv + 2048 + lane * 8) = f32x2{lb, mb};
    } else {
      lds_stf4(xa, f32x4{oa[0], oa[1], oa[2], oa[3]}); lds_stf4(xa + 1024, f32x4{oa[4], oa[5], oa[6], oa[7]});
      *(LAS f32x2*)(uintptr_t)(priv + 2048 + lane * 8) = f32x2{la, ma};
#pragma unroll
      for (int j = 0; j < 8; ++j) oa[j] = ob[j];
      ma = mb; la = lb;
    }
    BARRIER();
    const unsigned yp = lds0 + L_PRIV + (wid ^ 4) * PRIV;
    const f32x4 y0 = lds_ldf4(yp + lane * 16), y1 = lds_ldf4(yp + 1024 + lane * 16);
    const f32x2 y2 = *(const LAS f32x2*)(uintptr_t)(yp + 2048 + lane * 8);
    const float M = fmaxf(ma, y2[1]);
    float f1 = EX(ma - M), f2 = EX(y2[1] - M);
    const float inv = RCP(la * f1 + y2[0] * f2);
    f1 *= inv; f2 *= inv;
    u32x4 po;
    po[0] = san(pack_bf16(oa[0] * f1 + y0[0] * f2, oa[1] * f1 + y0[1] * f2));
    po[1] = san(pack_bf16(oa[2] * f1 + y0[2] * f2, oa[3] * f1 + y0[3] * f2));
    po[2] = san(pack_bf16(oa[4] * f1 + y1[0] * f2, oa[5] * f1 + y1[1] * f2));
    po[3] = san(pack_bf16(oa[6] * f1 + y1[2] * f2, oa[7] * f1 + y1[3] * f2));
    ho_st(rs, HO_O + ((qb * 8 + qg * 2 + kh) * H + head) * 1024 + lane * 16, po);
  }
  lds_stu4(lds0 + X_OFF + row * 272 + c8 * 16, xv);
  if (row == 0) lds_stu4(lds0 + X_OFF + 32 * 272 + c8 * 16, wrv);
  STAMP(8);

  {
    u32x4 o0 = {SENT, SENT, SENT, SENT}, o1 = o0, o2 = o0, o3 = o0;
    const int oo = HO_O + (tt * H + wid * 4) * 1024 + lane * 16;
    for (unsigned spin = 0;; ++spin) {
      if (__any(pend(o0))) o0 = ho_ld(rs, oo);
      if (__any(pend(o1))) o1 = ho_ld(rs, oo + 1024);
      if (__any(pend(o2))) o2 = ho_ld(rs, oo + 2048);
      if (__any(pend(o3))) o3 = ho_ld(rs, oo + 3072);
      if (!__any(pend(o0) || pend(o1) || pend(o2) || pend(o3)) || spin > SPIN_CAP) break;
      __builtin_amdgcn_s_sleep(1);
    }
    const unsigned oa2 = lds0 + wid * 4096 + lane * 16;
    lds_stu4(oa2, o0); lds_stu4(oa2 + 1024, o1); lds_stu4(oa2 + 2048, o2); lds_stu4(oa2 + 3072, o3);
  }
  BARRIER();
  STAMP(9);
  if (isd1) {
    const unsigned fa = lds0 + F_OFF + wid * 2048 + lane * 16;
    lds_stu4(fa, fw0); lds_stu4(fa + 1024, fw1); lds_stu4(fa + 16384, fw2); lds_stu4(fa + 17408, fw3);
  } else if (isd2) {
    const unsigned fa = lds0 + F_OFF + wid * 1024 + lane * 16;
    lds_stu4(fa, fw0); lds_stu4(fa + 8192, fw1); lds_stu4(fa + 16384, fw2); lds_stu4(fa + 24576, fw3); lds_stu4(fa + 32768, fw4); lds_stu4(fa + 40960, fw5);
  }
  BARRIER();
  STAMP(10);
  if (wid == 0) {
    u32x4 sv[8];
#pragma unroll
    for (int j = 0; j < 8; ++j) sv[j] = u32x4{SENT, SENT, SENT, SENT};
    const int so = HO_ST + (tt * 16 + 8 * hh) * 512 + r32 * 16;
    for (unsigned spin = 0;; ++spin) {
      bool pn = false;
#pragma unroll
      for (int j = 0; j < 8; ++j) if (__any(pend(sv[j]))) sv[j] = ho_ld(rs, so + j * 512);
#pragma unroll
      for (int j = 0; j < 8; ++j) pn = pn || pend(sv[j]);
      if (!__any(pn) || spin > SPIN_CAP) break;
      __builtin_amdgcn_s_sleep(1);
    }
    float s1 = 0.f, s2 = 0.f, s3 = 0.f;
#pragma unroll
    for (int j = 0; j < 8; ++j) { s1 += __uint_as_float(sv[j][0]); s2 += __uint_as_float(sv[j][1]); s3 += __uint_as_float(sv[j][2]); }
    s1 = sum_halves(s1); s2 = sum_halves(s2); s3 = sum_halves(s3);
    float mbv, rden;
    ln_stats(s1, s2, mbv, rden);
    ho_st1(rs, HO_KEY + (2 * cbk + hh) * (S * 4) + (tt * 32 + r32) * 4, sort_key((s3 - mbv * swr) * rden, tt * 32 + r32));
    if (lane < 32) { lds_stf(lds0 + S_OFF + 512 + lane * 8, mbv); lds_stf(lds0 + S_OFF + 516 + lane * 8, rden); }
  }
  const u32x4 hv = lds_ldu4(lds0 + X_OFF + row * 272 + c8 * 16);
  float h[8];
#pragma unroll
  for (int j = 0; j < 4; ++j) { h[2 * j] = bf_lo(hv[j]); h[2 * j + 1] = bf_hi(hv[j]); }
  BARRIER();
  STAMP(11);
  u32x4 lv, ov;
  {
    const float mbv = lds_ldf(lds0 + S_OFF + 512 + row * 8), rden = lds_ldf(lds0 + S_OFF + 516 + row * 8);
#pragma unroll
    for (int j = 0; j < 4; ++j) {
      const unsigned d = pack_bf16(h[2 * j] - mbv, h[2 * j + 1] - mbv);
      lv[j] = pack_bf16(bf_lo(d) * rden, bf_hi(d) * rden);
      ov[j] = pack_bf16(h[2 * j] + bf_lo(lv[j]), h[2 * j + 1] + bf_hi(lv[j]));
    }
    ho_st(rs, HO_LN + goff, u32x4{san(lv[0]), san(lv[1]), san(lv[2]), san(lv[3])});
  }
  {
    u32x4 kv = {SENT, SENT, SENT, SENT};
    for (unsigned spin = 0;; ++spin) {
      if (pend(kv)) kv = ho_ld(rs, HO_KEY + xcd * (S * 4) + tid * 16);
      if (!__any(pend(kv)) || spin > SPIN_CAP) break;
      __builtin_amdgcn_s_sleep(1);
    }
    STAMP(12);
    unsigned k0 = kv[0], k1 = kv[1], k2 = kv[2], k3 = kv[3], t;
    t = max(k0, k1); k1 = min(k0, k1); k0 = t;  t = max(k2, k3); k3 = min(k2, k3); k2 = t;
    t = max(k0, k2); k2 = min(k0, k2); k0 = t;  t = max(k1, k3); k3 = min(k1, k3); k1 = t;
    t = max(k1, k2); k2 = min(k1, k2); k1 = t;
    unsigned mine = 0;
#pragma unroll
    for (int c = 0; c < 8; ++c) {
      const unsigned m = wave_max_u(k0);
      if (lane == c) mine = m;
      if (k0 == m) { k0 = k1; k1 = k2; k2 = k3; k3 = 0; }
    }
    if (lane < 8) lds_stu(lds0 + T_OFF + (wid * 8 + lane) * 4, mine);
    BARRIER();
    const unsigned cand = lds_ldu(lds0 + T_OFF + (tid >> 3) * 4);
    const u32x4 o0 = lds_ldu4(lds0 + T_OFF + (tid & 7) * 32), o1 = lds_ldu4(lds0 + T_OFF + (tid & 7) * 32 + 16);
    unsigned rank = 0;
#pragma unroll
    for (int j = 0; j < 4; ++j) { rank += (o0[j] > cand) ? 1u : 0u; rank += (o1[j] > cand) ? 1u : 0u; }
    rank += dpp_u<DPP_X1>(rank); rank += dpp_u<DPP_X2>(rank); rank += dpp_u<DPP_HMIR>(rank);
    if ((tid & 7) == 0 && rank < 8) lds_stu(lds0 + T_OFF + 256 + rank * 4, cand);
    BARRIER();
  }
  STAMP(13);
  const unsigned mykey = lds_ldu(lds0 + T_OFF + 256 + (lane & 7) * 4);
  const int myrow = 2047 - (int)(mykey & 0x7ffu);
  if (isd1) {
    u32x4 a0, a1;
    const unsigned fl = lds0 + F_OFF + wid * 2048 + (8 * g + (i16 >> 2)) * 32 + (i16 & 3) * 8;
    const bf16x8 f0 = cat8(tr_read(fl), tr_read(fl + 128)), f1 = cat8(tr_read(fl + 1024), tr_read(fl + 1024 + 128));
    const bf16x8 f2 = cat8(tr_read(fl + 16384), tr_read(fl + 16384 + 128)), f3 = cat8(tr_read(fl + 17408), tr_read(fl + 17408 + 128));
    const int ao = HO_LN + (myrow * D + wid * 64 + g * 8) * 2;
    for (unsigned spin = 0;; ++spin) {
      a0 = ho_ld(rs, ao); a1 = ho_ld(rs, ao + 64);
      if (!__any(pend(a0) || pend(a1)) || spin > SPIN_CAP) break;
      __builtin_amdgcn_s_sleep(1);
    }
    f32x4 ag = {}, au = {};
    ag = MFMA16(BC8(a0), f0, ag); au = MFMA16(BC8(a0), f2, au);
    ag = MFMA16(BC8(a1), f1, ag); au = MFMA16(BC8(a1), f3, au);
    if (lane < 32) { lds_stf4(lds0 + R_OFF + wid * 1024 + lane * 16, ag); lds_stf4(lds0 + R_OFF + wid * 1024 + 512 + lane * 16, au); }
    BARRIER();
    if (wid == 0) {
      const int tok = lane >> 3, cp = lane & 7;
      const unsigned ra = lds0 + R_OFF + (((tok >> 2) * 16 + cp * 2) * 4 + (tok & 3)) * 4;
      float g0 = 0.f, g1 = 0.f, u0 = 0.f, u1 = 0.f;
#pragma unroll
      for (int w2 = 0; w2 < 8; ++w2) {
        g0 += lds_ldf(ra + w2 * 1024); g1 += lds_ldf(ra + w2 * 1024 + 16); u0 += lds_ldf(ra + w2 * 1024 + 512); u1 += lds_ldf(ra + w2 * 1024 + 528);
      }
      g0 = rbf(g0); g1 = rbf(g1); u0 = rbf(u0); u1 = rbf(u1);
      const float s0 = rbf(g0 * sigmoid(g0)), s1 = rbf(g1 * sigmoid(g1));
      ho_st1(rs, HO_ACT + d1 * 256 + lane * 4, san(pack_bf16(s0 * u0, s1 * u1)));
    }
    STAMP(14);
  }
  {
    const int grow = tt * 32 + row;
    int sel = -1;
#pragma unroll
    for (int c = 0; c < 8; ++c) if (__builtin_amdgcn_readlane(myrow, c) == grow) sel = c;
    if (sel < 0) __builtin_amdgcn_raw_buffer_store_b128(ov, __builtin_amdgcn_make_buffer_rsrc(out, 0, S * D * 2, 0x00020000), goff, 0, 16);
    else ho_st(rs, HO_HS + (sel * D + cbk * 128 + c8 * 8) * 2, u32x4{san(hv[0]), san(hv[1]), san(hv[2]), san(hv[3])});
    if (b == 0 && tid == 0) __builtin_amdgcn_raw_buffer_store_b32(tag, rs, WS_EP, 0, 16);
  }
  if (isd2) {
    const int n0 = sub * 16;
    unsigned hw = SENT, lw = SENT, fkey = 0;
    const int tok = lane >> 3, cp = lane & 7;
    if (wid == 0) {
      fkey = lds_ldu(lds0 + T_OFF + 256 + tok * 4);
      const int frow = 2047 - (int)(fkey & 0x7ffu);
      for (unsigned spin = 0;; ++spin) {
        if (hw == SENT) hw = __builtin_amdgcn_raw_buffer_load_b32(rs, HO_HS + (tok * D + n0 + cp * 2) * 2, 0, 16);
        if (lw == SENT) lw = __builtin_amdgcn_raw_buffer_load_b32(rs, HO_LN + (frow * D + n0 + cp * 2) * 2, 0, 16);
        if (!__any(hw == SENT || lw == SENT) || spin > SPIN_CAP) break;
        __builtin_amdgcn_s_sleep(1);
      }
    }
    bf16x8 ff[6];
    const unsigned fl = lds0 + F_OFF + wid * 1024 + (8 * g + (i16 >> 2)) * 32 + (i16 & 3) * 8;
#pragma unroll
    for (int j = 0; j < 6; ++j) ff[j] = cat8(tr_read(fl + j * 8192), tr_read(fl + j * 8192 + 128));
    u32x4 av[6];
#pragma unroll
    for (int j = 0; j < 6; ++j) av[j] = (wid + 8 * j < 43) ? u32x4{SENT, SENT, SENT, SENT} : u32x4{0u, 0u, 0u, 0u};
    const int ao = HO_ACT + ((wid * 2 + (g >> 1)) * 8 + (lane & 7)) * 32 + (g & 1) * 16;
    for (unsigned spin = 0;; ++spin) {
      bool p = false;
#pragma unroll
      for (int j = 0; j < 6; ++j) if (__any(pend(av[j]))) av[j] = ho_ld(rs, ao + j * 4096);
#pragma unroll
      for (int j = 0; j < 6; ++j) p = p || pend(av[j]);
      if (!__any(p) || spin > SPIN_CAP) break;
      __builtin_amdgcn_s_sleep(1);
    }
    STAMP(14);
    f32x4 y = {};
#pragma unroll
    for (int j = 0; j < 6; ++j) if (wid + 8 * j < 43) y = MFMA16(BC8(av[j]), ff[j], y);
    if (lane < 32) lds_stf4(lds0 + R_OFF + wid * 512 + lane * 16, y);
    BARRIER();
    if (wid == 0) {
      const unsigned ra = lds0 + R_OFF + (((tok >> 2) * 16 + cp * 2) * 4 + (tok & 3)) * 4;
      float y0 = 0.f, y1 = 0.f;
#pragma unroll
      for (int w2 = 0; w2 < 8; ++w2) { y0 += lds_ldf(ra + w2 * 512); y1 += lds_ldf(ra + w2 * 512 + 16); }
      const float sig = rbf(sigmoid(key_score(fkey)));
      y0 = rbf(rbf(y0) * sig); y1 = rbf(rbf(y1) * sig);
      *(unsigned*)((char*)out + ((size_t)(2047 - (int)(fkey & 0x7ffu)) * D + n0 + cp * 2) * 2) =
          pack_bf16(bf_lo(hw) + rbf(bf_lo(lw) + y0), bf_hi(hw) + rbf(bf_hi(lw) + y1));
    }
  }
  }
  STAMP(15); STAMP_OUT(0);
}

extern "C" void kernel_launch(void* const* d_in, const int* in_sizes, int n_in, void* d_out, int out_size, void* d_ws, size_t ws_size,
                              hipStream_t stream) {
  (void)hipFuncSetAttribute((const void*)k_fused, hipFuncAttributeMaxDynamicSharedMemorySize, LDS_BYTES);
  k_fused<<<256, 768, LDS_BYTES, stream>>>((const uint16_t*)d_in[0], (const uint16_t*)d_in[1], (const uint16_t*)d_in[2], (const uint16_t*)d_in[4],
                                           (const uint16_t*)d_in[5], (const uint16_t*)d_in[6], (const uint16_t*)d_in[7], (char*)d_ws, (uint16_t*)d_out);
}
```

```cpp
#include <hip/hip_runtime.h>
#include <hip/hip_bf16.h>
#include <stdint.h>

typedef short bf16x8 __attribute__((ext_vector_type(8)));
typedef short bf16x4 __attribute__((ext_vector_type(4)));
typedef float f32x16 __attribute__((ext_vector_type(16)));
typedef float f32x4 __attribute__((ext_vector_type(4)));
typedef float f32x2 __attribute__((ext_vector_type(2)));
typedef __bf16 bf16x2_t __attribute__((ext_vector_type(2)));
typedef unsigned u32x4 __attribute__((ext_vector_type(4)));
typedef unsigned u32x2 __attribute__((ext_vector_type(2)));
#define LAS __attribute__((address_space(3)))
#define GAS __attribute__((address_space(1)))
#define DI __device__ __forceinline__
#define MFMA32(a, b, c) __builtin_amdgcn_mfma_f32_32x32x16_bf16((a), (b), (c), 0, 0, 0)
#define MFMA16(a, b, c) __builtin_amdgcn_mfma_f32_16x16x32_bf16((a), (b), (c), 0, 0, 0)
#define SBAR() __builtin_amdgcn_sched_barrier(0)
#define PIN(x) asm volatile("" : "+v"(x))
#define BC8(x) __builtin_bit_cast(bf16x8, x)

constexpr int S = 2048, D = 512, H = 32, F = 1376;
constexpr float LN_EPS = 1e-5f;
constexpr float QSCALE = 0.25f * 1.4426950408889634f;
constexpr unsigned SENT = 0xFFFFFFFFu;
constexpr unsigned SPIN_CAP = 1u << 16;

constexpr int WS_Q = 0;
constexpr int WS_KV = WS_Q + H * 64 * 1024;
constexpr int WS_FA = WS_KV + H * 64 * 2048;
constexpr int WS_FR = WS_FA + H * 64 * 128;
constexpr int WS_EP = WS_FR + 4096;
constexpr int WS_HO = WS_EP + 4096;
constexpr int HO_O = WS_HO;
constexpr int HO_LN = HO_O + 64 * H * 1024;
constexpr int HO_ST = HO_LN + S * D * 2;
constexpr int HO_KEY = HO_ST + 64 * 16 * 512;
constexpr int HO_ACT = HO_KEY + 8 * S * 4;
constexpr int HO_HS = HO_ACT + 24576;
constexpr int HO_END = HO_HS + 8192;
constexpr int HO_PER_WG = 19 * 1024;
static_assert(WS_HO + 256 * HO_PER_WG >= HO_END, "reset covers the hand-off words");
constexpr int WS_BYTES = WS_HO + 256 * HO_PER_WG;

#define STAMP_DECL do {} while (0)
#define STAMP(i) do {} while (0)
#define STAMP_OUT(k) do {} while (0)

#define FSTAMP(i) do {} while (0)

DI unsigned pack_bf16(float lo, float hi) {
  f32x2 v = {lo, hi};
  bf16x2_t r = __builtin_convertvector(v, bf16x2_t);
  return __builtin_bit_cast(unsigned, r);
}
DI float bf_lo(unsigned v) { return __uint_as_float(v << 16); }
DI float bf_hi(unsigned v) { return __uint_as_float(v & 0xffff0000u); }
DI float rbf(float x) { return bf_lo(pack_bf16(x, x)); }
DI float sum_halves(float v) {
  auto r = __builtin_amdgcn_permlane32_swap(__float_as_uint(v), __float_as_uint(v), false, false);
  return __uint_as_float(r[0]) + __uint_as_float(r[1]);
}
DI float sum_rows(float v) {
  auto r = __builtin_amdgcn_permlane16_swap(__float_as_uint(v), __float_as_uint(v), false, false);
  return sum_halves(__uint_as_float(r[0]) + __uint_as_float(r[1]));
}
DI float max_halves(float v) {
  auto r = __builtin_amdgcn_permlane32_swap(__float_as_uint(v), __float_as_uint(v), false, false);
  return fmaxf(__uint_as_float(r[0]), __uint_as_float(r[1]));
}
template <int CTRL> DI float dpp_f(float v) { return __uint_as_float(__builtin_amdgcn_update_dpp(0u, __float_as_uint(v), CTRL, 0xf, 0xf, true)); }
template <int CTRL> DI unsigned dpp_u(unsigned v) { return __builtin_amdgcn_update_dpp(0u, v, CTRL, 0xf, 0xf, true); }
constexpr int DPP_X1 = 0xB1, DPP_X2 = 0x4E, DPP_HMIR = 0x141, DPP_MIR = 0x140;
DI float quad_sum(float v) { v += dpp_f<DPP_X1>(v); v += dpp_f<DPP_X2>(v); return v; }
DI float row16_sum(float v) { v = quad_sum(v); v += dpp_f<DPP_HMIR>(v); v += dpp_f<DPP_MIR>(v); return v; }
DI void glds16(const void* sbase, unsigned voff, unsigned lds_base) {
  unsigned sv;
  asm volatile("s_mov_b32 %0, m0\n\ts_mov_b32 m0, %3\n\ts_nop 0\n\tglobal_load_lds_dwordx4 %1, %2\n\ts_mov_b32 m0, %0"
               : "=&s"(sv) : "v"(voff), "s"(sbase), "s"(lds_base) : "memory");
}
DI void glds16s(const void* sbase, unsigned voff, unsigned lds_base) {
  unsigned sv;
  asm volatile("s_mov_b32 %0, m0\n\ts_mov_b32 m0, %3\n\ts_nop 0\n\tglobal_load_lds_dwordx4 %1, %2 sc1\n\ts_mov_b32 m0, %0"
               : "=&s"(sv) : "v"(voff), "s"(sbase), "s"(lds_base) : "memory");
}
DI void glds4(const void* sbase, unsigned voff, unsigned lds_base) {
  unsigned sv;
  asm volatile("s_mov_b32 %0, m0\n\ts_mov_b32 m0, %3\n\ts_nop 0\n\tglobal_load_lds_dword %1, %2\n\ts_mov_b32 m0, %0"
               : "=&s"(sv) : "v"(voff), "s"(sbase), "s"(lds_base) : "memory");
}
#define WAIT_VM(N) asm volatile("s_waitcnt vmcnt(" #N ")" ::: "memory")
#define WAIT_LGKM0() asm volatile("s_waitcnt lgkmcnt(0)" ::: "memory")
#define BARRIER() asm volatile("s_waitcnt lgkmcnt(0)\n\ts_barrier" ::: "memory")
typedef short v4i16_t __attribute__((ext_vector_type(4)));
DI bf16x4 tr_read(unsigned addr) {
  return __builtin_bit_cast(bf16x4, __builtin_amdgcn_ds_read_tr16_b64_v4i16((LAS v4i16_t*)(uintptr_t)addr));
}
DI bf16x8 lds_ld128(unsigned addr) { return *(const LAS bf16x8*)(uintptr_t)addr; }
DI f32x4 lds_ldf4(unsigned addr) { return *(const LAS f32x4*)(uintptr_t)addr; }
DI u32x4 lds_ldu4(unsigned addr) { return *(const LAS u32x4*)(uintptr_t)addr; }
DI void lds_stf4(unsigned addr, f32x4 v) { *(LAS f32x4*)(uintptr_t)addr = v; }
DI void lds_stu4(unsigned addr, u32x4 v) { *(LAS u32x4*)(uintptr_t)addr = v; }
DI void lds_stf(unsigned addr, float v) { *(LAS float*)(uintptr_t)addr = v; }
DI void lds_stu(unsigned addr, unsigned v) { *(LAS unsigned*)(uintptr_t)addr = v; }
DI float lds_ldf(unsigned addr) { return *(const LAS float*)(uintptr_t)addr; }
DI unsigned lds_ldu(unsigned addr) { return *(const LAS unsigned*)(uintptr_t)addr; }
DI bf16x8 cat8(bf16x4 a, bf16x4 b) { return __builtin_shufflevector(a, b, 0, 1, 2, 3, 4, 5, 6, 7); }
typedef __amdgpu_buffer_rsrc_t rsrc_t;
DI u32x4 ho_ld(rsrc_t rs, int off) { return __builtin_amdgcn_raw_buffer_load_b128(rs, off, 0, 16); }
DI void ho_st(rsrc_t rs, int off, u32x4 v) { __builtin_amdgcn_raw_buffer_store_b128(v, rs, off, 0, 16); }
DI void ho_st1(rsrc_t rs, int off, unsigned v) { __builtin_amdgcn_raw_buffer_store_b32(v, rs, off, 0, 16); }
DI unsigned san(unsigned v) { return min(v, SENT - 1u); }
#define POLL_PAUSE(n) do { __builtin_amdgcn_s_sleep(n); asm volatile("" ::: "memory"); } while (0)
DI bool pend(u32x4 v) { return max(max(v[0], v[1]), max(v[2], v[3])) == SENT; }

constexpr int LDS_BYTES = 163840;
constexpr int KVT = 2080;
constexpr int L_PRIV = 64 * KVT, PRIV = 2560;
constexpr int L_STG = L_PRIV + 8 * PRIV;
constexpr int L_RDY = L_STG + 4 * 2048;
constexpr int L_RS = L_RDY + 64;
static_assert(L_RS + 1024 <= LDS_BYTES, "lds map");
constexpr unsigned X_OFF = 32768, S_OFF = 45056, T_OFF = 55296, R_OFF = 57344, F_OFF = 81920;
constexpr int N_D1 = 86;
constexpr int CODE_PF_BYTES = 25 * 1024;
struct AFrag { bf16x8 x0, x1, q, k, v; };

#define EX(x) __builtin_amdgcn_exp2f(x)
#define PK(a, b) pack_bf16(a, b)
#define RCP(x) __builtin_amdgcn_rcpf(x)
#define UNIT(SC, SN, KN, QN, NEGN, OPREV, VPREV, OCUR, VCUR0, LOADS) do { \
    float e0 = EX(SC[0]), e1 = EX(SC[1]); SBAR(); \
    OPREV = MFMA32(VPREV, BC8(p1), OPREV); \
    LOADS; \
    SBAR(); \
    float e2 = EX(SC[2]), e3 = EX(SC[3]); SBAR(); \
    p0[0] = PK(e0, e1); SBAR(); float e4 = EX(SC[4]), e5 = EX(SC[5]); SBAR(); \
    p0[1] = PK(e2, e3); SBAR(); float e6 = EX(SC[6]), e7 = EX(SC[7]); SBAR(); \
    SN = MFMA32(KN, QN, NEGN); SBAR(); \
    p0[2] = PK(e4, e5); SBAR(); e0 = EX(SC[8]); e1 = EX(SC[9]); SBAR(); \
    p0[3] = PK(e6, e7); SBAR(); e2 = EX(SC[10]); e3 = EX(SC[11]); SBAR(); \
    OCUR = MFMA32(VCUR0, BC8(p0), OCUR); SBAR(); \
    p1[0] = PK(e0, e1); SBAR(); e4 = EX(SC[12]); e5 = EX(SC[13]); SBAR(); \
    p1[1] = PK(e2, e3); SBAR(); e6 = EX(SC[14]); e7 = EX(SC[15]); SBAR(); \
    p1[2] = PK(e4, e5); SBAR(); \
    p1[3] = PK(e6, e7); SBAR(); \
  } while (0)
#define TWO_TILES(U) do { \
    UNIT(sA, sB, k0, qb2, ng, ob, v11, oa, v00, k1 = lds_ld128(ka + (U + 1) * KVT); v10 = lds_ld128(va0 + (U + 1) * KVT)); \
    UNIT(sB, sA, k1, qa, ng, oa, v01, ob, v00, v11 = lds_ld128(va1 + (U + 1) * KVT)); \
    UNIT(sA, sB, k1, qb2, ng, ob, v01, oa, v10, k0 = lds_ld128(ka + (U + 2) * KVT); v00 = lds_ld128(va0 + (U + 2) * KVT)); \
    UNIT(sB, sA, k0, qa, ng, oa, v11, ob, v10, v01 = lds_ld128(va1 + (U + 2) * KVT)); \
  } while (0)

DI int fresh_lane() {
  int l;
  asm volatile("v_mbcnt_lo_u32_b32 %0, -1, 0\n\tv_mbcnt_hi_u32_b32 %0, -1, %0" : "=v"(l));
  return l;
}
#define RELANE() const int lane = fresh_lane(), tid = wid * 64 + lane, r32 = lane & 31, hh = lane >> 5, g = lane >> 4, i16 = lane & 15; \
  (void)tid; (void)r32; (void)hh; (void)g; (void)i16
DI float tile_max(const f32x16& s) {
  float m = fmaxf(fmaxf(s[0], s[1]), s[2]);
#pragma unroll
  for (int j = 3; j < 15; j += 2) m = fmaxf(fmaxf(m, s[j]), s[j + 1]);
  return fmaxf(m, s[15]);
}
DI unsigned sort_key(float r, int idx) {
  unsigned u = pack_bf16(r, r) & 0xffffu;
  if ((u & 0x7fffu) > 0x7f80u) u = 0xffffu;
  u = (u & 0x8000u) ? (~u & 0xffffu) : (u | 0x8000u);
  return (u << 11) | (unsigned)(2047 - idx);
}
DI float key_score(unsigned key) {
  unsigned u = (key >> 11) & 0xffffu;
  u = (u & 0x8000u) ? (u & 0x7fffu) : (~u & 0xffffu);
  return __uint_as_float(u << 16);
}
DI void ln_stats(float s1, float s2, float& mb, float& rden) {
  const float mean = s1 * (1.f / D);
  const float var = fmaxf(s2 * (1.f / D) - mean * mean, 0.f);
  mb = rbf(mean);
  rden = RCP(rbf(__builtin_amdgcn_sqrtf(rbf(rbf(var) + LN_EPS))));
}
DI float sigmoid(float v) { return RCP(1.f + EX(v * -1.4426950408889634f)); }
DI void wait_rdy(unsigned need) {
  for (unsigned spin = 0;; ++spin) {
    const u32x4 r = *(volatile const LAS u32x4*)(uintptr_t)L_RDY;
    const unsigned m = min(min(r[0], r[1]), min(r[2], r[3]));
    if (__builtin_amdgcn_readfirstlane(m) >= need || spin > (SPIN_CAP << 4)) break;
    __builtin_amdgcn_s_sleep(1);
  }
  asm volatile("" ::: "memory");
}
DI void wait_word(unsigned addr, unsigned need) {
  for (unsigned spin = 0;; ++spin) {
    const unsigned r = *(volatile const LAS unsigned*)(uintptr_t)addr;
    if (__builtin_amdgcn_readfirstlane(r) >= need || spin > (SPIN_CAP << 4)) break;
    __builtin_amdgcn_s_sleep(1);
  }
  asm volatile("" ::: "memory");
}
DI unsigned gt_bit(unsigned m, unsigned a, unsigned c) { return __builtin_amdgcn_alignbit(m, c - a, 31); }
DI unsigned count_gt(u32x4 a, u32x4 b, unsigned c) {
  unsigned m = 0, n = 0;
#pragma unroll
  for (int j = 0; j < 4; ++j) { m = gt_bit(m, a[j], c); n = gt_bit(n, b[j], c); }
  return __builtin_popcount(m) + __builtin_popcount(n);
}
DI unsigned max4(u32x4 v) { return max(max(v[0], v[1]), max(v[2], v[3])); }
template <int N> DI void poll_all(rsrc_t rs, int off, int stride, u32x4 (&v)[N], int back = 0) {
  for (unsigned spin = 0;; ++spin) {
#pragma unroll
    for (int j = 0; j < N; ++j) v[j] = ho_ld(rs, off + j * stride - (j == N - 1 ? back : 0));
    unsigned m = 0;
#pragma unroll
    for (int j = 0; j < N; ++j) m = max(m, max4(v[j]));
    if (__builtin_amdgcn_ballot_w64(m == SENT) == 0 || spin > SPIN_CAP) break;
    POLL_PAUSE(1);
  }
}
DI unsigned sum8_u(unsigned v) { v += dpp_u<DPP_X1>(v); v += dpp_u<DPP_X2>(v); v += dpp_u<DPP_HMIR>(v); return v; }

__global__ __launch_bounds__(768, 1) void k_fused(const uint16_t* __restrict__ x, const uint16_t* __restrict__ wqkv, const uint16_t* __restrict__ wo,
                                                   const uint16_t* __restrict__ wr, const uint16_t* __restrict__ wg, const uint16_t* __restrict__ wu,
                                                   const uint16_t* __restrict__ wd, char* __restrict__ ws, uint16_t* __restrict__ out) {
  extern __shared__ __attribute__((aligned(16))) char smem[];
  asm volatile("" :: "s"((unsigned)(uintptr_t)smem));
  const int tid = threadIdx.x, lane = tid & 63, wid = __builtin_amdgcn_readfirstlane(tid >> 6);
  const int b = blockIdx.x, xcd = b & 7, sub = b >> 3;
  const int r32 = lane & 31, hh = lane >> 5, g = lane >> 4, i16 = lane & 15;
  const rsrc_t rs = __builtin_amdgcn_make_buffer_rsrc(ws, 0, WS_BYTES, 0x00020000);
  const int rb = (xcd >> 1) * 8 + (sub & 7), hq = (xcd & 1) * 4 + (sub >> 3);
  const int head = xcd * 4 + (sub & 3), qb = sub >> 2;
  const int tt = xcd * 8 + (sub >> 2), cbk = sub & 3;
  const int d1 = (xcd < 7 && sub < 13) ? xcd * 13 + sub : 999;
  const bool isd1 = d1 < N_D1, isd2 = xcd == 7;
  STAMP_DECL; STAMP(0);
  unsigned code_pf = 0;
  if (wid == 7) {
    const unsigned long long pc = __builtin_amdgcn_s_getpc() & ~0xFFull;
    const unsigned off = min((unsigned)((sub & 3) * 8192 + lane * 128), (unsigned)(CODE_PF_BYTES - 128));
    asm volatile("global_load_dword %0, %1, %2" : "=v"(code_pf) : "v"(off), "s"(pc) : "memory");
  }

  if (__builtin_expect(wid >= 8, 1)) {
    const int hw = wid - 8;
    __builtin_amdgcn_s_setprio(3);
    {
      const int rot = ((sub & 7) + 2 * (sub >> 3)) & 7;
      const char* base; unsigned vo0, vo1, cstr, pstr;
      if (hw < 3) {
        base = (const char*)wqkv + hw * 1024 + hq * 128; cstr = 64 * 3072; pstr = 8 * 3072;
        vo0 = (lane >> 3) * 3072 + (((lane & 7) ^ ((g & 1) << 1)) * 16); vo1 = (lane >> 3) * 3072 + (((lane & 7) ^ (((g & 1) | 2) << 1)) * 16);
      } else {
        base = (const char*)x + (size_t)rb * 65536; cstr = 128; pstr = 8192;
        vo0 = (lane >> 3) * 1024 + (((lane & 7) ^ g) * 16); vo1 = (lane >> 3) * 1024 + (((lane & 7) ^ (4 + g)) * 16);
      }
      auto issue = [&](int c) {
        const char* src = base + (size_t)((c + rot) & 7) * cstr;
        const unsigned dst = (c & 3) * 32768 + hw * 8192;
#pragma unroll
        for (int p = 0; p < 8; ++p) glds16(src + (size_t)p * pstr, (p & 1) ? vo1 : vo0, dst + p * 1024);
      };
#pragma unroll 1
      for (int c = -3; c < 7; ++c) {
        if (c >= -1) {
          if (c <= 5) { WAIT_VM(8); } else { WAIT_VM(0); }
          BARRIER();
        }
        if (c + 3 < 8) issue(c + 3);
      }
    }
    unsigned tag = __builtin_amdgcn_raw_buffer_load_b32(rs, WS_EP, 0, 16);
    tag = __builtin_amdgcn_readfirstlane(tag) + 1u;
    BARRIER();
    STAMP(4);
    if (lane < 32) {
      const unsigned o = (lane & 1) ? 0u : 0x3f803f80u;
      lds_stu4((hw * 16 + (lane >> 1)) * KVT + 2048 + (lane & 1) * 16, u32x4{o, o, o, o});
    }
    wait_word(L_RDY + 36, 8u);
    for (unsigned spin = 0;; ++spin) {
      const u32x2 f = __builtin_amdgcn_raw_buffer_load_b64(rs, WS_FA + (head * 64 + ((lane >> 1) & 7) * 8 + hw * 2 + (lane & 1)) * 128, 0, 16);
      if (__all(f[0] == tag && f[1] == ~tag) || spin > SPIN_CAP) break;
      POLL_PAUSE(1);
    }
    STAMP(5);
    {
      const char* kvg = ws + WS_KV + (size_t)head * (64 * 2048) + hw * 4096;
      const char* qsrc = ws + WS_Q + (size_t)(head * 64 + qb * 8 + hw * 2) * 1024;
      auto kv_step = [&](int st) {
#pragma unroll
        for (int hf = 0; hf < 2; ++hf)
#pragma unroll
          for (int p = 0; p < 2; ++p)
            glds16s(kvg + (st >> 1) * 16384 + (st & 1) * 2048 + hf * 65536 + p * 1024, lane * 16, (hw + st * 4 + hf * 32) * KVT + p * 1024);
      };
      glds16s(qsrc, lane * 16, L_STG + hw * 2048); glds16s(qsrc + 1024, lane * 16, L_STG + hw * 2048 + 1024);
      kv_step(0); kv_step(1);
      asm volatile("s_waitcnt vmcnt(4) lgkmcnt(0)" ::: "memory");
      lds_stu(L_RDY + hw * 4, 1u);
      kv_step(2); kv_step(3);
      WAIT_VM(8);
      lds_stu(L_RDY + hw * 4, 2u);
      kv_step(4); kv_step(5);
      WAIT_VM(8);
      lds_stu(L_RDY + hw * 4, 4u);
      kv_step(6); kv_step(7);
      WAIT_VM(0);
      lds_stu(L_RDY + hw * 4, 8u);
    }
    STAMP(6);
    if (hw == 0) {
      const u32x4 sv = {SENT, SENT, SENT, SENT};
#pragma unroll 1
      for (int j = 0; j < 19; ++j) __builtin_amdgcn_raw_buffer_store_b128(sv, rs, lane * 16, WS_HO + b * HO_PER_WG + j * 1024, 16);
      WAIT_VM(0);
      if (lane == 0) __builtin_amdgcn_raw_buffer_store_b64(u32x2{tag, ~tag}, rs, WS_FR + b * 8, 0, 16);
    }
    bf16x8 wf[32];
    float swr = 0.f;
    f32x16 acc;
    {
      const char* wsrc = (const char*)wo + (lane >> 2) * 1024 + cbk * 256 + hw * 64 + (lane & 3) * 16;
#pragma unroll
      for (int i = 0; i < 32; ++i) wf[i] = *(const bf16x8*)(wsrc + i * 16384);
      const unsigned stg = L_STG + hw * 2048;
      const unsigned wor = stg + (4 * (g >> 1) + (i16 >> 2)) * 64 + (g & 1) * 32 + (i16 & 3) * 8;
      wait_word(L_RDY + 16 + hw * 4, 2u);
#pragma unroll
      for (int i = 0; i < 32; ++i) {
        *(LAS bf16x8*)(uintptr_t)(stg + (i & 1) * 1024 + lane * 16) = wf[i];
        WAIT_LGKM0();
        wf[i] = cat8(tr_read(wor + (i & 1) * 1024), tr_read(wor + (i & 1) * 1024 + 512));
      }
      WAIT_LGKM0();
      const char* xs = (const char*)x + ((tt * 32 + r32) * D + cbk * 128 + hw * 32 + 4 * hh) * 2;
#pragma unroll
      for (int gq = 0; gq < 4; ++gq) {
        const u32x2 xw = *(const u32x2*)(xs + gq * 16);
        acc[4 * gq] = bf_lo(xw[0]); acc[4 * gq + 1] = bf_hi(xw[0]); acc[4 * gq + 2] = bf_lo(xw[1]); acc[4 * gq + 3] = bf_hi(xw[1]);
      }
      lds_stf(stg + r32 * 4, bf_lo(wr[cbk * 128 + hw * 32 + r32]));
      const u32x4 wra = *(const u32x4*)((const char*)wr + lane * 16);
#pragma unroll
      for (int j = 0; j < 4; ++j) swr += bf_lo(wra[j]) + bf_hi(wra[j]);
      swr = __uint_as_float(__builtin_amdgcn_readfirstlane(__float_as_uint(sum_rows(row16_sum(swr)))));
      PIN(acc);
      if (isd1) {
#pragma unroll 1
        for (int p = 0; p < 4; ++p) glds4(hw < 2 ? wg : wu, (((hw & 1) * 4 + p) * 64 + lane) * (F * 2) + d1 * 32, stg + 1024);
      } else if (isd2) {
#pragma unroll 1
        for (int p = hw; p < 22; p += 4) glds4(wd, min(p * 64 + lane, F - 1) * (D * 2) + sub * 32, stg + 1024);
      }
      WAIT_VM(0);
    }
    STAMP(7);
    if (hw == 0) {
      for (unsigned spin = 0;; ++spin) {
        const u32x4 f0 = ho_ld(rs, WS_FR + lane * 16), f1 = ho_ld(rs, WS_FR + 1024 + lane * 16);
        const bool okf = f0[0] == tag && f0[1] == ~tag && f0[2] == tag && f0[3] == ~tag && f1[0] == tag && f1[1] == ~tag && f1[2] == tag && f1[3] == ~tag;
        if (__all(okf) || spin > SPIN_CAP) break;
        POLL_PAUSE(8);
      }
    }
    STAMP(8);
    BARRIER();
    {
      RELANE();
      const unsigned ol = lane * 16;
      const int sto = HO_ST + (tt * 16 + cbk * 4 + hw) * 512 + r32 * 16;
      const unsigned ha = X_OFF + r32 * 272 + (hw * 32 + 4 * hh) * 2, wa = L_STG + hw * 2048 + 4 * hh * 4;
      f32x4 wv[4];
      auto wait_bit = [&](int bit) {
        for (unsigned spin = 0;; ++spin) {
          const unsigned m = __builtin_amdgcn_readfirstlane(*(volatile const LAS unsigned*)(uintptr_t)(L_RDY + 32));
          if (((m >> bit) & 1u) || spin > (SPIN_CAP << 4)) break;
          __builtin_amdgcn_s_sleep(1);
        }
        asm volatile("" ::: "memory");
      };
      bf16x8 of[4], nf[4];
      wait_bit(0);
#pragma unroll
      for (int i = 0; i < 4; ++i) of[i] = lds_ld128(ol + i * 1024);
#pragma unroll
      for (int w2 = 0; w2 < 8; ++w2) {
        unsigned mk = 0;
        if (w2 >= 1 && w2 < 7) {
          mk = *(volatile const LAS unsigned*)(uintptr_t)(L_RDY + 32);
          asm volatile("" ::: "memory");
#pragma unroll
          for (int i = 0; i < 4; ++i) nf[i] = lds_ld128(ol + (w2 * 4 + 4 + i) * 1024);
        }
        if (w2 == 6) {
#pragma unroll
          for (int gq = 0; gq < 4; ++gq) wv[gq] = lds_ldf4(wa + gq * 32);
        }
        SBAR();
#pragma unroll
        for (int i = 0; i < 4; ++i) acc = MFMA32(wf[w2 * 4 + i], of[i], acc);
        SBAR();
        if (w2 < 7 && !((__builtin_amdgcn_readfirstlane(mk) >> (w2 + 1)) & 1u)) {
          wait_bit(w2 + 1);
#pragma unroll
          for (int i = 0; i < 4; ++i) nf[i] = lds_ld128(ol + (w2 * 4 + 4 + i) * 1024);
        }
#pragma unroll
        for (int i = 0; i < 4; ++i) of[i] = nf[i];
      }
      STAMP(9);
      f32x2 t1 = {0.f, 0.f}, t2 = t1, t3 = t1;
#pragma unroll
      for (int j = 0; j < 8; ++j) {
        const f32x2 v = {acc[2 * j], acc[2 * j + 1]}, w = {wv[j >> 1][2 * (j & 1)], wv[j >> 1][2 * (j & 1) + 1]};
        t1 += v; t2 += v * v; t3 += v * w;
      }
      const float s1 = sum_halves(t1[0] + t1[1]), s2 = sum_halves(t2[0] + t2[1]), s3 = sum_halves(t3[0] + t3[1]);
      const u32x4 rec = {san(__float_as_uint(s1)), san(__float_as_uint(s2)), san(__float_as_uint(s3)), 0u};
      if (lane < 32) ho_st(rs, sto, rec);
      lds_stu4(S_OFF + (cbk * 4 + hw) * 528 + r32 * 16, rec);
#pragma unroll
      for (int gq = 0; gq < 4; ++gq)
        *(LAS u32x2*)(uintptr_t)(ha + gq * 16) = u32x2{pack_bf16(acc[4 * gq], acc[4 * gq + 1]), pack_bf16(acc[4 * gq + 2], acc[4 * gq + 3])};
    }
    BARRIER();
    if (hw == 0) {
      RELANE();
      f32x4 t = lds_ldf4(S_OFF + hh * 8 * 528 + r32 * 16);
#pragma unroll
      for (int j = 1; j < 8; ++j) t += lds_ldf4(S_OFF + (hh * 8 + j) * 528 + r32 * 16);
      const float s1 = sum_halves(t[0]), s2 = sum_halves(t[1]), s3 = sum_halves(t[2]);
      float mbv, rden;
      ln_stats(s1, s2, mbv, rden);
      ho_st1(rs, HO_KEY + (2 * cbk + hh) * (S * 4) + (tt * 32 + r32) * 4, sort_key((s3 - mbv * swr) * rden, tt * 32 + r32));
    }
    STAMP(11);
    {
      constexpr unsigned G = T_OFF, M1 = T_OFF + 256, C2 = T_OFF + 512, SEL = T_OFF + 640, C1 = T_OFF + 1024;
      const int ht = hw * 64 + fresh_lane(), t3 = ht & 3, t4 = ht >> 2;
      u32x4 kv[2];
      poll_all<2>(rs, HO_KEY + xcd * (S * 4) + ht * 32, 16, kv);
      STAMP(12);
      const unsigned m0 = max4(kv[0]), m1 = max4(kv[1]);
      unsigned gm = max(m0, m1); gm = max(gm, dpp_u<DPP_X1>(gm)); gm = max(gm, dpp_u<DPP_X2>(gm));
      lds_stu(G + t4 * 4, gm);
      BARRIER();
      {
        unsigned rk = count_gt(lds_ldu4(G + t3 * 64), lds_ldu4(G + t3 * 64 + 16), gm) + count_gt(lds_ldu4(G + t3 * 64 + 32), lds_ldu4(G + t3 * 64 + 48), gm);
        rk += dpp_u<DPP_X1>(rk); rk += dpp_u<DPP_X2>(rk);
        if (rk < 8) {
          lds_stu4(C1 + (rk * 8 + t3 * 2) * 16, kv[0]); lds_stu4(C1 + (rk * 8 + t3 * 2 + 1) * 16, kv[1]);
          *(LAS u32x2*)(uintptr_t)(M1 + (rk * 8 + t3 * 2) * 4) = u32x2{m0, m1};
        }
      }
      BARRIER();
      {
        const unsigned cm = lds_ldu(M1 + t4 * 4), key = lds_ldu(C1 + ht * 4);
        unsigned rk = count_gt(lds_ldu4(M1 + t3 * 64), lds_ldu4(M1 + t3 * 64 + 16), cm) + count_gt(lds_ldu4(M1 + t3 * 64 + 32), lds_ldu4(M1 + t3 * 64 + 48), cm);
        rk += dpp_u<DPP_X1>(rk); rk += dpp_u<DPP_X2>(rk);
        if (rk < 8) lds_stu(C2 + rk * 16 + t3 * 4, key);
      }
      BARRIER();
      {
        const unsigned key = lds_ldu(C2 + (ht >> 3) * 4);
        const u32x4 o = lds_ldu4(C2 + (ht & 7) * 16);
        const unsigned rk = sum8_u(((key - o[0]) >> 31) + ((key - o[1]) >> 31) + ((key - o[2]) >> 31) + ((key - o[3]) >> 31));
        if (rk < 8) lds_stu(SEL + rk * 4, key);
      }
      BARRIER();
    }
    STAMP(15); STAMP_OUT(0);
    return;
  }

  unsigned tag;
  {
    const int rt = wid & 1, hd = wid >> 1, sw = hd >> 1, par = rt ^ (hd & 1);
    tag = __builtin_amdgcn_raw_buffer_load_b32(rs, WS_EP, 0, 16);
    const int xr0 = rt * 32 + sw * 16 + i16, xr1 = xr0 ^ 16;
    const int xf = (i16 >> 1) & 7;
    const unsigned xb0 = 24576 + (xr0 >> 3) * 1024 + (xr0 & 7) * 128, xb1 = 24576 + (xr1 >> 3) * 1024 + (xr1 & 7) * 128;
    const unsigned xoA = ((4 * par + g) ^ xf) * 16, xoB = ((4 * (par ^ 1) + g) ^ xf) * 16;
    const unsigned wl = (8 * g + (i16 >> 2)) * 128 + ((hd ^ (((i16 >> 3) & 1) | ((g & 1) << 1))) * 32) + (i16 & 3) * 8;
    unsigned x0A = xb0 + xoA, x1A = xb1 + xoA, x0B = xb0 + xoB, x1B = xb1 + xoB, wAa = wl + par * 4096, wBa = wl + (par ^ 1) * 4096;
#define RDX(a, so) lds_ld128((a) + (so))
#define RDW(a, so, t) cat8(tr_read((a) + (so) + (t) * 8192), tr_read((a) + (so) + (t) * 8192 + 512))
    f32x4 aq0 = {}, aq1 = {}, ak0 = {}, ak1 = {}, av0 = {}, av1 = {};
    f32x4 rsum = {}, rsq = {}, cs0 = {}, cs1 = {}, cs2 = {};
    bf16x8 ones8 = {0x3f80, 0x3f80, 0x3f80, 0x3f80, 0x3f80, 0x3f80, 0x3f80, 0x3f80};
    PIN(ones8);
    if (tid < 16) lds_stu(L_RDY + tid * 4, 0u);
    const unsigned pv = L_PRIV + wid * PRIV, po = L_PRIV + (wid ^ 1) * PRIV;
    unsigned ad_rs = (g == (i16 >> 2)) ? L_RS + ((rt * 4 + hd) * 16 + i16) * 8 : pv + lane * 16;
    unsigned ad_cq = (i16 == 0) ? po + 1280 + g * 16 : pv + lane * 16, ad_cv = po + 1280 + 128 + i16 * 4;
    unsigned rd_rs = L_RS + ((rt * 4 + (g & 1) * 2) * 16 + i16) * 8, rd_cq = pv + 1280 + g * 16, rd_cv = pv + 1280 + 128 + i16 * 4;
    unsigned ad_mr = pv + 2048 + r32 * 4, rd_mr = pv + 2048 + (sw * 16 + 4 * g) * 4;
    const int ahead = hq * 4 + hd, tile = rb * 2 + rt;
    unsigned go_k = WS_KV + (ahead * 64 + tile) * 2048 + (lane ^ (sw * 16)) * 16, go_q = WS_Q + (ahead * 64 + tile) * 1024 + (lane ^ (sw * 16)) * 16;
    unsigned go_v = WS_KV + (ahead * 64 + tile) * 2048 + 1024 + (lane ^ (sw * 32)) * 16;
    PIN(ad_rs); PIN(ad_cq); PIN(ad_cv); PIN(rd_rs); PIN(rd_cq); PIN(rd_cv); PIN(ad_mr); PIN(rd_mr); PIN(go_k); PIN(go_q); PIN(go_v);
    PIN(x0A); PIN(x1A); PIN(x0B); PIN(x1B); PIN(wAa); PIN(wBa);
    BARRIER();
    STAMP(1);
    AFrag fa, fb;
    fa.q = RDW(wAa, 0, 0); fa.x0 = RDX(x0A, 0); fa.x1 = RDX(x1A, 0); fa.k = RDW(wAa, 0, 1); fa.v = RDW(wAa, 0, 2);
    fb.q = RDW(wBa, 0, 0); fb.x0 = RDX(x0B, 0); fb.x1 = RDX(x1B, 0); fb.k = RDW(wBa, 0, 1); fb.v = RDW(wBa, 0, 2);
#pragma unroll
    for (int c = 0; c < 7; ++c) {
      asm volatile("s_barrier" ::: "memory");
      FSTAMP(16 + c);
      if (((c + 1) & 1) == 0) {
        const unsigned d = ((c + 1) & 2) ? 65536u : (unsigned)-65536;
        x0A += d; x1A += d; x0B += d; x1B += d; wAa += d; wBa += d;
        PIN(x0A); PIN(x1A); PIN(x0B); PIN(x1B); PIN(wAa); PIN(wBa);
      }
      const unsigned so = ((c + 1) & 1) * 32768;
      SBAR();
      aq0 = MFMA16(fa.q, fa.x0, aq0); aq1 = MFMA16(fa.q, fa.x1, aq1);
      cs0 = MFMA16(fa.q, ones8, cs0);
      SBAR(); fa.q = RDW(wAa, so, 0); SBAR();
      ak0 = MFMA16(fa.k, fa.x0, ak0); ak1 = MFMA16(fa.k, fa.x1, ak1);
      cs1 = MFMA16(fa.k, ones8, cs1);
      SBAR(); fa.k = RDW(wAa, so, 1); SBAR();
      av0 = MFMA16(fa.x0, fa.v, av0); av1 = MFMA16(fa.x1, fa.v, av1);
      SBAR(); fa.x1 = RDX(x1A, so); SBAR();
      cs2 = MFMA16(ones8, fa.v, cs2); rsum = MFMA16(ones8, fa.x0, rsum); rsq = MFMA16(fa.x0, fa.x0, rsq);
      SBAR(); fa.x0 = RDX(x0A, so); fa.v = RDW(wAa, so, 2); SBAR();
      aq0 = MFMA16(fb.q, fb.x0, aq0); aq1 = MFMA16(fb.q, fb.x1, aq1);
      SBAR(); fb.q = RDW(wBa, so, 0); SBAR();
      ak0 = MFMA16(fb.k, fb.x0, ak0); ak1 = MFMA16(fb.k, fb.x1, ak1);
      SBAR(); fb.k = RDW(wBa, so, 1); SBAR();
      av0 = MFMA16(fb.x0, fb.v, av0); av1 = MFMA16(fb.x1, fb.v, av1);
      SBAR(); fb.x0 = RDX(x0B, so); fb.x1 = RDX(x1B, so); fb.v = RDW(wBa, so, 2); SBAR();
    }
    rsum = MFMA16(ones8, fa.x0, rsum); rsq = MFMA16(fa.x0, fa.x0, rsq);
    cs0 = MFMA16(fa.q, ones8, cs0); cs1 = MFMA16(fa.k, ones8, cs1); cs2 = MFMA16(ones8, fa.v, cs2);
    SBAR();
    ak0 = MFMA16(fa.k, fa.x0, ak0); ak1 = MFMA16(fa.k, fa.x1, ak1);
    SBAR();
    {
      const bool b0 = lane & 1, b1 = lane & 2;
      const float dlo = b0 ? rsq[1] : rsq[0], dhi = b0 ? rsq[3] : rsq[2];
      *(LAS f32x2*)(uintptr_t)ad_rs = f32x2{rsum[0], b1 ? dhi : dlo};
      lds_stf4(ad_cq, cs0); lds_stf4(ad_cq + 64, cs1); lds_stf(ad_cv, cs2[0]);
    }
    STAMP(2);
    BARRIER();
    {
      const f32x2 pa = *(const LAS f32x2*)(uintptr_t)rd_rs, pb = *(const LAS f32x2*)(uintptr_t)(rd_rs + 128);
      const f32x4 oq = lds_ldf4(rd_cq), ok = lds_ldf4(rd_cq + 64);
      const float ov = lds_ldf(rd_cv);
      SBAR();
      ak0 = MFMA16(fb.k, fb.x0, ak0); ak1 = MFMA16(fb.k, fb.x1, ak1);
      aq0 = MFMA16(fa.q, fa.x0, aq0); aq1 = MFMA16(fa.q, fa.x1, aq1);
      SBAR();
      float mb, rden;
      ln_stats(pa[0] + pb[0], pa[1] + pb[1], mb, rden);
      aq0 = MFMA16(fb.q, fb.x0, aq0); aq1 = MFMA16(fb.q, fb.x1, aq1);
      av0 = MFMA16(fa.x0, fa.v, av0); av1 = MFMA16(fa.x1, fa.v, av1);
      av0 = MFMA16(fb.x0, fb.v, av0); av1 = MFMA16(fb.x1, fb.v, av1);
      lds_stf(ad_mr, mb); lds_stf(ad_mr + 128, rden);
      const f32x4 cq = cs0 + oq, ck = cs1 + ok;
      const float cv = cs2[0] + ov;
      const auto sm = __builtin_amdgcn_permlane16_swap(__float_as_uint(mb), __float_as_uint(mb), false, false);
      const auto sd = __builtin_amdgcn_permlane16_swap(__float_as_uint(rden), __float_as_uint(rden), false, false);
      const float m0 = __uint_as_float(sw ? sm[1] : sm[0]), m1 = __uint_as_float(sw ? sm[0] : sm[1]);
      const float d0 = __uint_as_float(sw ? sd[1] : sd[0]), d1v = __uint_as_float(sw ? sd[0] : sd[1]);
      WAIT_LGKM0();
      const f32x4 mv0 = lds_ldf4(rd_mr), mv1 = lds_ldf4(rd_mr ^ 64), dv0 = lds_ldf4(rd_mr + 128), dv1 = lds_ldf4((rd_mr ^ 64) + 128);
      unsigned a0, a1, b0, b1;
      a0 = pack_bf16((ak0[0] - m0 * ck[0]) * d0, (ak0[1] - m0 * ck[1]) * d0); a1 = pack_bf16((ak0[2] - m0 * ck[2]) * d0, (ak0[3] - m0 * ck[3]) * d0);
      b0 = pack_bf16((ak1[0] - m1 * ck[0]) * d1v, (ak1[1] - m1 * ck[1]) * d1v); b1 = pack_bf16((ak1[2] - m1 * ck[2]) * d1v, (ak1[3] - m1 * ck[3]) * d1v);
      { auto r0 = __builtin_amdgcn_permlane16_swap(a0, b0, false, false); auto r1 = __builtin_amdgcn_permlane16_swap(a1, b1, false, false);
        ho_st(rs, go_k, u32x4{r0[0], r1[0], r0[1], r1[1]}); }
      const float e0 = d0 * QSCALE, e1 = d1v * QSCALE;
      a0 = pack_bf16((aq0[0] - m0 * cq[0]) * e0, (aq0[1] - m0 * cq[1]) * e0); a1 = pack_bf16((aq0[2] - m0 * cq[2]) * e0, (aq0[3] - m0 * cq[3]) * e0);
      b0 = pack_bf16((aq1[0] - m1 * cq[0]) * e1, (aq1[1] - m1 * cq[1]) * e1); b1 = pack_bf16((aq1[2] - m1 * cq[2]) * e1, (aq1[3] - m1 * cq[3]) * e1);
      { auto r0 = __builtin_amdgcn_permlane16_swap(a0, b0, false, false); auto r1 = __builtin_amdgcn_permlane16_swap(a1, b1, false, false);
        ho_st(rs, go_q, u32x4{r0[0], r1[0], r0[1], r1[1]}); }
      a0 = pack_bf16((av0[0] - mv0[0] * cv) * dv0[0], (av0[1] - mv0[1] * cv) * dv0[1]); a1 = pack_bf16((av0[2] - mv0[2] * cv) * dv0[2], (av0[3] - mv0[3] * cv) * dv0[3]);
      b0 = pack_bf16((av1[0] - mv1[0] * cv) * dv1[0], (av1[1] - mv1[1] * cv) * dv1[1]); b1 = pack_bf16((av1[2] - mv1[2] * cv) * dv1[2], (av1[3] - mv1[3] * cv) * dv1[3]);
      { auto r0 = __builtin_amdgcn_permlane32_swap(a0, b0, false, false); auto r1 = __builtin_amdgcn_permlane32_swap(a1, b1, false, false);
        ho_st(rs, go_v, u32x4{r0[0], r1[0], r0[1], r1[1]}); }
    }
    tag = __builtin_amdgcn_readfirstlane(tag) + 1u;
    STAMP(3);
    asm volatile("s_waitcnt vmcnt(0)" : "+v"(code_pf) :: "memory");
    if (lane == 0) {
      __builtin_amdgcn_raw_buffer_store_b64(u32x2{tag, ~tag}, rs, WS_FA + (ahead * 64 + tile) * 128, 0, 16);
      __hip_atomic_fetch_add((LAS unsigned*)(uintptr_t)(L_RDY + 36), 1u, __ATOMIC_RELAXED, __HIP_MEMORY_SCOPE_WORKGROUP);
    }
    STAMP(4);
  }

  const int qg = wid & 3, kh = wid >> 2;
  u32x4 fw0 = {}, fw1 = {}, fw2 = {}, fw3 = {}, fw4 = {}, fw5 = {};
  const char* fb0 = isd1 ? (const char*)wg + wid * (64 * F * 2) + d1 * 32 : (const char*)wd + wid * (32 * D * 2) + sub * 32;
  const char* fb2 = isd1 ? (const char*)wu + wid * (64 * F * 2) + d1 * 32 : fb0 + 2 * (256 * D * 2);
  const unsigned fst = isd1 ? 32 * F * 2 : 256 * D * 2;
  const unsigned fs4 = isd1 ? 0u : 2 * fst, fs5 = (isd1 || wid >= 3) ? 0u : 3 * fst;
  unsigned fvo = (lane >> 1) * (isd1 ? F * 2 : D * 2) + (lane & 1) * 16;
  const int fit = (isd1 || isd2) ? 6 : -1;
  f32x16 oa = {}, ob = {};
  bf16x8 qa, qb2;
  float mref;
  {
    unsigned ka = kh * 32 * KVT + lane * 16;
    unsigned va0 = kh * 32 * KVT + (r32 < 16 ? 1024 + (hh * 16 + r32) * 16 : (r32 == 16 ? 2048 : 2064));
    unsigned va1 = va0 + (r32 < 16 ? 512 : 0);
    unsigned qad = L_STG + qg * 2048 + kh * 1024 + lane * 16;
    u32x4 p0, p1 = {0, 0, 0, 0};
    PIN(ka); PIN(va0); PIN(va1); PIN(qad); PIN(fvo); PIN(oa); PIN(ob); PIN(p1);
    PIN(fw0); PIN(fw1); PIN(fw2); PIN(fw3); PIN(fw4); PIN(fw5);
    __builtin_amdgcn_s_setprio(2);
    wait_rdy(1u);
    STAMP(6);
    bf16x8 k0 = lds_ld128(ka), k1;
    qa = lds_ld128(qad); qb2 = lds_ld128(qad ^ 1024);
    bf16x8 v00 = lds_ld128(va0), v01 = lds_ld128(va1), v10, v11 = lds_ld128(va0);
    f32x16 ng, sA, sB;
    {
      const f32x16 z = {};
      sA = MFMA32(k0, qa, z); sB = MFMA32(k0, qb2, z);
      mref = max_halves(fmaxf(tile_max(sA), tile_max(sB)));
    }
#pragma unroll
    for (int j = 0; j < 16; ++j) { ng[j] = -mref; sA[j] -= mref; }
    PIN(ng); PIN(sA);
    for (int it = 0; it < 8; ++it) {
      FSTAMP(32 + 2 * it);
      const unsigned need = (0x8042u >> (4 * it)) & 15u;
      if (need) {
        wait_rdy(need);
        if (it == 0 && fresh_lane() == 0) __hip_atomic_fetch_add((LAS unsigned*)(uintptr_t)(L_RDY + 16 + qg * 4), 1u, __ATOMIC_RELAXED, __HIP_MEMORY_SCOPE_WORKGROUP);
      }
      { unsigned tv_;
        asm volatile("s_cmp_lg_u32 %[it], %[fit]\n\ts_cbranch_scc1 .Lff%=\n\t"
          "global_load_dwordx4 %[a0], %[vo], %[b0]\n\tglobal_load_dwordx4 %[a2], %[vo], %[b2]\n\tv_add_u32 %[tv], %[st], %[vo]\n\t"
          "global_load_dwordx4 %[a1], %[tv], %[b0]\n\tglobal_load_dwordx4 %[a3], %[tv], %[b2]\n\tv_add_u32 %[tv], %[s4], %[vo]\n\t"
          "global_load_dwordx4 %[a4], %[tv], %[b2]\n\tv_add_u32 %[tv], %[s5], %[vo]\n\t"
          "global_load_dwordx4 %[a5], %[tv], %[b2]\n.Lff%=:\n\t"
          "s_cmp_lt_u32 %[it], 5\n\ts_cbranch_scc1 .Lpr%=\n\ts_setprio 1\n\ts_cmp_lt_u32 %[it], 7\n\ts_cbranch_scc1 .Lpr%=\n\ts_setprio 0\n.Lpr%=:"
          : [a0] "+v"(fw0), [a1] "+v"(fw1), [a2] "+v"(fw2), [a3] "+v"(fw3), [a4] "+v"(fw4), [a5] "+v"(fw5), [tv] "=&v"(tv_)
          : [it] "s"(it), [fit] "s"(fit), [vo] "v"(fvo), [b0] "s"(fb0), [b2] "s"(fb2), [st] "s"(fst), [s4] "s"(fs4), [s5] "s"(fs5) : "memory", "scc"); }
      SBAR();
      TWO_TILES(0);
      TWO_TILES(2);
      ka += 4 * KVT; va0 += 4 * KVT; va1 += 4 * KVT;
    }
    ob = MFMA32(v11, BC8(p1), ob);
  }
  asm volatile("s_waitcnt vmcnt(0)" : "+v"(fw0), "+v"(fw1), "+v"(fw2), "+v"(fw3), "+v"(fw4), "+v"(fw5) :: "memory");
  {
  RELANE();
  const unsigned priv = L_PRIV + wid * PRIV;
  float ma = mref, mb = mref;
  float la = sum_halves(oa[8]), lb = sum_halves(ob[8]);
  {
    float t = 0.f;
#pragma unroll
    for (int j = 0; j < 8; ++j) t += fabsf(oa[j]) + fabsf(ob[j]);
    const bool ok = (la <= 16777216.f) && (lb <= 16777216.f) && (la >= 5.9604645e-8f) && (lb >= 5.9604645e-8f) && (t <= 3.0e38f);
    if (__builtin_expect(!__all(ok), 0)) {
      const unsigned kaddr = kh * 32 * KVT + lane * 16;
      const unsigned vaddr0 = kh * 32 * KVT + (r32 < 16 ? 1024 + (hh * 16 + r32) * 16 : (r32 == 16 ? 2048 : 2064));
      const unsigned vaddr1 = vaddr0 + (r32 < 16 ? 512 : 0);
      const f32x16 z = {};
      ma = -INFINITY; mb = -INFINITY;
      for (int t2 = 0; t2 < 32; ++t2) {
        const bf16x8 kf = lds_ld128(kaddr + t2 * KVT);
        ma = fmaxf(ma, tile_max(MFMA32(kf, qa, z)));
        mb = fmaxf(mb, tile_max(MFMA32(kf, qb2, z)));
      }
      ma = max_halves(ma); mb = max_halves(mb);
      oa = z; ob = z;
      for (int t2 = 0; t2 < 32; ++t2) {
        const bf16x8 kf = lds_ld128(kaddr + t2 * KVT), v0 = lds_ld128(vaddr0 + t2 * KVT), v1 = lds_ld128(vaddr1 + t2 * KVT);
        const f32x16 sa = MFMA32(kf, qa, z), sb = MFMA32(kf, qb2, z);
        u32x4 pa0, pa1, pb0, pb1;
#pragma unroll
        for (int j = 0; j < 4; ++j) {
          pa0[j] = PK(EX(sa[2 * j] - ma), EX(sa[2 * j + 1] - ma)); pa1[j] = PK(EX(sa[8 + 2 * j] - ma), EX(sa[9 + 2 * j] - ma));
          pb0[j] = PK(EX(sb[2 * j] - mb), EX(sb[2 * j + 1] - mb)); pb1[j] = PK(EX(sb[8 + 2 * j] - mb), EX(sb[9 + 2 * j] - mb));
        }
        oa = MFMA32(v0, BC8(pa0), oa); oa = MFMA32(v1, BC8(pa1), oa);
        ob = MFMA32(v0, BC8(pb0), ob); ob = MFMA32(v1, BC8(pb1), ob);
      }
      la = sum_halves(oa[8]); lb = sum_halves(ob[8]);
    }
  }
  STAMP(7);
  const int row = tid >> 4, c8 = tid & 15;
  const int goff = ((tt * 32 + row) * D + cbk * 128 + c8 * 8) * 2;
  const int po_probe = HO_O + (tt * H + wid * 4 + (lane & 3)) * 1024 + 1020;
  unsigned probe;
  {
    lds_stf4(priv + lane * 16, f32x4{ob[0], ob[1], ob[2], ob[3]}); lds_stf4(priv + 1024 + lane * 16, f32x4{ob[4], ob[5], ob[6], ob[7]});
    *(LAS f32x2*)(uintptr_t)(priv + 2048 + lane * 8) = f32x2{lb, mb};
    BARRIER();
    probe = __builtin_amdgcn_raw_buffer_load_b32(rs, po_probe, 0, 16);
    SBAR();
    const unsigned yp = L_PRIV + (wid ^ 4) * PRIV;
    const f32x2 y2 = *(const LAS f32x2*)(uintptr_t)(yp + 2048 + lane * 8);
    const f32x4 y0 = lds_ldf4(yp + lane * 16), y1 = lds_ldf4(yp + 1024 + lane * 16);
    const float M = fmaxf(ma, y2[1]);
    float f1 = EX(ma - M), f2 = EX(y2[1] - M);
    const float inv = RCP(la * f1 + y2[0] * f2);
    f1 *= inv; f2 *= inv;
    u32x4 po;
    po[0] = san(pack_bf16(oa[0] * f1 + y0[0] * f2, oa[1] * f1 + y0[1] * f2));
    po[1] = san(pack_bf16(oa[2] * f1 + y0[2] * f2, oa[3] * f1 + y0[3] * f2));
    po[2] = san(pack_bf16(oa[4] * f1 + y1[0] * f2, oa[5] * f1 + y1[1] * f2));
    po[3] = san(pack_bf16(oa[6] * f1 + y1[2] * f2, oa[7] * f1 + y1[3] * f2));
    ho_st(rs, HO_O + ((qb * 8 + qg * 2 + kh) * H + head) * 1024 + lane * 16, po);
  }
  STAMP(8);

  {
    const int oo = HO_O + (tt * H + wid * 4) * 1024 + lane * 16;
    for (unsigned spin = 0;; ++spin) {
      if (__builtin_amdgcn_ballot_w64(probe == SENT) == 0 || spin > SPIN_CAP) break;
      POLL_PAUSE(1);
      probe = __builtin_amdgcn_raw_buffer_load_b32(rs, po_probe, 0, 16);
    }
    u32x4 o0 = ho_ld(rs, oo), o1 = ho_ld(rs, oo + 1024), o2 = ho_ld(rs, oo + 2048), o3 = ho_ld(rs, oo + 3072);
    for (unsigned spin = 0;; ++spin) {
      const unsigned long long p0 = __builtin_amdgcn_ballot_w64(pend(o0)), p1 = __builtin_amdgcn_ballot_w64(pend(o1));
      const unsigned long long p2 = __builtin_amdgcn_ballot_w64(pend(o2)), p3 = __builtin_amdgcn_ballot_w64(pend(o3));
      if ((p0 | p1 | p2 | p3) == 0 || spin > SPIN_CAP) break;
      POLL_PAUSE(1);
      if (p0) o0 = ho_ld(rs, oo);
      if (p1) o1 = ho_ld(rs, oo + 1024);
      if (p2) o2 = ho_ld(rs, oo + 2048);
      if (p3) o3 = ho_ld(rs, oo + 3072);
    }
    const unsigned oa2 = wid * 4096 + lane * 16;
    lds_stu4(oa2, o0); lds_stu4(oa2 + 1024, o1); lds_stu4(oa2 + 2048, o2); lds_stu4(oa2 + 3072, o3);
    WAIT_LGKM0();
    if (lane == 0) __hip_atomic_fetch_or((LAS unsigned*)(uintptr_t)(L_RDY + 32), 1u << wid, __ATOMIC_RELAXED, __HIP_MEMORY_SCOPE_WORKGROUP);
  }
  STAMP(9);
  if (isd1) {
    const unsigned fa = F_OFF + wid * 2048 + lane * 16;
    lds_stu4(fa, fw0); lds_stu4(fa + 1024, fw1); lds_stu4(fa + 16384, fw2); lds_stu4(fa + 17408, fw3);
  } else if (isd2) {
    const unsigned fa = F_OFF + wid * 1024 + lane * 16;
    lds_stu4(fa, fw0); lds_stu4(fa + 8192, fw1); lds_stu4(fa + 16384, fw2); lds_stu4(fa + 24576, fw3); lds_stu4(fa + 32768, fw4); lds_stu4(fa + 40960, fw5);
  }
  WAIT_LGKM0();
  bf16x8 ff[6];
  {
    const unsigned ft = (8 * g + (i16 >> 2)) * 32 + (i16 & 3) * 8;
    const unsigned fl = F_OFF + wid * (isd1 ? 2048 : 1024) + ft, s1 = isd1 ? 1024 : 8192;
    ff[0] = cat8(tr_read(fl), tr_read(fl + 128)); ff[1] = cat8(tr_read(fl + s1), tr_read(fl + s1 + 128));
    ff[2] = cat8(tr_read(fl + 16384), tr_read(fl + 16384 + 128)); ff[3] = cat8(tr_read(fl + 16384 + s1), tr_read(fl + 16384 + s1 + 128));
    ff[4] = cat8(tr_read(fl + 32768), tr_read(fl + 32768 + 128)); ff[5] = cat8(tr_read(fl + 40960), tr_read(fl + 40960 + 128));
  }
  if (wid < 6) {
    const int rec = ((cbk + 1 + (wid >> 1)) & 3) * 4 + (wid & 1) * 2 + hh;
    u32x4 sv[1];
    poll_all<1>(rs, HO_ST + (tt * 16 + rec) * 512 + r32 * 16, 0, sv);
    lds_stu4(S_OFF + rec * 528 + r32 * 16, sv[0]);
    STAMP(10);
  }
  BARRIER();
  STAMP(11);
  const u32x4 hv = lds_ldu4(X_OFF + row * 272 + c8 * 16);
  u32x4 ov;
  {
    const f32x2 pr = *(const LAS f32x2*)(uintptr_t)(S_OFF + c8 * 528 + row * 16);
    f32x2 st;
    { float mbv, rden; ln_stats(row16_sum(pr[0]), row16_sum(pr[1]), mbv, rden); st = f32x2{mbv, rden}; }
    u32x4 lv;
#pragma unroll
    for (int j = 0; j < 4; ++j) {
      const float h0 = bf_lo(hv[j]), h1 = bf_hi(hv[j]);
      const unsigned d = pack_bf16(h0 - st[0], h1 - st[0]);
      lv[j] = pack_bf16(bf_lo(d) * st[1], bf_hi(d) * st[1]);
      ov[j] = pack_bf16(h0 + bf_lo(lv[j]), h1 + bf_hi(lv[j]));
    }
    lv = u32x4{san(lv[0]), san(lv[1]), san(lv[2]), san(lv[3])};
    PIN(lv);
    BARRIER();
    ho_st(rs, HO_LN + goff, lv);
  }
  BARRIER(); BARRIER(); BARRIER();
  STAMP(13);
  const unsigned mykey = lds_ldu(T_OFF + 640 + (lane & 7) * 4);
  const int myrow = 2047 - (int)(mykey & 0x7ffu);
  if (isd1) {
    u32x4 ax[2];
    poll_all<2>(rs, HO_LN + (myrow * D + wid * 64 + g * 8) * 2, 64, ax);
    f32x4 ag = {}, au = {};
    ag = MFMA16(BC8(ax[0]), ff[0], ag); au = MFMA16(BC8(ax[0]), ff[2], au);
    ag = MFMA16(BC8(ax[1]), ff[1], ag); au = MFMA16(BC8(ax[1]), ff[3], au);
    lds_stf4(R_OFF + wid * 1024 + lane * 16, hh ? au : ag);
    BARRIER();
    if (wid == 0) {
      f32x4 sum = lds_ldf4(R_OFF + lane * 16);
#pragma unroll
      for (int w2 = 1; w2 < 8; ++w2) sum += lds_ldf4(R_OFF + w2 * 1024 + lane * 16);
      const auto x0 = __builtin_amdgcn_permlane32_swap(__float_as_uint(sum[0]), __float_as_uint(sum[2]), false, false);
      const auto x1 = __builtin_amdgcn_permlane32_swap(__float_as_uint(sum[1]), __float_as_uint(sum[3]), false, false);
      const float g0 = rbf(__uint_as_float(x0[0])), u0 = rbf(__uint_as_float(x0[1])), g1 = rbf(__uint_as_float(x1[0])), u1 = rbf(__uint_as_float(x1[1]));
      const float a0f = rbf(g0 * sigmoid(g0)) * u0, a1f = rbf(g1 * sigmoid(g1)) * u1;
      const float n0 = dpp_f<DPP_X1>(a0f), n1 = dpp_f<DPP_X1>(a1f);
      const bool odd = lane & 1;
      const int tok = 4 * (g & 1) + 2 * hh + (odd ? 1 : 0);
      ho_st1(rs, HO_ACT + d1 * 256 + tok * 32 + (i16 >> 1) * 4, san(odd ? pack_bf16(n1, a1f) : pack_bf16(a0f, n0)));
    }
    STAMP(14);
  }
  {
    const int grow = tt * 32 + row;
    int sel = -1;
#pragma unroll
    for (int c = 0; c < 8; ++c) if (__builtin_amdgcn_readlane(myrow, c) == grow) sel = c;
    if (sel < 0) __builtin_amdgcn_raw_buffer_store_b128(ov, __builtin_amdgcn_make_buffer_rsrc(out, 0, S * D * 2, 0x00020000), goff, 0, 16);
    else ho_st(rs, HO_HS + (sel * D + cbk * 128 + c8 * 8) * 2, u32x4{san(hv[0]), san(hv[1]), san(hv[2]), san(hv[3])});
    if (b == 0 && tid == 0) __builtin_amdgcn_raw_buffer_store_b32(tag, rs, WS_EP, 0, 16);
  }
  if (isd2) {
    const int n0 = sub * 16;
    unsigned hw = SENT, lw = SENT, fkey = 0;
    const bool odd = lane & 1;
    const int tok = 4 * (g & 1) + 2 * hh + (odd ? 1 : 0), cp = i16 >> 1;
    float sig = 0.f;
    if (wid == 0) {
      fkey = lds_ldu(T_OFF + 640 + tok * 4);
      const int frow = 2047 - (int)(fkey & 0x7ffu);
      sig = rbf(sigmoid(key_score(fkey)));
      for (unsigned spin = 0;; ++spin) {
        if (hw == SENT) hw = __builtin_amdgcn_raw_buffer_load_b32(rs, HO_HS + (tok * D + n0 + cp * 2) * 2, 0, 16);
        if (lw == SENT) lw = __builtin_amdgcn_raw_buffer_load_b32(rs, HO_LN + (frow * D + n0 + cp * 2) * 2, 0, 16);
        if (!__any(hw == SENT || lw == SENT) || spin > SPIN_CAP) break;
        POLL_PAUSE(1);
      }
    }
    u32x4 av[6];
    const int ao = HO_ACT + ((wid * 2 + (g >> 1)) * 8 + (lane & 7)) * 32 + (g & 1) * 16;
    poll_all<6>(rs, ao, 4096, av, wid < 3 ? 0 : 4096);
    STAMP(14);
    f32x4 y = {}, y2 = {};
    y = MFMA16(BC8(av[0]), ff[0], y); y2 = MFMA16(BC8(av[1]), ff[1], y2);
    y = MFMA16(BC8(av[2]), ff[2], y); y2 = MFMA16(BC8(av[3]), ff[3], y2);
    y = MFMA16(BC8(av[4]), ff[4], y);
    if (wid < 3) y2 = MFMA16(BC8(av[5]), ff[5], y2);
    lds_stf4(R_OFF + wid * 1024 + lane * 16, hh ? y2 : y);
    BARRIER();
    if (wid == 0) {
      f32x4 sum = lds_ldf4(R_OFF + lane * 16);
#pragma unroll
      for (int w2 = 1; w2 < 8; ++w2) sum += lds_ldf4(R_OFF + w2 * 1024 + lane * 16);
      const auto x0 = __builtin_amdgcn_permlane32_swap(__float_as_uint(sum[0]), __float_as_uint(sum[2]), false, false);
      const auto x1 = __builtin_amdgcn_permlane32_swap(__float_as_uint(sum[1]), __float_as_uint(sum[3]), false, false);
      const float t0 = __uint_as_float(x0[0]) + __uint_as_float(x0[1]), t1 = __uint_as_float(x1[0]) + __uint_as_float(x1[1]);
      const float n0f = dpp_f<DPP_X1>(t0), n1f = dpp_f<DPP_X1>(t1);
      const float ylo = rbf(rbf(odd ? n1f : t0) * sig), yhi = rbf(rbf(odd ? t1 : n0f) * sig);
      *(unsigned*)((char*)out + ((size_t)(2047 - (int)(fkey & 0x7ffu)) * D + n0 + cp * 2) * 2) =
          pack_bf16(bf_lo(hw) + rbf(bf_lo(lw) + ylo), bf_hi(hw) + rbf(bf_hi(lw) + yhi));
    }
  }
  }
  STAMP(15); STAMP_OUT(0);
}

extern "C" void kernel_launch(void* const* d_in, const int* in_sizes, int n_in, void* d_out, int out_size, void* d_ws, size_t ws_size,
                              hipStream_t stream) {
  (void)hipFuncSetAttribute((const void*)k_fused, hipFuncAttributeMaxDynamicSharedMemorySize, LDS_BYTES);
  k_fused<<<256, 768, LDS_BYTES, stream>>>((const uint16_t*)d_in[0], (const uint16_t*)d_in[1], (const uint16_t*)d_in[2], (const uint16_t*)d_in[4],
                                           (const uint16_t*)d_in[5], (const uint16_t*)d_in[6], (const uint16_t*)d_in[7], (char*)d_ws, (uint16_t*)d_out);
}
```

```cpp
#define PROFILE_STAMPS 1
#include <hip/hip_runtime.h>
#include <hip/hip_bf16.h>
#include <stdint.h>
#include <stdio.h>

typedef short bf16x8 __attribute__((ext_vector_type(8)));
typedef short bf16x4 __attribute__((ext_vector_type(4)));
typedef float f32x16 __attribute__((ext_vector_type(16)));
typedef float f32x4 __attribute__((ext_vector_type(4)));
typedef float f32x2 __attribute__((ext_vector_type(2)));
typedef __bf16 bf16x2_t __attribute__((ext_vector_type(2)));
typedef unsigned u32x4 __attribute__((ext_vector_type(4)));
typedef unsigned u32x2 __attribute__((ext_vector_type(2)));
#define LAS __attribute__((address_space(3)))
#define GAS __attribute__((address_space(1)))
#define DI __device__ __forceinline__
#define MFMA32(a, b, c) __builtin_amdgcn_mfma_f32_32x32x16_bf16((a), (b), (c), 0, 0, 0)
#define MFMA16(a, b, c) __builtin_amdgcn_mfma_f32_16x16x32_bf16((a), (b), (c), 0, 0, 0)
#define SBAR() __builtin_amdgcn_sched_barrier(0)
#define PIN(x) asm volatile("" : "+v"(x))
#define BC8(x) __builtin_bit_cast(bf16x8, x)

constexpr int S = 2048, D = 512, H = 32, F = 1376;
constexpr float LN_EPS = 1e-5f;
constexpr float QSCALE = 0.25f * 1.4426950408889634f;
constexpr unsigned SENT = 0xFFFFFFFFu;
constexpr unsigned SPIN_CAP = 1u << 16;

constexpr int WS_Q = 0;
constexpr int WS_KV = WS_Q + H * 64 * 1024;
constexpr int WS_FA = WS_KV + H * 64 * 2048;
constexpr int WS_FR = WS_FA + H * 64 * 128;
constexpr int WS_EP = WS_FR + 4096;
constexpr int WS_HO = WS_EP + 4096;
constexpr int HO_O = WS_HO;
constexpr int HO_LN = HO_O + 64 * H * 1024;
constexpr int HO_ST = HO_LN + S * D * 2;
constexpr int HO_KEY = HO_ST + 64 * 16 * 512;
constexpr int HO_ACT = HO_KEY + 8 * S * 4;
constexpr int HO_HS = HO_ACT + 24576;
constexpr int HO_END = HO_HS + 8192;
constexpr int HO_PER_WG = 19 * 1024;
static_assert(WS_HO + 256 * HO_PER_WG >= HO_END, "reset covers the hand-off words");
constexpr int WS_BYTES = WS_HO + 256 * HO_PER_WG;

constexpr size_t WS_DBG = 64u << 20;
#define STAMP_DECL unsigned st_r = 0, st_c = 0
#define STAMP(i) do { unsigned long long r_, c_; __builtin_amdgcn_sched_barrier(0); \
    asm volatile("s_memrealtime %0\n\ts_memtime %1\n\ts_waitcnt lgkmcnt(0)" : "=s"(r_), "=s"(c_) :: "memory"); \
    const bool m_ = (int)(threadIdx.x & 63) == (i); st_r = m_ ? (unsigned)r_ : st_r; st_c = m_ ? (unsigned)c_ : st_c; PIN(st_r); PIN(st_c); __builtin_amdgcn_sched_barrier(0); } while (0)
#define RL(i) ((int)(__builtin_amdgcn_readlane((int)st_r, (i)) ? (unsigned)__builtin_amdgcn_readlane((int)st_r, (i)) % 100000u : 0u))
#define STAMP_OUT(k) do { if ((blockIdx.x < 8 || blockIdx.x >= 248) && ((threadIdx.x >> 6) == 4 || (threadIdx.x >> 6) == 8)) { \
    const int a0 = RL(0), a1 = RL(1), a2 = RL(2), a3 = RL(3), a4 = RL(4), a5 = RL(5), a6 = RL(6), a7 = RL(7), a8 = RL(8), a9 = RL(9), a10 = RL(10), a11 = RL(11), a12 = RL(12), a13 = RL(13), a14 = RL(14), a15 = RL(15); \
    if ((threadIdx.x & 63) == 0) printf("ST b%d w%d: %d %d %d %d %d %d %d %d %d %d %d %d %d %d %d %d\n", blockIdx.x, threadIdx.x >> 6, a0, a1, a2, a3, a4, a5, a6, a7, a8, a9, a10, a11, a12, a13, a14, a15); } } while (0)

#define FSTAMP(i) do {} while (0)

DI unsigned pack_bf16(float lo, float hi) {
  f32x2 v = {lo, hi};
  bf16x2_t r = __builtin_convertvector(v, bf16x2_t);
  return __builtin_bit_cast(unsigned, r);
}
DI float bf_lo(unsigned v) { return __uint_as_float(v << 16); }
DI float bf_hi(unsigned v) { return __uint_as_float(v & 0xffff0000u); }
DI float rbf(float x) { return bf_lo(pack_bf16(x, x)); }
DI float sum_halves(float v) {
  auto r = __builtin_amdgcn_permlane32_swap(__float_as_uint(v), __float_as_uint(v), false, false);
  return __uint_as_float(r[0]) + __uint_as_float(r[1]);
}
DI float sum_rows(float v) {
  auto r = __builtin_amdgcn_permlane16_swap(__float_as_uint(v), __float_as_uint(v), false, false);
  return sum_halves(__uint_as_float(r[0]) + __uint_as_float(r[1]));
}
DI float max_halves(float v) {
  auto r = __builtin_amdgcn_permlane32_swap(__float_as_uint(v), __float_as_uint(v), false, false);
  return fmaxf(__uint_as_float(r[0]), __uint_as_float(r[1]));
}
template <int CTRL> DI float dpp_f(float v) { return __uint_as_float(__builtin_amdgcn_update_dpp(0u, __float_as_uint(v), CTRL, 0xf, 0xf, true)); }
template <int CTRL> DI unsigned dpp_u(unsigned v) { return __builtin_amdgcn_update_dpp(0u, v, CTRL, 0xf, 0xf, true); }
constexpr int DPP_X1 = 0xB1, DPP_X2 = 0x4E, DPP_HMIR = 0x141, DPP_MIR = 0x140;
DI float quad_sum(float v) { v += dpp_f<DPP_X1>(v); v += dpp_f<DPP_X2>(v); return v; }
DI float row16_sum(float v) { v = quad_sum(v); v += dpp_f<DPP_HMIR>(v); v += dpp_f<DPP_MIR>(v); return v; }
DI void glds16(const void* sbase, unsigned voff, unsigned lds_base) {
  unsigned sv;
  asm volatile("s_mov_b32 %0, m0\n\ts_mov_b32 m0, %3\n\ts_nop 0\n\tglobal_load_lds_dwordx4 %1, %2\n\ts_mov_b32 m0, %0"
               : "=&s"(sv) : "v"(voff), "s"(sbase), "s"(lds_base) : "memory");
}
DI void glds16s(const void* sbase, unsigned voff, unsigned lds_base) {
  unsigned sv;
  asm volatile("s_mov_b32 %0, m0\n\ts_mov_b32 m0, %3\n\ts_nop 0\n\tglobal_load_lds_dwordx4 %1, %2 sc1\n\ts_mov_b32 m0, %0"
               : "=&s"(sv) : "v"(voff), "s"(sbase), "s"(lds_base) : "memory");
}
DI void glds4(const void* sbase, unsigned voff, unsigned lds_base) {
  unsigned sv;
  asm volatile("s_mov_b32 %0, m0\n\ts_mov_b32 m0, %3\n\ts_nop 0\n\tglobal_load_lds_dword %1, %2\n\ts_mov_b32 m0, %0"
               : "=&s"(sv) : "v"(voff), "s"(sbase), "s"(lds_base) : "memory");
}
#define WAIT_VM(N) asm volatile("s_waitcnt vmcnt(" #N ")" ::: "memory")
#define WAIT_LGKM0() asm volatile("s_waitcnt lgkmcnt(0)" ::: "memory")
#define BARRIER() asm volatile("s_waitcnt lgkmcnt(0)\n\ts_barrier" ::: "memory")
typedef short v4i16_t __attribute__((ext_vector_type(4)));
DI bf16x4 tr_read(unsigned addr) {
  return __builtin_bit_cast(bf16x4, __builtin_amdgcn_ds_read_tr16_b64_v4i16((LAS v4i16_t*)(uintptr_t)addr));
}
DI bf16x8 lds_ld128(unsigned addr) { return *(const LAS bf16x8*)(uintptr_t)addr; }
DI f32x4 lds_ldf4(unsigned addr) { return *(const LAS f32x4*)(uintptr_t)addr; }
DI u32x4 lds_ldu4(unsigned addr) { return *(const LAS u32x4*)(uintptr_t)addr; }
DI void lds_stf4(unsigned addr, f32x4 v) { *(LAS f32x4*)(uintptr_t)addr = v; }
DI void lds_stu4(unsigned addr, u32x4 v) { *(LAS u32x4*)(uintptr_t)addr = v; }
DI void lds_stf(unsigned addr, float v) { *(LAS float*)(uintptr_t)addr = v; }
DI void lds_stu(unsigned addr, unsigned v) { *(LAS unsigned*)(uintptr_t)addr = v; }
DI float lds_ldf(unsigned addr) { return *(const LAS float*)(uintptr_t)addr; }
DI unsigned lds_ldu(unsigned addr) { return *(const LAS unsigned*)(uintptr_t)addr; }
DI bf16x8 cat8(bf16x4 a, bf16x4 b) { return __builtin_shufflevector(a, b, 0, 1, 2, 3, 4, 5, 6, 7); }
typedef __amdgpu_buffer_rsrc_t rsrc_t;
DI u32x4 ho_ld(rsrc_t rs, int off) { return __builtin_amdgcn_raw_buffer_load_b128(rs, off, 0, 16); }
DI void ho_st(rsrc_t rs, int off, u32x4 v) { __builtin_amdgcn_raw_buffer_store_b128(v, rs, off, 0, 16); }
DI void ho_st1(rsrc_t rs, int off, unsigned v) { __builtin_amdgcn_raw_buffer_store_b32(v, rs, off, 0, 16); }
DI unsigned san(unsigned v) { return min(v, SENT - 1u); }
#define POLL_PAUSE(n) do { __builtin_amdgcn_s_sleep(n); asm volatile("" ::: "memory"); } while (0)
DI bool pend(u32x4 v) { return max(max(v[0], v[1]), max(v[2], v[3])) == SENT; }

constexpr int LDS_BYTES = 163840;
constexpr int KVT = 2080;
constexpr int L_PRIV = 64 * KVT, PRIV = 2560;
constexpr int L_STG = L_PRIV + 8 * PRIV;
constexpr int L_RDY = L_STG + 4 * 2048;
constexpr int L_RS = L_RDY + 64;
static_assert(L_RS + 1024 <= LDS_BYTES, "lds map");
constexpr unsigned X_OFF = 32768, S_OFF = 45056, T_OFF = 55296, R_OFF = 57344, F_OFF = 81920;
constexpr int N_D1 = 86;
constexpr int CODE_PF_BYTES = 25 * 1024;
struct AFrag { bf16x8 x0, x1, q, k, v; };

#define EX(x) __builtin_amdgcn_exp2f(x)
#define PK(a, b) pack_bf16(a, b)
#define RCP(x) __builtin_amdgcn_rcpf(x)
#define UNIT(SC, SN, KN, QN, NEGN, OPREV, VPREV, OCUR, VCUR0, LOADS) do { \
    float e0 = EX(SC[0]), e1 = EX(SC[1]); SBAR(); \
    OPREV = MFMA32(VPREV, BC8(p1), OPREV); \
    LOADS; \
    SBAR(); \
    float e2 = EX(SC[2]), e3 = EX(SC[3]); SBAR(); \
    p0[0] = PK(e0, e1); SBAR(); float e4 = EX(SC[4]), e5 = EX(SC[5]); SBAR(); \
    p0[1] = PK(e2, e3); SBAR(); float e6 = EX(SC[6]), e7 = EX(SC[7]); SBAR(); \
    SN = MFMA32(KN, QN, NEGN); SBAR(); \
    p0[2] = PK(e4, e5); SBAR(); e0 = EX(SC[8]); e1 = EX(SC[9]); SBAR(); \
    p0[3] = PK(e6, e7); SBAR(); e2 = EX(SC[10]); e3 = EX(SC[11]); SBAR(); \
    OCUR = MFMA32(VCUR0, BC8(p0), OCUR); SBAR(); \
    p1[0] = PK(e0, e1); SBAR(); e4 = EX(SC[12]); e5 = EX(SC[13]); SBAR(); \
    p1[1] = PK(e2, e3); SBAR(); e6 = EX(SC[14]); e7 = EX(SC[15]); SBAR(); \
    p1[2] = PK(e4, e5); SBAR(); \
    p1[3] = PK(e6, e7); SBAR(); \
  } while (0)
#define TWO_TILES(U) do { \
    UNIT(sA, sB, k0, qb2, ng, ob, v11, oa, v00, k1 = lds_ld128(ka + (U + 1) * KVT); v10 = lds_ld128(va0 + (U + 1) * KVT)); \
    UNIT(sB, sA, k1, qa, ng, oa, v01, ob, v00, v11 = lds_ld128(va1 + (U + 1) * KVT)); \
    UNIT(sA, sB, k1, qb2, ng, ob, v01, oa, v10, k0 = lds_ld128(ka + (U + 2) * KVT); v00 = lds_ld128(va0 + (U + 2) * KVT)); \
    UNIT(sB, sA, k0, qa, ng, oa, v11, ob, v10, v01 = lds_ld128(va1 + (U + 2) * KVT)); \
  } while (0)

DI int fresh_lane() {
  int l;
  asm volatile("v_mbcnt_lo_u32_b32 %0, -1, 0\n\tv_mbcnt_hi_u32_b32 %0, -1, %0" : "=v"(l));
  return l;
}
#define RELANE() const int lane = fresh_lane(), tid = wid * 64 + lane, r32 = lane & 31, hh = lane >> 5, g = lane >> 4, i16 = lane & 15; \
  (void)tid; (void)r32; (void)hh; (void)g; (void)i16
DI float tile_max(const f32x16& s) {
  float m = fmaxf(fmaxf(s[0], s[1]), s[2]);
#pragma unroll
  for (int j = 3; j < 15; j += 2) m = fmaxf(fmaxf(m, s[j]), s[j + 1]);
  return fmaxf(m, s[15]);
}
DI unsigned sort_key(float r, int idx) {
  unsigned u = pack_bf16(r, r) & 0xffffu;
  if ((u & 0x7fffu) > 0x7f80u) u = 0xffffu;
  u = (u & 0x8000u) ? (~u & 0xffffu) : (u | 0x8000u);
  return (u << 11) | (unsigned)(2047 - idx);
}
DI float key_score(unsigned key) {
  unsigned u = (key >> 11) & 0xffffu;
  u = (u & 0x8000u) ? (u & 0x7fffu) : (~u & 0xffffu);
  return __uint_as_float(u << 16);
}
DI void ln_stats(float s1, float s2, float& mb, float& rden) {
  const float mean = s1 * (1.f / D);
  const float var = fmaxf(s2 * (1.f / D) - mean * mean, 0.f);
  mb = rbf(mean);
  rden = RCP(rbf(__builtin_amdgcn_sqrtf(rbf(rbf(var) + LN_EPS))));
}
DI float sigmoid(float v) { return RCP(1.f + EX(v * -1.4426950408889634f)); }
DI void wait_rdy(unsigned need) {
  for (unsigned spin = 0;; ++spin) {
    const u32x4 r = *(volatile const LAS u32x4*)(uintptr_t)L_RDY;
    const unsigned m = min(min(r[0], r[1]), min(r[2], r[3]));
    if (__builtin_amdgcn_readfirstlane(m) >= need || spin > (SPIN_CAP << 4)) break;
    __builtin_amdgcn_s_sleep(1);
  }
  asm volatile("" ::: "memory");
}
DI void wait_word(unsigned addr, unsigned need) {
  for (unsigned spin = 0;; ++spin) {
    const unsigned r = *(volatile const LAS unsigned*)(uintptr_t)addr;
    if (__builtin_amdgcn_readfirstlane(r) >= need || spin > (SPIN_CAP << 4)) break;
    __builtin_amdgcn_s_sleep(1);
  }
  asm volatile("" ::: "memory");
}
DI unsigned gt_bit(unsigned m, unsigned a, unsigned c) { return __builtin_amdgcn_alignbit(m, c - a, 31); }
DI unsigned count_gt(u32x4 a, u32x4 b, unsigned c) {
  unsigned m = 0, n = 0;
#pragma unroll
  for (int j = 0; j < 4; ++j) { m = gt_bit(m, a[j], c); n = gt_bit(n, b[j], c); }
  return __builtin_popcount(m) + __builtin_popcount(n);
}
DI unsigned max4(u32x4 v) { return max(max(v[0], v[1]), max(v[2], v[3])); }
template <int N> DI void poll_all(rsrc_t rs, int off, int stride, u32x4 (&v)[N], int back = 0) {
  for (unsigned spin = 0;; ++spin) {
#pragma unroll
    for (int j = 0; j < N; ++j) v[j] = ho_ld(rs, off + j * stride - (j == N - 1 ? back : 0));
    unsigned m = 0;
#pragma unroll
    for (int j = 0; j < N; ++j) m = max(m, max4(v[j]));
    if (__builtin_amdgcn_ballot_w64(m == SENT) == 0 || spin > SPIN_CAP) break;
    POLL_PAUSE(1);
  }
}
DI unsigned sum8_u(unsigned v) { v += dpp_u<DPP_X1>(v); v += dpp_u<DPP_X2>(v); v += dpp_u<DPP_HMIR>(v); return v; }

__global__ __launch_bounds__(768, 1) void k_fused(const uint16_t* __restrict__ x, const uint16_t* __restrict__ wqkv, const uint16_t* __restrict__ wo,
                                                   const uint16_t* __restrict__ wr, const uint16_t* __restrict__ wg, const uint16_t* __restrict__ wu,
                                                   const uint16_t* __restrict__ wd, char* __restrict__ ws, uint16_t* __restrict__ out) {
  extern __shared__ __attribute__((aligned(16))) char smem[];
  asm volatile("" :: "s"((unsigned)(uintptr_t)smem));
  const int tid = threadIdx.x, lane = tid & 63, wid = __builtin_amdgcn_readfirstlane(tid >> 6);
  const int b = blockIdx.x, xcd = b & 7, sub = b >> 3;
  const int r32 = lane & 31, hh = lane >> 5, g = lane >> 4, i16 = lane & 15;
  const rsrc_t rs = __builtin_amdgcn_make_buffer_rsrc(ws, 0, WS_BYTES, 0x00020000);
  const int rb = (xcd >> 1) * 8 + (sub & 7), hq = (xcd & 1) * 4 + (sub >> 3);
  const int head = xcd * 4 + (sub & 3), qb = sub >> 2;
  const int tt = xcd * 8 + (sub >> 2), cbk = sub & 3;
  const int d1 = (xcd < 7 && sub < 13) ? xcd * 13 + sub : 999;
  const bool isd1 = d1 < N_D1, isd2 = xcd == 7;
  STAMP_DECL; STAMP(0);
  unsigned code_pf = 0;
  if (wid == 7) {
    const unsigned long long pc = __builtin_amdgcn_s_getpc() & ~0xFFull;
    const unsigned off = min((unsigned)((sub & 3) * 8192 + lane * 128), (unsigned)(CODE_PF_BYTES - 128));
    asm volatile("global_load_dword %0, %1, %2" : "=v"(code_pf) : "v"(off), "s"(pc) : "memory");
  }

  if (__builtin_expect(wid >= 8, 1)) {
    const int hw = wid - 8;
    __builtin_amdgcn_s_setprio(3);
    {
      const int rot = ((sub & 7) + 2 * (sub >> 3)) & 7;
      const char* base; unsigned vo0, vo1, cstr, pstr;
      if (hw < 3) {
        base = (const char*)wqkv + hw * 1024 + hq * 128; cstr = 64 * 3072; pstr = 8 * 3072;
        vo0 = (lane >> 3) * 3072 + (((lane & 7) ^ ((g & 1) << 1)) * 16); vo1 = (lane >> 3) * 3072 + (((lane & 7) ^ (((g & 1) | 2) << 1)) * 16);
      } else {
        base = (const char*)x + (size_t)rb * 65536; cstr = 128; pstr = 8192;
        vo0 = (lane >> 3) * 1024 + (((lane & 7) ^ g) * 16); vo1 = (lane >> 3) * 1024 + (((lane & 7) ^ (4 + g)) * 16);
      }
      auto issue = [&](int c) {
        const char* src = base + (size_t)((c + rot) & 7) * cstr;
        const unsigned dst = (c & 3) * 32768 + hw * 8192;
#pragma unroll
        for (int p = 0; p < 8; ++p) glds16(src + (size_t)p * pstr, (p & 1) ? vo1 : vo0, dst + p * 1024);
      };
#pragma unroll 1
      for (int c = -3; c < 7; ++c) {
        if (c >= -1) {
          if (c <= 5) { WAIT_VM(8); } else { WAIT_VM(0); }
          BARRIER();
        }
        if (c + 3 < 8) issue(c + 3);
      }
    }
    unsigned tag = __builtin_amdgcn_raw_buffer_load_b32(rs, WS_EP, 0, 16);
    tag = __builtin_amdgcn_readfirstlane(tag) + 1u;
    BARRIER();
    STAMP(4);
    if (lane < 32) {
      const unsigned o = (lane & 1) ? 0u : 0x3f803f80u;
      lds_stu4((hw * 16 + (lane >> 1)) * KVT + 2048 + (lane & 1) * 16, u32x4{o, o, o, o});
    }
    wait_word(L_RDY + 36, 8u);
    for (unsigned spin = 0;; ++spin) {
      const u32x2 f = __builtin_amdgcn_raw_buffer_load_b64(rs, WS_FA + (head * 64 + ((lane >> 1) & 7) * 8 + hw * 2 + (lane & 1)) * 128, 0, 16);
      if (__all(f[0] == tag && f[1] == ~tag) || spin > SPIN_CAP) break;
      POLL_PAUSE(1);
    }
    STAMP(5);
    {
      const char* kvg = ws + WS_KV + (size_t)head * (64 * 2048) + hw * 4096;
      const char* qsrc = ws + WS_Q + (size_t)(head * 64 + qb * 8 + hw * 2) * 1024;
      auto kv_step = [&](int st) {
#pragma unroll
        for (int hf = 0; hf < 2; ++hf)
#pragma unroll
          for (int p = 0; p < 2; ++p)
            glds16s(kvg + (st >> 1) * 16384 + (st & 1) * 2048 + hf * 65536 + p * 1024, lane * 16, (hw + st * 4 + hf * 32) * KVT + p * 1024);
      };
      glds16s(qsrc, lane * 16, L_STG + hw * 2048); glds16s(qsrc + 1024, lane * 16, L_STG + hw * 2048 + 1024);
      kv_step(0); kv_step(1);
      asm volatile("s_waitcnt vmcnt(4) lgkmcnt(0)" ::: "memory");
      lds_stu(L_RDY + hw * 4, 1u);
      kv_step(2); kv_step(3);
      WAIT_VM(8);
      lds_stu(L_RDY + hw * 4, 2u);
      kv_step(4); kv_step(5);
      WAIT_VM(8);
      lds_stu(L_RDY + hw * 4, 4u);
      kv_step(6); kv_step(7);
      WAIT_VM(0);
      lds_stu(L_RDY + hw * 4, 8u);
    }
    STAMP(6);
    if (hw == 0) {
      const u32x4 sv = {SENT, SENT, SENT, SENT};
#pragma unroll 1
      for (int j = 0; j < 19; ++j) __builtin_amdgcn_raw_buffer_store_b128(sv, rs, lane * 16, WS_HO + b * HO_PER_WG + j * 1024, 16);
      WAIT_VM(0);
      if (lane == 0) __builtin_amdgcn_raw_buffer_store_b64(u32x2{tag, ~tag}, rs, WS_FR + b * 8, 0, 16);
    }
    bf16x8 wf[32];
    float swr = 0.f;
    f32x16 acc;
    {
      const char* wsrc = (const char*)wo + (lane >> 2) * 1024 + cbk * 256 + hw * 64 + (lane & 3) * 16;
#pragma unroll
      for (int i = 0; i < 32; ++i) wf[i] = *(const bf16x8*)(wsrc + i * 16384);
      const unsigned stg = L_STG + hw * 2048;
      const unsigned wor = stg + (4 * (g >> 1) + (i16 >> 2)) * 64 + (g & 1) * 32 + (i16 & 3) * 8;
      wait_word(L_RDY + 16 + hw * 4, 2u);
#pragma unroll
      for (int i = 0; i < 32; ++i) {
        *(LAS bf16x8*)(uintptr_t)(stg + (i & 1) * 1024 + lane * 16) = wf[i];
        WAIT_LGKM0();
        wf[i] = cat8(tr_read(wor + (i & 1) * 1024), tr_read(wor + (i & 1) * 1024 + 512));
      }
      WAIT_LGKM0();
      const char* xs = (const char*)x + ((tt * 32 + r32) * D + cbk * 128 + hw * 32 + 4 * hh) * 2;
#pragma unroll
      for (int gq = 0; gq < 4; ++gq) {
        const u32x2 xw = *(const u32x2*)(xs + gq * 16);
        acc[4 * gq] = bf_lo(xw[0]); acc[4 * gq + 1] = bf_hi(xw[0]); acc[4 * gq + 2] = bf_lo(xw[1]); acc[4 * gq + 3] = bf_hi(xw[1]);
      }
      lds_stf(stg + r32 * 4, bf_lo(wr[cbk * 128 + hw * 32 + r32]));
      const u32x4 wra = *(const u32x4*)((const char*)wr + lane * 16);
#pragma unroll
      for (int j = 0; j < 4; ++j) swr += bf_lo(wra[j]) + bf_hi(wra[j]);
      swr = __uint_as_float(__builtin_amdgcn_readfirstlane(__float_as_uint(sum_rows(row16_sum(swr)))));
      PIN(acc);
      if (isd1) {
#pragma unroll 1
        for (int p = 0; p < 4; ++p) glds4(hw < 2 ? wg : wu, (((hw & 1) * 4 + p) * 64 + lane) * (F * 2) + d1 * 32, stg + 1024);
      } else if (isd2) {
#pragma unroll 1
        for (int p = hw; p < 22; p += 4) glds4(wd, min(p * 64 + lane, F - 1) * (D * 2) + sub * 32, stg + 1024);
      }
      WAIT_VM(0);
    }
    STAMP(7);
    if (hw == 0) {
      for (unsigned spin = 0;; ++spin) {
        const u32x4 f0 = ho_ld(rs, WS_FR + lane * 16), f1 = ho_ld(rs, WS_FR + 1024 + lane * 16);
        const bool okf = f0[0] == tag && f0[1] == ~tag && f0[2] == tag && f0[3] == ~tag && f1[0] == tag && f1[1] == ~tag && f1[2] == tag && f1[3] == ~tag;
        if (__all(okf) || spin > SPIN_CAP) break;
        POLL_PAUSE(8);
      }
    }
    STAMP(8);
    BARRIER();
    {
      RELANE();
      const unsigned ol = lane * 16;
      const int sto = HO_ST + (tt * 16 + cbk * 4 + hw) * 512 + r32 * 16;
      const unsigned ha = X_OFF + r32 * 272 + (hw * 32 + 4 * hh) * 2, wa = L_STG + hw * 2048 + 4 * hh * 4;
      f32x4 wv[4];
      auto wait_bit = [&](int bit) {
        for (unsigned spin = 0;; ++spin) {
          const unsigned m = __builtin_amdgcn_readfirstlane(*(volatile const LAS unsigned*)(uintptr_t)(L_RDY + 32));
          if (((m >> bit) & 1u) || spin > (SPIN_CAP << 4)) break;
          __builtin_amdgcn_s_sleep(1);
        }
        asm volatile("" ::: "memory");
      };
      bf16x8 of[4], nf[4];
      wait_bit(0);
#pragma unroll
      for (int i = 0; i < 4; ++i) of[i] = lds_ld128(ol + i * 1024);
#pragma unroll
      for (int w2 = 0; w2 < 8; ++w2) {
        unsigned mk = 0;
        if (w2 >= 1 && w2 < 7) {
          mk = *(volatile const LAS unsigned*)(uintptr_t)(L_RDY + 32);
          asm volatile("" ::: "memory");
#pragma unroll
          for (int i = 0; i < 4; ++i) nf[i] = lds_ld128(ol + (w2 * 4 + 4 + i) * 1024);
        }
        if (w2 == 6) {
#pragma unroll
          for (int gq = 0; gq < 4; ++gq) wv[gq] = lds_ldf4(wa + gq * 32);
        }
        SBAR();
#pragma unroll
        for (int i = 0; i < 4; ++i) acc = MFMA32(wf[w2 * 4 + i], of[i], acc);
        SBAR();
        if (w2 < 7 && !((__builtin_amdgcn_readfirstlane(mk) >> (w2 + 1)) & 1u)) {
          wait_bit(w2 + 1);
#pragma unroll
          for (int i = 0; i < 4; ++i) nf[i] = lds_ld128(ol + (w2 * 4 + 4 + i) * 1024);
        }
#pragma unroll
        for (int i = 0; i < 4; ++i) of[i] = nf[i];
      }
      STAMP(9);
      f32x2 t1 = {0.f, 0.f}, t2 = t1, t3 = t1;
#pragma unroll
      for (int j = 0; j < 8; ++j) {
        const f32x2 v = {acc[2 * j], acc[2 * j + 1]}, w = {wv[j >> 1][2 * (j & 1)], wv[j >> 1][2 * (j & 1) + 1]};
        t1 += v; t2 += v * v; t3 += v * w;
      }
      const float s1 = sum_halves(t1[0] + t1[1]), s2 = sum_halves(t2[0] + t2[1]), s3 = sum_halves(t3[0] + t3[1]);
      const u32x4 rec = {san(__float_as_uint(s1)), san(__float_as_uint(s2)), san(__float_as_uint(s3)), 0u};
      if (lane < 32) ho_st(rs, sto, rec);
      lds_stu4(S_OFF + (cbk * 4 + hw) * 528 + r32 * 16, rec);
#pragma unroll
      for (int gq = 0; gq < 4; ++gq)
        *(LAS u32x2*)(uintptr_t)(ha + gq * 16) = u32x2{pack_bf16(acc[4 * gq], acc[4 * gq + 1]), pack_bf16(acc[4 * gq + 2], acc[4 * gq + 3])};
    }
    BARRIER();
    if (hw == 0) {
      RELANE();
      f32x4 t = lds_ldf4(S_OFF + hh * 8 * 528 + r32 * 16);
#pragma unroll
      for (int j = 1; j < 8; ++j) t += lds_ldf4(S_OFF + (hh * 8 + j) * 528 + r32 * 16);
      const float s1 = sum_halves(t[0]), s2 = sum_halves(t[1]), s3 = sum_halves(t[2]);
      float mbv, rden;
      ln_stats(s1, s2, mbv, rden);
      ho_st1(rs, HO_KEY + (2 * cbk + hh) * (S * 4) + (tt * 32 + r32) * 4, sort_key((s3 - mbv * swr) * rden, tt * 32 + r32));
    }
    STAMP(11);
    {
      constexpr unsigned G = T_OFF, M1 = T_OFF + 256, C2 = T_OFF + 512, SEL = T_OFF + 640, C1 = T_OFF + 1024;
      const int ht = hw * 64 + fresh_lane(), t3 = ht & 3, t4 = ht >> 2;
      u32x4 kv[2];
      poll_all<2>(rs, HO_KEY + xcd * (S * 4) + ht * 32, 16, kv);
      STAMP(12);
      const unsigned m0 = max4(kv[0]), m1 = max4(kv[1]);
      unsigned gm = max(m0, m1); gm = max(gm, dpp_u<DPP_X1>(gm)); gm = max(gm, dpp_u<DPP_X2>(gm));
      lds_stu(G + t4 * 4, gm);
      BARRIER();
      {
        unsigned rk = count_gt(lds_ldu4(G + t3 * 64), lds_ldu4(G + t3 * 64 + 16), gm) + count_gt(lds_ldu4(G + t3 * 64 + 32), lds_ldu4(G + t3 * 64 + 48), gm);
        rk += dpp_u<DPP_X1>(rk); rk += dpp_u<DPP_X2>(rk);
        if (rk < 8) {
          lds_stu4(C1 + (rk * 8 + t3 * 2) * 16, kv[0]); lds_stu4(C1 + (rk * 8 + t3 * 2 + 1) * 16, kv[1]);
          *(LAS u32x2*)(uintptr_t)(M1 + (rk * 8 + t3 * 2) * 4) = u32x2{m0, m1};
        }
      }
      BARRIER();
      {
        const unsigned cm = lds_ldu(M1 + t4 * 4), key = lds_ldu(C1 + ht * 4);
        unsigned rk = count_gt(lds_ldu4(M1 + t3 * 64), lds_ldu4(M1 + t3 * 64 + 16), cm) + count_gt(lds_ldu4(M1 + t3 * 64 + 32), lds_ldu4(M1 + t3 * 64 + 48), cm);
        rk += dpp_u<DPP_X1>(rk); rk += dpp_u<DPP_X2>(rk);
        if (rk < 8) lds_stu(C2 + rk * 16 + t3 * 4, key);
      }
      BARRIER();
      {
        const unsigned key = lds_ldu(C2 + (ht >> 3) * 4);
        const u32x4 o = lds_ldu4(C2 + (ht & 7) * 16);
        const unsigned rk = sum8_u(((key - o[0]) >> 31) + ((key - o[1]) >> 31) + ((key - o[2]) >> 31) + ((key - o[3]) >> 31));
        if (rk < 8) lds_stu(SEL + rk * 4, key);
      }
      BARRIER();
    }
    STAMP(15); STAMP_OUT(0);
    return;
  }

  unsigned tag;
  {
    const int rt = wid & 1, hd = wid >> 1, sw = hd >> 1, par = rt ^ (hd & 1);
    tag = __builtin_amdgcn_raw_buffer_load_b32(rs, WS_EP, 0, 16);
    const int xr0 = rt * 32 + sw * 16 + i16, xr1 = xr0 ^ 16;
    const int xf = (i16 >> 1) & 7;
    const unsigned xb0 = 24576 + (xr0 >> 3) * 1024 + (xr0 & 7) * 128, xb1 = 24576 + (xr1 >> 3) * 1024 + (xr1 & 7) * 128;
    const unsigned xoA = ((4 * par + g) ^ xf) * 16, xoB = ((4 * (par ^ 1) + g) ^ xf) * 16;
    const unsigned wl = (8 * g + (i16 >> 2)) * 128 + ((hd ^ (((i16 >> 3) & 1) | ((g & 1) << 1))) * 32) + (i16 & 3) * 8;
    unsigned x0A = xb0 + xoA, x1A = xb1 + xoA, x0B = xb0 + xoB, x1B = xb1 + xoB, wAa = wl + par * 4096, wBa = wl + (par ^ 1) * 4096;
#define RDX(a, so) lds_ld128((a) + (so))
#define RDW(a, so, t) cat8(tr_read((a) + (so) + (t) * 8192), tr_read((a) + (so) + (t) * 8192 + 512))
    f32x4 aq0 = {}, aq1 = {}, ak0 = {}, ak1 = {}, av0 = {}, av1 = {};
    f32x4 rsum = {}, rsq = {}, cs0 = {}, cs1 = {}, cs2 = {};
    bf16x8 ones8 = {0x3f80, 0x3f80, 0x3f80, 0x3f80, 0x3f80, 0x3f80, 0x3f80, 0x3f80};
    PIN(ones8);
    if (tid < 16) lds_stu(L_RDY + tid * 4, 0u);
    const unsigned pv = L_PRIV + wid * PRIV, po = L_PRIV + (wid ^ 1) * PRIV;
    unsigned ad_rs = (g == (i16 >> 2)) ? L_RS + ((rt * 4 + hd) * 16 + i16) * 8 : pv + lane * 16;
    unsigned ad_cq = (i16 == 0) ? po + 1280 + g * 16 : pv + lane * 16, ad_cv = po + 1280 + 128 + i16 * 4;
    unsigned rd_rs = L_RS + ((rt * 4 + (g & 1) * 2) * 16 + i16) * 8, rd_cq = pv + 1280 + g * 16, rd_cv = pv + 1280 + 128 + i16 * 4;
    unsigned ad_mr = pv + 2048 + r32 * 4, rd_mr = pv + 2048 + (sw * 16 + 4 * g) * 4;
    const int ahead = hq * 4 + hd, tile = rb * 2 + rt;
    unsigned go_k = WS_KV + (ahead * 64 + tile) * 2048 + (lane ^ (sw * 16)) * 16, go_q = WS_Q + (ahead * 64 + tile) * 1024 + (lane ^ (sw * 16)) * 16;
    unsigned go_v = WS_KV + (ahead * 64 + tile) * 2048 + 1024 + (lane ^ (sw * 32)) * 16;
    PIN(ad_rs); PIN(ad_cq); PIN(ad_cv); PIN(rd_rs); PIN(rd_cq); PIN(rd_cv); PIN(ad_mr); PIN(rd_mr); PIN(go_k); PIN(go_q); PIN(go_v);
    PIN(x0A); PIN(x1A); PIN(x0B); PIN(x1B); PIN(wAa); PIN(wBa);
    BARRIER();
    STAMP(1);
    AFrag fa, fb;
    fa.q = RDW(wAa, 0, 0); fa.x0 = RDX(x0A, 0); fa.x1 = RDX(x1A, 0); fa.k = RDW(wAa, 0, 1); fa.v = RDW(wAa, 0, 2);
    fb.q = RDW(wBa, 0, 0); fb.x0 = RDX(x0B, 0); fb.x1 = RDX(x1B, 0); fb.k = RDW(wBa, 0, 1); fb.v = RDW(wBa, 0, 2);
#pragma unroll
    for (int c = 0; c < 7; ++c) {
      asm volatile("s_barrier" ::: "memory");
      FSTAMP(16 + c);
      if (((c + 1) & 1) == 0) {
        const unsigned d = ((c + 1) & 2) ? 65536u : (unsigned)-65536;
        x0A += d; x1A += d; x0B += d; x1B += d; wAa += d; wBa += d;
        PIN(x0A); PIN(x1A); PIN(x0B); PIN(x1B); PIN(wAa); PIN(wBa);
      }
      const unsigned so = ((c + 1) & 1) * 32768;
      SBAR();
      aq0 = MFMA16(fa.q, fa.x0, aq0); aq1 = MFMA16(fa.q, fa.x1, aq1);
      cs0 = MFMA16(fa.q, ones8, cs0);
      SBAR(); fa.q = RDW(wAa, so, 0); SBAR();
      ak0 = MFMA16(fa.k, fa.x0, ak0); ak1 = MFMA16(fa.k, fa.x1, ak1);
      cs1 = MFMA16(fa.k, ones8, cs1);
      SBAR(); fa.k = RDW(wAa, so, 1); SBAR();
      av0 = MFMA16(fa.x0, fa.v, av0); av1 = MFMA16(fa.x1, fa.v, av1);
      SBAR(); fa.x1 = RDX(x1A, so); SBAR();
      cs2 = MFMA16(ones8, fa.v, cs2); rsum = MFMA16(ones8, fa.x0, rsum); rsq = MFMA16(fa.x0, fa.x0, rsq);
      SBAR(); fa.x0 = RDX(x0A, so); fa.v = RDW(wAa, so, 2); SBAR();
      aq0 = MFMA16(fb.q, fb.x0, aq0); aq1 = MFMA16(fb.q, fb.x1, aq1);
      SBAR(); fb.q = RDW(wBa, so, 0); SBAR();
      ak0 = MFMA16(fb.k, fb.x0, ak0); ak1 = MFMA16(fb.k, fb.x1, ak1);
      SBAR(); fb.k = RDW(wBa, so, 1); SBAR();
      av0 = MFMA16(fb.x0, fb.v, av0); av1 = MFMA16(fb.x1, fb.v, av1);
      SBAR(); fb.x0 = RDX(x0B, so); fb.x1 = RDX(x1B, so); fb.v = RDW(wBa, so, 2); SBAR();
    }
    rsum = MFMA16(ones8, fa.x0, rsum); rsq = MFMA16(fa.x0, fa.x0, rsq);
    cs0 = MFMA16(fa.q, ones8, cs0); cs1 = MFMA16(fa.k, ones8, cs1); cs2 = MFMA16(ones8, fa.v, cs2);
    SBAR();
    ak0 = MFMA16(fa.k, fa.x0, ak0); ak1 = MFMA16(fa.k, fa.x1, ak1);
    SBAR();
    {
      const bool b0 = lane & 1, b1 = lane & 2;
      const float dlo = b0 ? rsq[1] : rsq[0], dhi = b0 ? rsq[3] : rsq[2];
      *(LAS f32x2*)(uintptr_t)ad_rs = f32x2{rsum[0], b1 ? dhi : dlo};
      lds_stf4(ad_cq, cs0); lds_stf4(ad_cq + 64, cs1); lds_stf(ad_cv, cs2[0]);
    }
    STAMP(2);
    BARRIER();
    {
      const f32x2 pa = *(const LAS f32x2*)(uintptr_t)rd_rs, pb = *(const LAS f32x2*)(uintptr_t)(rd_rs + 128);
      const f32x4 oq = lds_ldf4(rd_cq), ok = lds_ldf4(rd_cq + 64);
      const float ov = lds_ldf(rd_cv);
      SBAR();
      ak0 = MFMA16(fb.k, fb.x0, ak0); ak1 = MFMA16(fb.k, fb.x1, ak1);
      aq0 = MFMA16(fa.q, fa.x0, aq0); aq1 = MFMA16(fa.q, fa.x1, aq1);
      SBAR();
      float mb, rden;
      ln_stats(pa[0] + pb[0], pa[1] + pb[1], mb, rden);
      aq0 = MFMA16(fb.q, fb.x0, aq0); aq1 = MFMA16(fb.q, fb.x1, aq1);
      av0 = MFMA16(fa.x0, fa.v, av0); av1 = MFMA16(fa.x1, fa.v, av1);
      av0 = MFMA16(fb.x0, fb.v, av0); av1 = MFMA16(fb.x1, fb.v, av1);
      lds_stf(ad_mr, mb); lds_stf(ad_mr + 128, rden);
      const f32x4 cq = cs0 + oq, ck = cs1 + ok;
      const float cv = cs2[0] + ov;
      const auto sm = __builtin_amdgcn_permlane16_swap(__float_as_uint(mb), __float_as_uint(mb), false, false);
      const auto sd = __builtin_amdgcn_permlane16_swap(__float_as_uint(rden), __float_as_uint(rden), false, false);
      const float m0 = __uint_as_float(sw ? sm[1] : sm[0]), m1 = __uint_as_float(sw ? sm[0] : sm[1]);
      const float d0 = __uint_as_float(sw ? sd[1] : sd[0]), d1v = __uint_as_float(sw ? sd[0] : sd[1]);
      WAIT_LGKM0();
      const f32x4 mv0 = lds_ldf4(rd_mr), mv1 = lds_ldf4(rd_mr ^ 64), dv0 = lds_ldf4(rd_mr + 128), dv1 = lds_ldf4((rd_mr ^ 64) + 128);
      unsigned a0, a1, b0, b1;
      a0 = pack_bf16((ak0[0] - m0 * ck[0]) * d0, (ak0[1] - m0 * ck[1]) * d0); a1 = pack_bf16((ak0[2] - m0 * ck[2]) * d0, (ak0[3] - m0 * ck[3]) * d0);
      b0 = pack_bf16((ak1[0] - m1 * ck[0]) * d1v, (ak1[1] - m1 * ck[1]) * d1v); b1 = pack_bf16((ak1[2] - m1 * ck[2]) * d1v, (ak1[3] - m1 * ck[3]) * d1v);
      { auto r0 = __builtin_amdgcn_permlane16_swap(a0, b0, false, false); auto r1 = __builtin_amdgcn_permlane16_swap(a1, b1, false, false);
        ho_st(rs, go_k, u32x4{r0[0], r1[0], r0[1], r1[1]}); }
      const float e0 = d0 * QSCALE, e1 = d1v * QSCALE;
      a0 = pack_bf16((aq0[0] - m0 * cq[0]) * e0, (aq0[1] - m0 * cq[1]) * e0); a1 = pack_bf16((aq0[2] - m0 * cq[2]) * e0, (aq0[3] - m0 * cq[3]) * e0);
      b0 = pack_bf16((aq1[0] - m1 * cq[0]) * e1, (aq1[1] - m1 * cq[1]) * e1); b1 = pack_bf16((aq1[2] - m1 * cq[2]) * e1, (aq1[3] - m1 * cq[3]) * e1);
      { auto r0 = __builtin_amdgcn_permlane16_swap(a0, b0, false, false); auto r1 = __builtin_amdgcn_permlane16_swap(a1, b1, false, false);
        ho_st(rs, go_q, u32x4{r0[0], r1[0], r0[1], r1[1]}); }
      a0 = pack_bf16((av0[0] - mv0[0] * cv) * dv0[0], (av0[1] - mv0[1] * cv) * dv0[1]); a1 = pack_bf16((av0[2] - mv0[2] * cv) * dv0[2], (av0[3] - mv0[3] * cv) * dv0[3]);
      b0 = pack_bf16((av1[0] - mv1[0] * cv) * dv1[0], (av1[1] - mv1[1] * cv) * dv1[1]); b1 = pack_bf16((av1[2] - mv1[2] * cv) * dv1[2], (av1[3] - mv1[3] * cv) * dv1[3]);
      { auto r0 = __builtin_amdgcn_permlane32_swap(a0, b0, false, false); auto r1 = __builtin_amdgcn_permlane32_swap(a1, b1, false, false);
        ho_st(rs, go_v, u32x4{r0[0], r1[0], r0[1], r1[1]}); }
    }
    tag = __builtin_amdgcn_readfirstlane(tag) + 1u;
    STAMP(3);
    asm volatile("s_waitcnt vmcnt(0)" : "+v"(code_pf) :: "memory");
    if (lane == 0) {
      __builtin_amdgcn_raw_buffer_store_b64(u32x2{tag, ~tag}, rs, WS_FA + (ahead * 64 + tile) * 128, 0, 16);
      __hip_atomic_fetch_add((LAS unsigned*)(uintptr_t)(L_RDY + 36), 1u, __ATOMIC_RELAXED, __HIP_MEMORY_SCOPE_WORKGROUP);
    }
    STAMP(4);
  }

  const int qg = wid & 3, kh = wid >> 2;
  u32x4 fw0 = {}, fw1 = {}, fw2 = {}, fw3 = {}, fw4 = {}, fw5 = {};
  const char* fb0 = isd1 ? (const char*)wg + wid * (64 * F * 2) + d1 * 32 : (const char*)wd + wid * (32 * D * 2) + sub * 32;
  const char* fb2 = isd1 ? (const char*)wu + wid * (64 * F * 2) + d1 * 32 : fb0 + 2 * (256 * D * 2);
  const unsigned fst = isd1 ? 32 * F * 2 : 256 * D * 2;
  const unsigned fs4 = isd1 ? 0u : 2 * fst, fs5 = (isd1 || wid >= 3) ? 0u : 3 * fst;
  unsigned fvo = (lane >> 1) * (isd1 ? F * 2 : D * 2) + (lane & 1) * 16;
  const int fit = (isd1 || isd2) ? 6 : -1;
  f32x16 oa = {}, ob = {};
  bf16x8 qa, qb2;
  float mref;
  {
    unsigned ka = kh * 32 * KVT + lane * 16;
    unsigned va0 = kh * 32 * KVT + (r32 < 16 ? 1024 + (hh * 16 + r32) * 16 : (r32 == 16 ? 2048 : 2064));
    unsigned va1 = va0 + (r32 < 16 ? 512 : 0);
    unsigned qad = L_STG + qg * 2048 + kh * 1024 + lane * 16;
    u32x4 p0, p1 = {0, 0, 0, 0};
    PIN(ka); PIN(va0); PIN(va1); PIN(qad); PIN(fvo); PIN(oa); PIN(ob); PIN(p1);
    PIN(fw0); PIN(fw1); PIN(fw2); PIN(fw3); PIN(fw4); PIN(fw5);
    __builtin_amdgcn_s_setprio(2);
    wait_rdy(1u);
    STAMP(6);
    bf16x8 k0 = lds_ld128(ka), k1;
    qa = lds_ld128(qad); qb2 = lds_ld128(qad ^ 1024);
    bf16x8 v00 = lds_ld128(va0), v01 = lds_ld128(va1), v10, v11 = lds_ld128(va0);
    f32x16 ng, sA, sB;
    {
      const f32x16 z = {};
      sA = MFMA32(k0, qa, z); sB = MFMA32(k0, qb2, z);
      mref = max_halves(fmaxf(tile_max(sA), tile_max(sB)));
    }
#pragma unroll
    for (int j = 0; j < 16; ++j) { ng[j] = -mref; sA[j] -= mref; }
    PIN(ng); PIN(sA);
    for (int it = 0; it < 8; ++it) {
      FSTAMP(32 + 2 * it);
      const unsigned need = (0x8042u >> (4 * it)) & 15u;
      if (need) {
        wait_rdy(need);
        if (it == 0 && fresh_lane() == 0) __hip_atomic_fetch_add((LAS unsigned*)(uintptr_t)(L_RDY + 16 + qg * 4), 1u, __ATOMIC_RELAXED, __HIP_MEMORY_SCOPE_WORKGROUP);
      }
      { unsigned tv_;
        asm volatile("s_cmp_lg_u32 %[it], %[fit]\n\ts_cbranch_scc1 .Lff%=\n\t"
          "global_load_dwordx4 %[a0], %[vo], %[b0]\n\tglobal_load_dwordx4 %[a2], %[vo], %[b2]\n\tv_add_u32 %[tv], %[st], %[vo]\n\t"
          "global_load_dwordx4 %[a1], %[tv], %[b0]\n\tglobal_load_dwordx4 %[a3], %[tv], %[b2]\n\tv_add_u32 %[tv], %[s4], %[vo]\n\t"
          "global_load_dwordx4 %[a4], %[tv], %[b2]\n\tv_add_u32 %[tv], %[s5], %[vo]\n\t"
          "global_load_dwordx4 %[a5], %[tv], %[b2]\n.Lff%=:\n\t"
          "s_cmp_lt_u32 %[it], 5\n\ts_cbranch_scc1 .Lpr%=\n\ts_setprio 1\n\ts_cmp_lt_u32 %[it], 7\n\ts_cbranch_scc1 .Lpr%=\n\ts_setprio 0\n.Lpr%=:"
          : [a0] "+v"(fw0), [a1] "+v"(fw1), [a2] "+v"(fw2), [a3] "+v"(fw3), [a4] "+v"(fw4), [a5] "+v"(fw5), [tv] "=&v"(tv_)
          : [it] "s"(it), [fit] "s"(fit), [vo] "v"(fvo), [b0] "s"(fb0), [b2] "s"(fb2), [st] "s"(fst), [s4] "s"(fs4), [s5] "s"(fs5) : "memory", "scc"); }
      SBAR();
      TWO_TILES(0);
      TWO_TILES(2);
      ka += 4 * KVT; va0 += 4 * KVT; va1 += 4 * KVT;
    }
    ob = MFMA32(v11, BC8(p1), ob);
  }
  asm volatile("s_waitcnt vmcnt(0)" : "+v"(fw0), "+v"(fw1), "+v"(fw2), "+v"(fw3), "+v"(fw4), "+v"(fw5) :: "memory");
  {
  RELANE();
  const unsigned priv = L_PRIV + wid * PRIV;
  float ma = mref, mb = mref;
  float la = sum_halves(oa[8]), lb = sum_halves(ob[8]);
  {
    float t = 0.f;
#pragma unroll
    for (int j = 0; j < 8; ++j) t += fabsf(oa[j]) + fabsf(ob[j]);
    const bool ok = (la <= 16777216.f) && (lb <= 16777216.f) && (la >= 5.9604645e-8f) && (lb >= 5.9604645e-8f) && (t <= 3.0e38f);
    if (__builtin_expect(!__all(ok), 0)) {
      const unsigned kaddr = kh * 32 * KVT + lane * 16;
      const unsigned vaddr0 = kh * 32 * KVT + (r32 < 16 ? 1024 + (hh * 16 + r32) * 16 : (r32 == 16 ? 2048 : 2064));
      const unsigned vaddr1 = vaddr0 + (r32 < 16 ? 512 : 0);
      const f32x16 z = {};
      ma = -INFINITY; mb = -INFINITY;
      for (int t2 = 0; t2 < 32; ++t2) {
        const bf16x8 kf = lds_ld128(kaddr + t2 * KVT);
        ma = fmaxf(ma, tile_max(MFMA32(kf, qa, z)));
        mb = fmaxf(mb, tile_max(MFMA32(kf, qb2, z)));
      }
      ma = max_halves(ma); mb = max_halves(mb);
      oa = z; ob = z;
      for (int t2 = 0; t2 < 32; ++t2) {
        const bf16x8 kf = lds_ld128(kaddr + t2 * KVT), v0 = lds_ld128(vaddr0 + t2 * KVT), v1 = lds_ld128(vaddr1 + t2 * KVT);
        const f32x16 sa = MFMA32(kf, qa, z), sb = MFMA32(kf, qb2, z);
        u32x4 pa0, pa1, pb0, pb1;
#pragma unroll
        for (int j = 0; j < 4; ++j) {
          pa0[j] = PK(EX(sa[2 * j] - ma), EX(sa[2 * j + 1] - ma)); pa1[j] = PK(EX(sa[8 + 2 * j] - ma), EX(sa[9 + 2 * j] - ma));
          pb0[j] = PK(EX(sb[2 * j] - mb), EX(sb[2 * j + 1] - mb)); pb1[j] = PK(EX(sb[8 + 2 * j] - mb), EX(sb[9 + 2 * j] - mb));
        }
        oa = MFMA32(v0, BC8(pa0), oa); oa = MFMA32(v1, BC8(pa1), oa);
        ob = MFMA32(v0, BC8(pb0), ob); ob = MFMA32(v1, BC8(pb1), ob);
      }
      la = sum_halves(oa[8]); lb = sum_halves(ob[8]);
    }
  }
  STAMP(7);
  const int row = tid >> 4, c8 = tid & 15;
  const int goff = ((tt * 32 + row) * D + cbk * 128 + c8 * 8) * 2;
  const int po_probe = HO_O + (tt * H + wid * 4 + (lane & 3)) * 1024 + 1020;
  unsigned probe;
  {
    lds_stf4(priv + lane * 16, f32x4{ob[0], ob[1], ob[2], ob[3]}); lds_stf4(priv + 1024 + lane * 16, f32x4{ob[4], ob[5], ob[6], ob[7]});
    *(LAS f32x2*)(uintptr_t)(priv + 2048 + lane * 8) = f32x2{lb, mb};
    BARRIER();
    probe = __builtin_amdgcn_raw_buffer_load_b32(rs, po_probe, 0, 16);
    SBAR();
    const unsigned yp = L_PRIV + (wid ^ 4) * PRIV;
    const f32x2 y2 = *(const LAS f32x2*)(uintptr_t)(yp + 2048 + lane * 8);
    const f32x4 y0 = lds_ldf4(yp + lane * 16), y1 = lds_ldf4(yp + 1024 + lane * 16);
    const float M = fmaxf(ma, y2[1]);
    float f1 = EX(ma - M), f2 = EX(y2[1] - M);
    const float inv = RCP(la * f1 + y2[0] * f2);
    f1 *= inv; f2 *= inv;
    u32x4 po;
    po[0] = san(pack_bf16(oa[0] * f1 + y0[0] * f2, oa[1] * f1 + y0[1] * f2));
    po[1] = san(pack_bf16(oa[2] * f1 + y0[2] * f2, oa[3] * f1 + y0[3] * f2));
    po[2] = san(pack_bf16(oa[4] * f1 + y1[0] * f2, oa[5] * f1 + y1[1] * f2));
    po[3] = san(pack_bf16(oa[6] * f1 + y1[2] * f2, oa[7] * f1 + y1[3] * f2));
    ho_st(rs, HO_O + ((qb * 8 + qg * 2 + kh) * H + head) * 1024 + lane * 16, po);
  }
  STAMP(8);

  {
    const int oo = HO_O + (tt * H + wid * 4) * 1024 + lane * 16;
    for (unsigned spin = 0;; ++spin) {
      if (__builtin_amdgcn_ballot_w64(probe == SENT) == 0 || spin > SPIN_CAP) break;
      POLL_PAUSE(1);
      probe = __builtin_amdgcn_raw_buffer_load_b32(rs, po_probe, 0, 16);
    }
    u32x4 o0 = ho_ld(rs, oo), o1 = ho_ld(rs, oo + 1024), o2 = ho_ld(rs, oo + 2048), o3 = ho_ld(rs, oo + 3072);
    for (unsigned spin = 0;; ++spin) {
      const unsigned long long p0 = __builtin_amdgcn_ballot_w64(pend(o0)), p1 = __builtin_amdgcn_ballot_w64(pend(o1));
      const unsigned long long p2 = __builtin_amdgcn_ballot_w64(pend(o2)), p3 = __builtin_amdgcn_ballot_w64(pend(o3));
      if ((p0 | p1 | p2 | p3) == 0 || spin > SPIN_CAP) break;
      POLL_PAUSE(1);
      if (p0) o0 = ho_ld(rs, oo);
      if (p1) o1 = ho_ld(rs, oo + 1024);
      if (p2) o2 = ho_ld(rs, oo + 2048);
      if (p3) o3 = ho_ld(rs, oo + 3072);
    }
    const unsigned oa2 = wid * 4096 + lane * 16;
    lds_stu4(oa2, o0); lds_stu4(oa2 + 1024, o1); lds_stu4(oa2 + 2048, o2); lds_stu4(oa2 + 3072, o3);
    WAIT_LGKM0();
    if (lane == 0) __hip_atomic_fetch_or((LAS unsigned*)(uintptr_t)(L_RDY + 32), 1u << wid, __ATOMIC_RELAXED, __HIP_MEMORY_SCOPE_WORKGROUP);
  }
  STAMP(9);
  if (isd1) {
    const unsigned fa = F_OFF + wid * 2048 + lane * 16;
    lds_stu4(fa, fw0); lds_stu4(fa + 1024, fw1); lds_stu4(fa + 16384, fw2); lds_stu4(fa + 17408, fw3);
  } else if (isd2) {
    const unsigned fa = F_OFF + wid * 1024 + lane * 16;
    lds_stu4(fa, fw0); lds_stu4(fa + 8192, fw1); lds_stu4(fa + 16384, fw2); lds_stu4(fa + 24576, fw3); lds_stu4(fa + 32768, fw4); lds_stu4(fa + 40960, fw5);
  }
  WAIT_LGKM0();
  bf16x8 ff[6];
  {
    const unsigned ft = (8 * g + (i16 >> 2)) * 32 + (i16 & 3) * 8;
    const unsigned fl = F_OFF + wid * (isd1 ? 2048 : 1024) + ft, s1 = isd1 ? 1024 : 8192;
    ff[0] = cat8(tr_read(fl), tr_read(fl + 128)); ff[1] = cat8(tr_read(fl + s1), tr_read(fl + s1 + 128));
    ff[2] = cat8(tr_read(fl + 16384), tr_read(fl + 16384 + 128)); ff[3] = cat8(tr_read(fl + 16384 + s1), tr_read(fl + 16384 + s1 + 128));
    ff[4] = cat8(tr_read(fl + 32768), tr_read(fl + 32768 + 128)); ff[5] = cat8(tr_read(fl + 40960), tr_read(fl + 40960 + 128));
  }
  if (wid < 6) {
    const int rec = ((cbk + 1 + (wid >> 1)) & 3) * 4 + (wid & 1) * 2 + hh;
    u32x4 sv[1];
    poll_all<1>(rs, HO_ST + (tt * 16 + rec) * 512 + r32 * 16, 0, sv);
    lds_stu4(S_OFF + rec * 528 + r32 * 16, sv[0]);
    STAMP(10);
  }
  BARRIER();
  STAMP(11);
  const u32x4 hv = lds_ldu4(X_OFF + row * 272 + c8 * 16);
  u32x4 ov;
  {
    const f32x2 pr = *(const LAS f32x2*)(uintptr_t)(S_OFF + c8 * 528 + row * 16);
    f32x2 st;
    { float mbv, rden; ln_stats(row16_sum(pr[0]), row16_sum(pr[1]), mbv, rden); st = f32x2{mbv, rden}; }
    u32x4 lv;
#pragma unroll
    for (int j = 0; j < 4; ++j) {
      const float h0 = bf_lo(hv[j]), h1 = bf_hi(hv[j]);
      const unsigned d = pack_bf16(h0 - st[0], h1 - st[0]);
      lv[j] = pack_bf16(bf_lo(d) * st[1], bf_hi(d) * st[1]);
      ov[j] = pack_bf16(h0 + bf_lo(lv[j]), h1 + bf_hi(lv[j]));
    }
    lv = u32x4{san(lv[0]), san(lv[1]), san(lv[2]), san(lv[3])};
    PIN(lv);
    BARRIER();
    ho_st(rs, HO_LN + goff, lv);
  }
  BARRIER(); BARRIER(); BARRIER();
  STAMP(13);
  const unsigned mykey = lds_ldu(T_OFF + 640 + (lane & 7) * 4);
  const int myrow = 2047 - (int)(mykey & 0x7ffu);
  if (isd1) {
    u32x4 ax[2];
    poll_all<2>(rs, HO_LN + (myrow * D + wid * 64 + g * 8) * 2, 64, ax);
    f32x4 ag = {}, au = {};
    ag = MFMA16(BC8(ax[0]), ff[0], ag); au = MFMA16(BC8(ax[0]), ff[2], au);
    ag = MFMA16(BC8(ax[1]), ff[1], ag); au = MFMA16(BC8(ax[1]), ff[3], au);
    lds_stf4(R_OFF + wid * 1024 + lane * 16, hh ? au : ag);
    BARRIER();
    if (wid == 0) {
      f32x4 sum = lds_ldf4(R_OFF + lane * 16);
#pragma unroll
      for (int w2 = 1; w2 < 8; ++w2) sum += lds_ldf4(R_OFF + w2 * 1024 + lane * 16);
      const auto x0 = __builtin_amdgcn_permlane32_swap(__float_as_uint(sum[0]), __float_as_uint(sum[2]), false, false);
      const auto x1 = __builtin_amdgcn_permlane32_swap(__float_as_uint(sum[1]), __float_as_uint(sum[3]), false, false);
      const float g0 = rbf(__uint_as_float(x0[0])), u0 = rbf(__uint_as_float(x0[1])), g1 = rbf(__uint_as_float(x1[0])), u1 = rbf(__uint_as_float(x1[1]));
      const float a0f = rbf(g0 * sigmoid(g0)) * u0, a1f = rbf(g1 * sigmoid(g1)) * u1;
      const float n0 = dpp_f<DPP_X1>(a0f), n1 = dpp_f<DPP_X1>(a1f);
      const bool odd = lane & 1;
      const int tok = 4 * (g & 1) + 2 * hh + (odd ? 1 : 0);
      ho_st1(rs, HO_ACT + d1 * 256 + tok * 32 + (i16 >> 1) * 4, san(odd ? pack_bf16(n1, a1f) : pack_bf16(a0f, n0)));
    }
    STAMP(14);
  }
  {
    const int grow = tt * 32 + row;
    int sel = -1;
#pragma unroll
    for (int c = 0; c < 8; ++c) if (__builtin_amdgcn_readlane(myrow, c) == grow) sel = c;
    if (sel < 0) __builtin_amdgcn_raw_buffer_store_b128(ov, __builtin_amdgcn_make_buffer_rsrc(out, 0, S * D * 2, 0x00020000), goff, 0, 16);
    else ho_st(rs, HO_HS + (sel * D + cbk * 128 + c8 * 8) * 2, u32x4{san(hv[0]), san(hv[1]), san(hv[2]), san(hv[3])});
    if (b == 0 && tid == 0) __builtin_amdgcn_raw_buffer_store_b32(tag, rs, WS_EP, 0, 16);
  }
  if (isd2) {
    const int n0 = sub * 16;
    unsigned hw = SENT, lw = SENT, fkey = 0;
    const bool odd = lane & 1;
    const int tok = 4 * (g & 1) + 2 * hh + (odd ? 1 : 0), cp = i16 >> 1;
    float sig = 0.f;
    if (wid == 0) {
      fkey = lds_ldu(T_OFF + 640 + tok * 4);
      const int frow = 2047 - (int)(fkey & 0x7ffu);
      sig = rbf(sigmoid(key_score(fkey)));
      for (unsigned spin = 0;; ++spin) {
        if (hw == SENT) hw = __builtin_amdgcn_raw_buffer_load_b32(rs, HO_HS + (tok * D + n0 + cp * 2) * 2, 0, 16);
        if (lw == SENT) lw = __builtin_amdgcn_raw_buffer_load_b32(rs, HO_LN + (frow * D + n0 + cp * 2) * 2, 0, 16);
        if (!__any(hw == SENT || lw == SENT) || spin > SPIN_CAP) break;
        POLL_PAUSE(1);
      }
    }
    u32x4 av[6];
    const int ao = HO_ACT + ((wid * 2 + (g >> 1)) * 8 + (lane & 7)) * 32 + (g & 1) * 16;
    poll_all<6>(rs, ao, 4096, av, wid < 3 ? 0 : 4096);
    STAMP(14);
    f32x4 y = {}, y2 = {};
    y = MFMA16(BC8(av[0]), ff[0], y); y2 = MFMA16(BC8(av[1]), ff[1], y2);
    y = MFMA16(BC8(av[2]), ff[2], y); y2 = MFMA16(BC8(av[3]), ff[3], y2);
    y = MFMA16(BC8(av[4]), ff[4], y);
    if (wid < 3) y2 = MFMA16(BC8(av[5]), ff[5], y2);
    lds_stf4(R_OFF + wid * 1024 + lane * 16, hh ? y2 : y);
    BARRIER();
    if (wid == 0) {
      f32x4 sum = lds_ldf4(R_OFF + lane * 16);
#pragma unroll
      for (int w2 = 1; w2 < 8; ++w2) sum += lds_ldf4(R_OFF + w2 * 1024 + lane * 16);
      const auto x0 = __builtin_amdgcn_permlane32_swap(__float_as_uint(sum[0]), __float_as_uint(sum[2]), false, false);
      const auto x1 = __builtin_amdgcn_permlane32_swap(__float_as_uint(sum[1]), __float_as_uint(sum[3]), false, false);
      const float t0 = __uint_as_float(x0[0]) + __uint_as_float(x0[1]), t1 = __uint_as_float(x1[0]) + __uint_as_float(x1[1]);
      const float n0f = dpp_f<DPP_X1>(t0), n1f = dpp_f<DPP_X1>(t1);
      const float ylo = rbf(rbf(odd ? n1f : t0) * sig), yhi = rbf(rbf(odd ? t1 : n0f) * sig);
      *(unsigned*)((char*)out + ((size_t)(2047 - (int)(fkey & 0x7ffu)) * D + n0 + cp * 2) * 2) =
          pack_bf16(bf_lo(hw) + rbf(bf_lo(lw) + ylo), bf_hi(hw) + rbf(bf_hi(lw) + yhi));
    }
  }
  }
  STAMP(15); STAMP_OUT(0);
}

extern "C" void kernel_launch(void* const* d_in, const int* in_sizes, int n_in, void* d_out, int out_size, void* d_ws, size_t ws_size,
                              hipStream_t stream) {
  (void)hipFuncSetAttribute((const void*)k_fused, hipFuncAttributeMaxDynamicSharedMemorySize, LDS_BYTES);
  k_fused<<<256, 768, LDS_BYTES, stream>>>((const uint16_t*)d_in[0], (const uint16_t*)d_in[1], (const uint16_t*)d_in[2], (const uint16_t*)d_in[4],
                                           (const uint16_t*)d_in[5], (const uint16_t*)d_in[6], (const uint16_t*)d_in[7], (char*)d_ws, (uint16_t*)d_out);
}
```

```cpp
#include <hip/hip_runtime.h>
#include <hip/hip_bf16.h>
#include <stdint.h>

typedef short bf16x8 __attribute__((ext_vector_type(8)));
typedef short bf16x4 __attribute__((ext_vector_type(4)));
typedef float f32x16 __attribute__((ext_vector_type(16)));
typedef float f32x8 __attribute__((ext_vector_type(8)));
typedef float f32x4 __attribute__((ext_vector_type(4)));
typedef __bf16 b16x8 __attribute__((ext_vector_type(8)));
typedef __bf16 b16x16 __attribute__((ext_vector_type(16)));
typedef float f32x2 __attribute__((ext_vector_type(2)));
typedef __bf16 bf16x2_t __attribute__((ext_vector_type(2)));
typedef unsigned u32x4 __attribute__((ext_vector_type(4)));
typedef unsigned u32x2 __attribute__((ext_vector_type(2)));
#define LAS __attribute__((address_space(3)))
#define GAS __attribute__((address_space(1)))
#define DI __device__ __forceinline__
#define MFMA32(a, b, c) __builtin_amdgcn_mfma_f32_32x32x16_bf16((a), (b), (c), 0, 0, 0)
#define MFMA16(a, b, c) __builtin_amdgcn_mfma_f32_16x16x32_bf16((a), (b), (c), 0, 0, 0)
#define SBAR() __builtin_amdgcn_sched_barrier(0)
#define PIN(x) asm volatile("" : "+v"(x))
#define BC8(x) __builtin_bit_cast(bf16x8, x)

constexpr int S = 2048, D = 512, H = 32, F = 1376;
constexpr float LN_EPS = 1e-5f;
constexpr float QSCALE = 0.25f * 1.4426950408889634f;
constexpr unsigned SENT = 0xFFFFFFFFu;
constexpr unsigned SPIN_CAP = 1u << 16;

constexpr int WS_Q = 0;
constexpr int WS_KV = WS_Q + H * 64 * 1024;
constexpr int WS_FA = WS_KV + H * 64 * 2048;
constexpr int WS_FR = WS_FA + H * 64 * 128;
constexpr int WS_EP = WS_FR + 4096;
constexpr int WS_HO = WS_EP + 4096;
constexpr int HO_O = WS_HO;
constexpr int HO_LN = HO_O + 64 * H * 1024;
constexpr int HO_ST = HO_LN + S * D * 2;
constexpr int HO_KEY = HO_ST + 64 * 16 * 512;
constexpr int HO_ACT = HO_KEY + 8 * S * 4;
constexpr int HO_HS = HO_ACT + 24576;
constexpr int HO_END = HO_HS + 8192;
constexpr int HO_PER_WG = 19 * 1024;
static_assert(WS_HO + 256 * HO_PER_WG >= HO_END, "reset covers the hand-off words");
constexpr int WS_BYTES = WS_HO + 256 * HO_PER_WG;

#define STAMP_DECL do {} while (0)
#define STAMP(i) do {} while (0)
#define STAMP_OUT(k) do {} while (0)

#define FSTAMP(i) do {} while (0)

DI unsigned pack_bf16(float lo, float hi) {
  f32x2 v = {lo, hi};
  bf16x2_t r = __builtin_convertvector(v, bf16x2_t);
  return __builtin_bit_cast(unsigned, r);
}
DI float bf_lo(unsigned v) { return __uint_as_float(v << 16); }
DI float bf_hi(unsigned v) { return __uint_as_float(v & 0xffff0000u); }
DI float rbf(float x) { return bf_lo(pack_bf16(x, x)); }
DI float sum_halves(float v) {
  auto r = __builtin_amdgcn_permlane32_swap(__float_as_uint(v), __float_as_uint(v), false, false);
  return __uint_as_float(r[0]) + __uint_as_float(r[1]);
}
DI float sum_rows(float v) {
  auto r = __builtin_amdgcn_permlane16_swap(__float_as_uint(v), __float_as_uint(v), false, false);
  return sum_halves(__uint_as_float(r[0]) + __uint_as_float(r[1]));
}
DI float max_halves(float v) {
  auto r = __builtin_amdgcn_permlane32_swap(__float_as_uint(v), __float_as_uint(v), false, false);
  return fmaxf(__uint_as_float(r[0]), __uint_as_float(r[1]));
}
template <int CTRL> DI float dpp_f(float v) { return __uint_as_float(__builtin_amdgcn_update_dpp(0u, __float_as_uint(v), CTRL, 0xf, 0xf, true)); }
template <int CTRL> DI unsigned dpp_u(unsigned v) { return __builtin_amdgcn_update_dpp(0u, v, CTRL, 0xf, 0xf, true); }
constexpr int DPP_X1 = 0xB1, DPP_X2 = 0x4E, DPP_HMIR = 0x141, DPP_MIR = 0x140;
DI float quad_sum(float v) { v += dpp_f<DPP_X1>(v); v += dpp_f<DPP_X2>(v); return v; }
DI float row16_sum(float v) { v = quad_sum(v); v += dpp_f<DPP_HMIR>(v); v += dpp_f<DPP_MIR>(v); return v; }
DI void glds16(const void* sbase, unsigned voff, unsigned lds_base) {
  unsigned sv;
  asm volatile("s_mov_b32 %0, m0\n\ts_mov_b32 m0, %3\n\ts_nop 0\n\tglobal_load_lds_dwordx4 %1, %2\n\ts_mov_b32 m0, %0"
               : "=&s"(sv) : "v"(voff), "s"(sbase), "s"(lds_base) : "memory");
}
DI void glds16s(const void* sbase, unsigned voff, unsigned lds_base) {
  unsigned sv;
  asm volatile("s_mov_b32 %0, m0\n\ts_mov_b32 m0, %3\n\ts_nop 0\n\tglobal_load_lds_dwordx4 %1, %2 sc1\n\ts_mov_b32 m0, %0"
               : "=&s"(sv) : "v"(voff), "s"(sbase), "s"(lds_base) : "memory");
}
DI void glds4(const void* sbase, unsigned voff, unsigned lds_base) {
  unsigned sv;
  asm volatile("s_mov_b32 %0, m0\n\ts_mov_b32 m0, %3\n\ts_nop 0\n\tglobal_load_lds_dword %1, %2\n\ts_mov_b32 m0, %0"
               : "=&s"(sv) : "v"(voff), "s"(sbase), "s"(lds_base) : "memory");
}
#define WAIT_VM(N) asm volatile("s_waitcnt vmcnt(" #N ")" ::: "memory")
#define WAIT_LGKM0() asm volatile("s_waitcnt lgkmcnt(0)" ::: "memory")
#define BARRIER() asm volatile("s_waitcnt lgkmcnt(0)\n\ts_barrier" ::: "memory")
typedef short v4i16_t __attribute__((ext_vector_type(4)));
DI bf16x4 tr_read(unsigned addr) {
  return __builtin_bit_cast(bf16x4, __builtin_amdgcn_ds_read_tr16_b64_v4i16((LAS v4i16_t*)(uintptr_t)addr));
}
DI bf16x8 lds_ld128(unsigned addr) { return *(const LAS bf16x8*)(uintptr_t)addr; }
DI f32x4 lds_ldf4(unsigned addr) { return *(const LAS f32x4*)(uintptr_t)addr; }
DI u32x4 lds_ldu4(unsigned addr) { return *(const LAS u32x4*)(uintptr_t)addr; }
DI void lds_stf4(unsigned addr, f32x4 v) { *(LAS f32x4*)(uintptr_t)addr = v; }
DI void lds_stu4(unsigned addr, u32x4 v) { *(LAS u32x4*)(uintptr_t)addr = v; }
DI void lds_stf(unsigned addr, float v) { *(LAS float*)(uintptr_t)addr = v; }
DI void lds_stu(unsigned addr, unsigned v) { *(LAS unsigned*)(uintptr_t)addr = v; }
DI float lds_ldf(unsigned addr) { return *(const LAS float*)(uintptr_t)addr; }
DI unsigned lds_ldu(unsigned addr) { return *(const LAS unsigned*)(uintptr_t)addr; }
DI bf16x8 cat8(bf16x4 a, bf16x4 b) { return __builtin_shufflevector(a, b, 0, 1, 2, 3, 4, 5, 6, 7); }
typedef __amdgpu_buffer_rsrc_t rsrc_t;
DI u32x4 ho_ld(rsrc_t rs, int off) { return __builtin_amdgcn_raw_buffer_load_b128(rs, off, 0, 16); }
DI void ho_st(rsrc_t rs, int off, u32x4 v) { __builtin_amdgcn_raw_buffer_store_b128(v, rs, off, 0, 16); }
DI void ho_st1(rsrc_t rs, int off, unsigned v) { __builtin_amdgcn_raw_buffer_store_b32(v, rs, off, 0, 16); }
DI unsigned san(unsigned v) { return min(v, SENT - 1u); }
#define POLL_PAUSE(n) do { __builtin_amdgcn_s_sleep(n); asm volatile("" ::: "memory"); } while (0)
DI bool pend(u32x4 v) { return max(max(v[0], v[1]), max(v[2], v[3])) == SENT; }

constexpr int LDS_BYTES = 163840;
constexpr int KVT = 2080;
constexpr int L_PRIV = 64 * KVT, PRIV = 2560;
constexpr int L_STG = L_PRIV + 8 * PRIV;
constexpr int L_RDY = L_STG + 4 * 2048;
constexpr int L_RS = L_RDY + 64;
static_assert(L_RS + 1024 <= LDS_BYTES, "lds map");
constexpr unsigned X_OFF = 32768, S_OFF = 45056, T_OFF = 55296, R_OFF = 57344, F_OFF = 81920;
constexpr int N_D1 = 86;
constexpr int CODE_PF_BYTES = 25 * 1024;
struct AFrag { bf16x8 x0, x1, q, k, v; };

#define EX(x) __builtin_amdgcn_exp2f(x)
#define RCP(x) __builtin_amdgcn_rcpf(x)
#define SMF(a, b, c) __builtin_amdgcn_smfmac_f32_32x32x32_bf16(__builtin_bit_cast(b16x8, a), __builtin_bit_cast(b16x16, b), (c), sidx, 0, 0)
DI f32x8 lo8(const f32x16& s) { return __builtin_shufflevector(s, s, 0, 1, 2, 3, 4, 5, 6, 7); }
DI f32x8 hi8(const f32x16& s) { return __builtin_shufflevector(s, s, 8, 9, 10, 11, 12, 13, 14, 15); }
#define UNIT(SC, SN, KN, QN, NEGN, OPREV, VPREV, OCUR, VCUR0, LOADS) do { \
    SC[0] = EX(SC[0]); SC[1] = EX(SC[1]); SBAR(); \
    OPREV = SMF(VPREV, hi8(SN), OPREV); \
    LOADS; \
    SBAR(); \
    SC[2] = EX(SC[2]); SC[3] = EX(SC[3]); SC[4] = EX(SC[4]); SC[5] = EX(SC[5]); SBAR(); \
    SN = MFMA32(KN, QN, NEGN); SBAR(); \
    SC[6] = EX(SC[6]); SC[7] = EX(SC[7]); SC[8] = EX(SC[8]); SC[9] = EX(SC[9]); SC[10] = EX(SC[10]); SBAR(); \
    OCUR = SMF(VCUR0, lo8(SC), OCUR); SBAR(); \
    SC[11] = EX(SC[11]); SC[12] = EX(SC[12]); SC[13] = EX(SC[13]); SC[14] = EX(SC[14]); SC[15] = EX(SC[15]); SBAR(); \
  } while (0)
#define TWO_TILES(U) do { \
    UNIT(sA, sB, k0, qb2, ng, ob, v11, oa, v00, k1 = lds_ld128(ka + (U + 1) * KVT); v10 = lds_ld128(va0 + (U + 1) * KVT)); \
    UNIT(sB, sA, k1, qa, ng, oa, v01, ob, v00, v11 = lds_ld128(va1 + (U + 1) * KVT)); \
    UNIT(sA, sB, k1, qb2, ng, ob, v01, oa, v10, k0 = lds_ld128(ka + (U + 2) * KVT); v00 = lds_ld128(va0 + (U + 2) * KVT)); \
    UNIT(sB, sA, k0, qa, ng, oa, v11, ob, v10, v01 = lds_ld128(va1 + (U + 2) * KVT)); \
  } while (0)

DI int fresh_lane() {
  int l;
  asm volatile("v_mbcnt_lo_u32_b32 %0, -1, 0\n\tv_mbcnt_hi_u32_b32 %0, -1, %0" : "=v"(l));
  return l;
}
#define RELANE() const int lane = fresh_lane(), tid = wid * 64 + lane, r32 = lane & 31, hh = lane >> 5, g = lane >> 4, i16 = lane & 15; \
  (void)tid; (void)r32; (void)hh; (void)g; (void)i16
DI float tile_max(const f32x16& s) {
  float m = fmaxf(fmaxf(s[0], s[1]), s[2]);
#pragma unroll
  for (int j = 3; j < 15; j += 2) m = fmaxf(fmaxf(m, s[j]), s[j + 1]);
  return fmaxf(m, s[15]);
}
DI unsigned sort_key(float r, int idx) {
  unsigned u = pack_bf16(r, r) & 0xffffu;
  if ((u & 0x7fffu) > 0x7f80u) u = 0xffffu;
  u = (u & 0x8000u) ? (~u & 0xffffu) : (u | 0x8000u);
  return (u << 11) | (unsigned)(2047 - idx);
}
DI float key_score(unsigned key) {
  unsigned u = (key >> 11) & 0xffffu;
  u = (u & 0x8000u) ? (u & 0x7fffu) : (~u & 0xffffu);
  return __uint_as_float(u << 16);
}
DI void ln_stats(float s1, float s2, float& mb, float& rden) {
  const float mean = s1 * (1.f / D);
  const float var = fmaxf(s2 * (1.f / D) - mean * mean, 0.f);
  mb = rbf(mean);
  rden = RCP(rbf(__builtin_amdgcn_sqrtf(rbf(rbf(var) + LN_EPS))));
}
DI float sigmoid(float v) { return RCP(1.f + EX(v * -1.4426950408889634f)); }
DI void wait_rdy(unsigned need) {
  for (unsigned spin = 0;; ++spin) {
    const u32x4 r = *(volatile const LAS u32x4*)(uintptr_t)L_RDY;
    const unsigned m = min(min(r[0], r[1]), min(r[2], r[3]));
    if (__builtin_amdgcn_readfirstlane(m) >= need || spin > (SPIN_CAP << 4)) break;
    __builtin_amdgcn_s_sleep(1);
  }
  asm volatile("" ::: "memory");
}
DI void wait_word(unsigned addr, unsigned need) {
  for (unsigned spin = 0;; ++spin) {
    const unsigned r = *(volatile const LAS unsigned*)(uintptr_t)addr;
    if (__builtin_amdgcn_readfirstlane(r) >= need || spin > (SPIN_CAP << 4)) break;
    __builtin_amdgcn_s_sleep(1);
  }
  asm volatile("" ::: "memory");
}
DI unsigned gt_bit(unsigned m, unsigned a, unsigned c) { return __builtin_amdgcn_alignbit(m, c - a, 31); }
DI unsigned count_gt(u32x4 a, u32x4 b, unsigned c) {
  unsigned m = 0, n = 0;
#pragma unroll
  for (int j = 0; j < 4; ++j) { m = gt_bit(m, a[j], c); n = gt_bit(n, b[j], c); }
  return __builtin_popcount(m) + __builtin_popcount(n);
}
DI unsigned max4(u32x4 v) { return max(max(v[0], v[1]), max(v[2], v[3])); }
template <int N> DI void poll_all(rsrc_t rs, int off, int stride, u32x4 (&v)[N], int back = 0) {
  for (unsigned spin = 0;; ++spin) {
#pragma unroll
    for (int j = 0; j < N; ++j) v[j] = ho_ld(rs, off + j * stride - (j == N - 1 ? back : 0));
    unsigned m = 0;
#pragma unroll
    for (int j = 0; j < N; ++j) m = max(m, max4(v[j]));
    if (__builtin_amdgcn_ballot_w64(m == SENT) == 0 || spin > SPIN_CAP) break;
    POLL_PAUSE(1);
  }
}
DI unsigned sum8_u(unsigned v) { v += dpp_u<DPP_X1>(v); v += dpp_u<DPP_X2>(v); v += dpp_u<DPP_HMIR>(v); return v; }

__global__ __launch_bounds__(768, 1) void k_fused(const uint16_t* __restrict__ x, const uint16_t* __restrict__ wqkv, const uint16_t* __restrict__ wo,
                                                   const uint16_t* __restrict__ wr, const uint16_t* __restrict__ wg, const uint16_t* __restrict__ wu,
                                                   const uint16_t* __restrict__ wd, char* __restrict__ ws, uint16_t* __restrict__ out) {
  extern __shared__ __attribute__((aligned(16))) char smem[];
  asm volatile("" :: "s"((unsigned)(uintptr_t)smem));
  const int tid = threadIdx.x, lane = tid & 63, wid = __builtin_amdgcn_readfirstlane(tid >> 6);
  const int b = blockIdx.x, xcd = b & 7, sub = b >> 3;
  const int r32 = lane & 31, hh = lane >> 5, g = lane >> 4, i16 = lane & 15;
  const rsrc_t rs = __builtin_amdgcn_make_buffer_rsrc(ws, 0, WS_BYTES, 0x00020000);
  const int rb = (xcd >> 1) * 8 + (sub & 7), hq = (xcd & 1) * 4 + (sub >> 3);
  const int head = xcd * 4 + (sub & 3), qb = sub >> 2;
  const int tt = xcd * 8 + (sub >> 2), cbk = sub & 3;
  const int d1 = (xcd < 7 && sub < 13) ? xcd * 13 + sub : 999;
  const bool isd1 = d1 < N_D1, isd2 = xcd == 7;
  STAMP_DECL; STAMP(0);
  unsigned code_pf = 0;
  if (wid == 7) {
    const unsigned long long pc = __builtin_amdgcn_s_getpc() & ~0xFFull;
    const unsigned off = min((unsigned)((sub & 3) * 8192 + lane * 128), (unsigned)(CODE_PF_BYTES - 128));
    asm volatile("global_load_dword %0, %1, %2" : "=v"(code_pf) : "v"(off), "s"(pc) : "memory");
  }

  if (__builtin_expect(wid >= 8, 1)) {
    const int hw = wid - 8;
    __builtin_amdgcn_s_setprio(3);
    {
      const int rot = ((sub & 7) + 2 * (sub >> 3)) & 7;
      const char* base; unsigned vo0, vo1, cstr, pstr;
      if (hw < 3) {
        base = (const char*)wqkv + hw * 1024 + hq * 128; cstr = 64 * 3072; pstr = 8 * 3072;
        vo0 = (lane >> 3) * 3072 + (((lane & 7) ^ ((g & 1) << 1)) * 16); vo1 = (lane >> 3) * 3072 + (((lane & 7) ^ (((g & 1) | 2) << 1)) * 16);
      } else {
        base = (const char*)x + (size_t)rb * 65536; cstr = 128; pstr = 8192;
        vo0 = (lane >> 3) * 1024 + (((lane & 7) ^ g) * 16); vo1 = (lane >> 3) * 1024 + (((lane & 7) ^ (4 + g)) * 16);
      }
      auto issue = [&](int c) {
        const char* src = base + (size_t)((c + rot) & 7) * cstr;
        const unsigned dst = (c & 3) * 32768 + hw * 8192;
#pragma unroll
        for (int p = 0; p < 8; ++p) glds16(src + (size_t)p * pstr, (p & 1) ? vo1 : vo0, dst + p * 1024);
      };
#pragma unroll 1
      for (int c = -3; c < 7; ++c) {
        if (c >= -1) {
          if (c <= 5) { WAIT_VM(8); } else { WAIT_VM(0); }
          BARRIER();
        }
        if (c + 3 < 8) issue(c + 3);
      }
    }
    unsigned tag = __builtin_amdgcn_raw_buffer_load_b32(rs, WS_EP, 0, 16);
    tag = __builtin_amdgcn_readfirstlane(tag) + 1u;
    BARRIER();
    STAMP(4);
    if (lane < 32) {
      const unsigned o = (lane & 1) ? 0u : 0x3f803f80u;
      lds_stu4((hw * 16 + (lane >> 1)) * KVT + 2048 + (lane & 1) * 16, u32x4{o, o, o, o});
    }
    wait_word(L_RDY + 36, 8u);
    {
      const int fo = WS_FA + (head * 64 + ((qb + (lane >> 1)) & 7) * 8 + hw * 2 + (lane & 1)) * 128;
      unsigned okm = 0;
      auto need = [&](unsigned bits) {
        for (unsigned spin = 0; (okm & bits) != bits && spin <= SPIN_CAP; ++spin) {
          const u32x2 f = __builtin_amdgcn_raw_buffer_load_b64(rs, fo, 0, 16);
          okm = (unsigned)__builtin_amdgcn_ballot_w64(f[0] == tag && f[1] == ~tag);
          if ((okm & bits) != bits) POLL_PAUSE(1);
        }
        asm volatile("" ::: "memory");
      };
      const char* kvg = ws + WS_KV + (size_t)head * (64 * 2048) + hw * 4096;
      const char* qsrc = ws + WS_Q + (size_t)(head * 64 + qb * 8 + hw * 2) * 1024;
      auto kv_step = [&](int i) {
        const char* src = kvg + ((qb + i) & 7) * 16384;
#pragma unroll
        for (int p = 0; p < 4; ++p) glds16s(src + p * 1024, lane * 16, (hw + i * 4 + (p >> 1) * 32) * KVT + (p & 1) * 1024);
      };
      need(0x3u);
      STAMP(5);
      glds16s(qsrc, lane * 16, L_STG + hw * 2048); glds16s(qsrc + 1024, lane * 16, L_STG + hw * 2048 + 1024);
      kv_step(0);
      if ((okm & 0xCu) == 0xCu) { kv_step(1); asm volatile("s_waitcnt vmcnt(4) lgkmcnt(0)" ::: "memory"); lds_stu(L_RDY + hw * 4, 1u); }
      else { asm volatile("s_waitcnt vmcnt(0) lgkmcnt(0)" ::: "memory"); lds_stu(L_RDY + hw * 4, 1u); need(0xCu); kv_step(1); }
      if ((okm & 0xF0u) != 0xF0u) { WAIT_VM(0); lds_stu(L_RDY + hw * 4, 2u); need(0xF0u); }
      kv_step(2); kv_step(3);
      WAIT_VM(8);
      lds_stu(L_RDY + hw * 4, 2u);
      if ((okm & 0xFF00u) != 0xFF00u) { WAIT_VM(0); lds_stu(L_RDY + hw * 4, 4u); need(0xFF00u); }
      kv_step(4); kv_step(5);
      WAIT_VM(8);
      lds_stu(L_RDY + hw * 4, 4u);
      kv_step(6); kv_step(7);
      WAIT_VM(0);
      lds_stu(L_RDY + hw * 4, 8u);
    }
    STAMP(6);
    if (hw == 0) {
      const u32x4 sv = {SENT, SENT, SENT, SENT};
#pragma unroll 1
      for (int j = 0; j < 19; ++j) __builtin_amdgcn_raw_buffer_store_b128(sv, rs, lane * 16, WS_HO + b * HO_PER_WG + j * 1024, 16);
      WAIT_VM(0);
      if (lane == 0) __builtin_amdgcn_raw_buffer_store_b64(u32x2{tag, ~tag}, rs, WS_FR + b * 8, 0, 16);
    }
    bf16x8 wf[32];
    float swr = 0.f;
    f32x16 acc;
    {
      const char* wsrc = (const char*)wo + (lane >> 2) * 1024 + cbk * 256 + hw * 64 + (lane & 3) * 16;
#pragma unroll
      for (int i = 0; i < 32; ++i) wf[i] = *(const bf16x8*)(wsrc + i * 16384);
      const unsigned stg = L_STG + hw * 2048;
      const unsigned wor = stg + (4 * (g >> 1) + (i16 >> 2)) * 64 + (g & 1) * 32 + (i16 & 3) * 8;
      wait_word(L_RDY + 16 + hw * 4, 2u);
#pragma unroll
      for (int i = 0; i < 32; ++i) {
        *(LAS bf16x8*)(uintptr_t)(stg + (i & 1) * 1024 + lane * 16) = wf[i];
        WAIT_LGKM0();
        wf[i] = cat8(tr_read(wor + (i & 1) * 1024), tr_read(wor + (i & 1) * 1024 + 512));
      }
      WAIT_LGKM0();
      const char* xs = (const char*)x + ((tt * 32 + r32) * D + cbk * 128 + hw * 32 + 4 * hh) * 2;
#pragma unroll
      for (int gq = 0; gq < 4; ++gq) {
        const u32x2 xw = *(const u32x2*)(xs + gq * 16);
        acc[4 * gq] = bf_lo(xw[0]); acc[4 * gq + 1] = bf_hi(xw[0]); acc[4 * gq + 2] = bf_lo(xw[1]); acc[4 * gq + 3] = bf_hi(xw[1]);
      }
      lds_stf(stg + r32 * 4, bf_lo(wr[cbk * 128 + hw * 32 + r32]));
      const u32x4 wra = *(const u32x4*)((const char*)wr + lane * 16);
#pragma unroll
      for (int j = 0; j < 4; ++j) swr += bf_lo(wra[j]) + bf_hi(wra[j]);
      swr = __uint_as_float(__builtin_amdgcn_readfirstlane(__float_as_uint(sum_rows(row16_sum(swr)))));
      PIN(acc);
      if (isd1) {
#pragma unroll 1
        for (int p = 0; p < 4; ++p) glds4(hw < 2 ? wg : wu, (((hw & 1) * 4 + p) * 64 + lane) * (F * 2) + d1 * 32, stg + 1024);
      } else if (isd2) {
#pragma unroll 1
        for (int p = hw; p < 22; p += 4) glds4(wd, min(p * 64 + lane, F - 1) * (D * 2) + sub * 32, stg + 1024);
      }
      WAIT_VM(0);
    }
    STAMP(7);
    if (hw == 0) {
      for (unsigned spin = 0;; ++spin) {
        const u32x4 f0 = ho_ld(rs, WS_FR + lane * 16), f1 = ho_ld(rs, WS_FR + 1024 + lane * 16);
        const bool okf = f0[0] == tag && f0[1] == ~tag && f0[2] == tag && f0[3] == ~tag && f1[0] == tag && f1[1] == ~tag && f1[2] == tag && f1[3] == ~tag;
        if (__all(okf) || spin > SPIN_CAP) break;
        POLL_PAUSE(8);
      }
    }
    STAMP(8);
    BARRIER();
    {
      RELANE();
      const unsigned ol = lane * 16;
      const int sto = HO_ST + (tt * 16 + cbk * 4 + hw) * 512 + r32 * 16;
      const unsigned ha = X_OFF + r32 * 272 + (hw * 32 + 4 * hh) * 2, wa = L_STG + hw * 2048 + 4 * hh * 4;
      f32x4 wv[4];
      auto wait_bit = [&](int bit) {
        for (unsigned spin = 0;; ++spin) {
          const unsigned m = __builtin_amdgcn_readfirstlane(*(volatile const LAS unsigned*)(uintptr_t)(L_RDY + 32));
          if (((m >> bit) & 1u) || spin > (SPIN_CAP << 4)) break;
          __builtin_amdgcn_s_sleep(1);
        }
        asm volatile("" ::: "memory");
      };
      bf16x8 of[4], nf[4];
      wait_bit(0);
#pragma unroll
      for (int i = 0; i < 4; ++i) of[i] = lds_ld128(ol + i * 1024);
#pragma unroll
      for (int w2 = 0; w2 < 8; ++w2) {
        unsigned mk = 0;
        if (w2 >= 1 && w2 < 7) {
          mk = *(volatile const LAS unsigned*)(uintptr_t)(L_RDY + 32);
          asm volatile("" ::: "memory");
#pragma unroll
          for (int i = 0; i < 4; ++i) nf[i] = lds_ld128(ol + (w2 * 4 + 4 + i) * 1024);
        }
        if (w2 == 6) {
#pragma unroll
          for (int gq = 0; gq < 4; ++gq) wv[gq] = lds_ldf4(wa + gq * 32);
        }
        SBAR();
#pragma unroll
        for (int i = 0; i < 4; ++i) acc = MFMA32(wf[w2 * 4 + i], of[i], acc);
        SBAR();
        if (w2 < 7 && !((__builtin_amdgcn_readfirstlane(mk) >> (w2 + 1)) & 1u)) {
          wait_bit(w2 + 1);
#pragma unroll
          for (int i = 0; i < 4; ++i) nf[i] = lds_ld128(ol + (w2 * 4 + 4 + i) * 1024);
        }
#pragma unroll
        for (int i = 0; i < 4; ++i) of[i] = nf[i];
      }
      STAMP(9);
      f32x2 t1 = {0.f, 0.f}, t2 = t1, t3 = t1;
#pragma unroll
      for (int j = 0; j < 8; ++j) {
        const f32x2 v = {acc[2 * j], acc[2 * j + 1]}, w = {wv[j >> 1][2 * (j & 1)], wv[j >> 1][2 * (j & 1) + 1]};
        t1 += v; t2 += v * v; t3 += v * w;
      }
      const float s1 = sum_halves(t1[0] + t1[1]), s2 = sum_halves(t2[0] + t2[1]), s3 = sum_halves(t3[0] + t3[1]);
      const u32x4 rec = {san(__float_as_uint(s1)), san(__float_as_uint(s2)), san(__float_as_uint(s3)), 0u};
      if (lane < 32) ho_st(rs, sto, rec);
      lds_stu4(S_OFF + (cbk * 4 + hw) * 528 + r32 * 16, rec);
#pragma unroll
      for (int gq = 0; gq < 4; ++gq)
        *(LAS u32x2*)(uintptr_t)(ha + gq * 16) = u32x2{pack_bf16(acc[4 * gq], acc[4 * gq + 1]), pack_bf16(acc[4 * gq + 2], acc[4 * gq + 3])};
    }
    BARRIER();
    if (hw == 0) {
      RELANE();
      f32x4 t = lds_ldf4(S_OFF + hh * 8 * 528 + r32 * 16);
#pragma unroll
      for (int j = 1; j < 8; ++j) t += lds_ldf4(S_OFF + (hh * 8 + j) * 528 + r32 * 16);
      const float s1 = sum_halves(t[0]), s2 = sum_halves(t[1]), s3 = sum_halves(t[2]);
      float mbv, rden;
      ln_stats(s1, s2, mbv, rden);
      ho_st1(rs, HO_KEY + (2 * cbk + hh) * (S * 4) + (tt * 32 + r32) * 4, sort_key((s3 - mbv * swr) * rden, tt * 32 + r32));
    }
    STAMP(11);
    {
      constexpr unsigned G = T_OFF, M1 = T_OFF + 256, C2 = T_OFF + 512, SEL = T_OFF + 640, C1 = T_OFF + 1024;
      const int ht = hw * 64 + fresh_lane(), t3 = ht & 3, t4 = ht >> 2;
      u32x4 kv[2];
      poll_all<2>(rs, HO_KEY + xcd * (S * 4) + ht * 32, 16, kv);
      STAMP(12);
      const unsigned m0 = max4(kv[0]), m1 = max4(kv[1]);
      unsigned gm = max(m0, m1); gm = max(gm, dpp_u<DPP_X1>(gm)); gm = max(gm, dpp_u<DPP_X2>(gm));
      lds_stu(G + t4 * 4, gm);
      BARRIER();
      {
        unsigned rk = count_gt(lds_ldu4(G + t3 * 64), lds_ldu4(G + t3 * 64 + 16), gm) + count_gt(lds_ldu4(G + t3 * 64 + 32), lds_ldu4(G + t3 * 64 + 48), gm);
        rk += dpp_u<DPP_X1>(rk); rk += dpp_u<DPP_X2>(rk);
        if (rk < 8) {
          lds_stu4(C1 + (rk * 8 + t3 * 2) * 16, kv[0]); lds_stu4(C1 + (rk * 8 + t3 * 2 + 1) * 16, kv[1]);
          *(LAS u32x2*)(uintptr_t)(M1 + (rk * 8 + t3 * 2) * 4) = u32x2{m0, m1};
        }
      }
      BARRIER();
      {
        const unsigned cm = lds_ldu(M1 + t4 * 4), key = lds_ldu(C1 + ht * 4);
        unsigned rk = count_gt(lds_ldu4(M1 + t3 * 64), lds_ldu4(M1 + t3 * 64 + 16), cm) + count_gt(lds_ldu4(M1 + t3 * 64 + 32), lds_ldu4(M1 + t3 * 64 + 48), cm);
        rk += dpp_u<DPP_X1>(rk); rk += dpp_u<DPP_X2>(rk);
        if (rk < 8) lds_stu(C2 + rk * 16 + t3 * 4, key);
      }
      BARRIER();
      {
        const unsigned key = lds_ldu(C2 + (ht >> 3) * 4);
        const u32x4 o = lds_ldu4(C2 + (ht & 7) * 16);
        const unsigned rk = sum8_u(((key - o[0]) >> 31) + ((key - o[1]) >> 31) + ((key - o[2]) >> 31) + ((key - o[3]) >> 31));
        if (rk < 8) lds_stu(SEL + rk * 4, key);
      }
      BARRIER();
    }
    STAMP(15); STAMP_OUT(0);
    return;
  }

  unsigned tag;
  {
    const int rt = wid & 1, hd = wid >> 1, sw = hd >> 1, par = rt ^ (hd & 1);
    tag = __builtin_amdgcn_raw_buffer_load_b32(rs, WS_EP, 0, 16);
    const int xr0 = rt * 32 + sw * 16 + i16, xr1 = xr0 ^ 16;
    const int xf = (i16 >> 1) & 7;
    const unsigned xb0 = 24576 + (xr0 >> 3) * 1024 + (xr0 & 7) * 128, xb1 = 24576 + (xr1 >> 3) * 1024 + (xr1 & 7) * 128;
    const unsigned xoA = ((4 * par + g) ^ xf) * 16, xoB = ((4 * (par ^ 1) + g) ^ xf) * 16;
    const unsigned wl = (8 * g + (i16 >> 2)) * 128 + ((hd ^ (((i16 >> 3) & 1) | ((g & 1) << 1))) * 32) + (i16 & 3) * 8;
    unsigned x0A = xb0 + xoA, x1A = xb1 + xoA, x0B = xb0 + xoB, x1B = xb1 + xoB, wAa = wl + par * 4096, wBa = wl + (par ^ 1) * 4096;
#define RDX(a, so) lds_ld128((a) + (so))
#define RDW(a, so, t) cat8(tr_read((a) + (so) + (t) * 8192), tr_read((a) + (so) + (t) * 8192 + 512))
    f32x4 aq0 = {}, aq1 = {}, ak0 = {}, ak1 = {}, av0 = {}, av1 = {};
    f32x4 rsum = {}, rsq = {}, cs0 = {}, cs1 = {}, cs2 = {};
    bf16x8 ones8 = {0x3f80, 0x3f80, 0x3f80, 0x3f80, 0x3f80, 0x3f80, 0x3f80, 0x3f80};
    PIN(ones8);
    if (tid < 16) lds_stu(L_RDY + tid * 4, 0u);
    const unsigned pv = L_PRIV + wid * PRIV, po = L_PRIV + (wid ^ 1) * PRIV;
    unsigned ad_rs = (g == (i16 >> 2)) ? L_RS + ((rt * 4 + hd) * 16 + i16) * 8 : pv + lane * 16;
    unsigned ad_cq = (i16 == 0) ? po + 1280 + g * 16 : pv + lane * 16, ad_cv = po + 1280 + 128 + i16 * 4;
    unsigned rd_rs = L_RS + ((rt * 4 + (g & 1) * 2) * 16 + i16) * 8, rd_cq = pv + 1280 + g * 16, rd_cv = pv + 1280 + 128 + i16 * 4;
    unsigned ad_mr = pv + 2048 + r32 * 4, rd_mr = pv + 2048 + (sw * 16 + 4 * g) * 4;
    const int ahead = hq * 4 + hd, tile = rb * 2 + rt;
    unsigned go_k = WS_KV + (ahead * 64 + tile) * 2048 + (lane ^ (sw * 16)) * 16, go_q = WS_Q + (ahead * 64 + tile) * 1024 + (lane ^ (sw * 16)) * 16;
    unsigned go_v = WS_KV + (ahead * 64 + tile) * 2048 + 1024 + ((g & 1) ^ sw) * 512 + (hh * 16 + i16) * 16;
    PIN(ad_rs); PIN(ad_cq); PIN(ad_cv); PIN(rd_rs); PIN(rd_cq); PIN(rd_cv); PIN(ad_mr); PIN(rd_mr); PIN(go_k); PIN(go_q); PIN(go_v);
    PIN(x0A); PIN(x1A); PIN(x0B); PIN(x1B); PIN(wAa); PIN(wBa);
    BARRIER();
    STAMP(1);
    AFrag fa, fb;
    fa.q = RDW(wAa, 0, 0); fa.x0 = RDX(x0A, 0); fa.x1 = RDX(x1A, 0); fa.k = RDW(wAa, 0, 1); fa.v = RDW(wAa, 0, 2);
    fb.q = RDW(wBa, 0, 0); fb.x0 = RDX(x0B, 0); fb.x1 = RDX(x1B, 0); fb.k = RDW(wBa, 0, 1); fb.v = RDW(wBa, 0, 2);
#pragma unroll
    for (int c = 0; c < 7; ++c) {
      asm volatile("s_barrier" ::: "memory");
      FSTAMP(16 + c);
      if (((c + 1) & 1) == 0) {
        const unsigned d = ((c + 1) & 2) ? 65536u : (unsigned)-65536;
        x0A += d; x1A += d; x0B += d; x1B += d; wAa += d; wBa += d;
        PIN(x0A); PIN(x1A); PIN(x0B); PIN(x1B); PIN(wAa); PIN(wBa);
      }
      const unsigned so = ((c + 1) & 1) * 32768;
      SBAR();
      aq0 = MFMA16(fa.q, fa.x0, aq0); aq1 = MFMA16(fa.q, fa.x1, aq1);
      cs0 = MFMA16(fa.q, ones8, cs0);
      SBAR(); fa.q = RDW(wAa, so, 0); SBAR();
      ak0 = MFMA16(fa.k, fa.x0, ak0); ak1 = MFMA16(fa.k, fa.x1, ak1);
      cs1 = MFMA16(fa.k, ones8, cs1);
      SBAR(); fa.k = RDW(wAa, so, 1); SBAR();
      av0 = MFMA16(fa.x0, fa.v, av0); av1 = MFMA16(fa.x1, fa.v, av1);
      SBAR(); fa.x1 = RDX(x1A, so); SBAR();
      cs2 = MFMA16(ones8, fa.v, cs2); rsum = MFMA16(ones8, fa.x0, rsum); rsq = MFMA16(fa.x0, fa.x0, rsq);
      SBAR(); fa.x0 = RDX(x0A, so); fa.v = RDW(wAa, so, 2); SBAR();
      aq0 = MFMA16(fb.q, fb.x0, aq0); aq1 = MFMA16(fb.q, fb.x1, aq1);
      SBAR(); fb.q = RDW(wBa, so, 0); SBAR();
      ak0 = MFMA16(fb.k, fb.x0, ak0); ak1 = MFMA16(fb.k, fb.x1, ak1);
      SBAR(); fb.k = RDW(wBa, so, 1); SBAR();
      av0 = MFMA16(fb.x0, fb.v, av0); av1 = MFMA16(fb.x1, fb.v, av1);
      SBAR(); fb.x0 = RDX(x0B, so); fb.x1 = RDX(x1B, so); fb.v = RDW(wBa, so, 2); SBAR();
    }
    rsum = MFMA16(ones8, fa.x0, rsum); rsq = MFMA16(fa.x0, fa.x0, rsq);
    cs0 = MFMA16(fa.q, ones8, cs0); cs1 = MFMA16(fa.k, ones8, cs1); cs2 = MFMA16(ones8, fa.v, cs2);
    SBAR();
    ak0 = MFMA16(fa.k, fa.x0, ak0); ak1 = MFMA16(fa.k, fa.x1, ak1);
    SBAR();
    {
      const bool b0 = lane & 1, b1 = lane & 2;
      const float dlo = b0 ? rsq[1] : rsq[0], dhi = b0 ? rsq[3] : rsq[2];
      *(LAS f32x2*)(uintptr_t)ad_rs = f32x2{rsum[0], b1 ? dhi : dlo};
      lds_stf4(ad_cq, cs0); lds_stf4(ad_cq + 64, cs1); lds_stf(ad_cv, cs2[0]);
    }
    STAMP(2);
    BARRIER();
    {
      const f32x2 pa = *(const LAS f32x2*)(uintptr_t)rd_rs, pb = *(const LAS f32x2*)(uintptr_t)(rd_rs + 128);
      const f32x4 oq = lds_ldf4(rd_cq), ok = lds_ldf4(rd_cq + 64);
      const float ov = lds_ldf(rd_cv);
      SBAR();
      ak0 = MFMA16(fb.k, fb.x0, ak0); ak1 = MFMA16(fb.k, fb.x1, ak1);
      aq0 = MFMA16(fa.q, fa.x0, aq0); aq1 = MFMA16(fa.q, fa.x1, aq1);
      SBAR();
      float mb, rden;
      ln_stats(pa[0] + pb[0], pa[1] + pb[1], mb, rden);
      aq0 = MFMA16(fb.q, fb.x0, aq0); aq1 = MFMA16(fb.q, fb.x1, aq1);
      av0 = MFMA16(fa.x0, fa.v, av0); av1 = MFMA16(fa.x1, fa.v, av1);
      av0 = MFMA16(fb.x0, fb.v, av0); av1 = MFMA16(fb.x1, fb.v, av1);
      lds_stf(ad_mr, mb); lds_stf(ad_mr + 128, rden);
      const f32x4 cq = cs0 + oq, ck = cs1 + ok;
      const float cv = cs2[0] + ov;
      const auto sm = __builtin_amdgcn_permlane16_swap(__float_as_uint(mb), __float_as_uint(mb), false, false);
      const auto sd = __builtin_amdgcn_permlane16_swap(__float_as_uint(rden), __float_as_uint(rden), false, false);
      const float m0 = __uint_as_float(sw ? sm[1] : sm[0]), m1 = __uint_as_float(sw ? sm[0] : sm[1]);
      const float d0 = __uint_as_float(sw ? sd[1] : sd[0]), d1v = __uint_as_float(sw ? sd[0] : sd[1]);
      WAIT_LGKM0();
      const f32x4 mv0 = lds_ldf4(rd_mr), mv1 = lds_ldf4(rd_mr ^ 64), dv0 = lds_ldf4(rd_mr + 128), dv1 = lds_ldf4((rd_mr ^ 64) + 128);
      unsigned a0, a1, b0, b1;
      a0 = pack_bf16((ak0[0] - m0 * ck[0]) * d0, (ak0[1] - m0 * ck[1]) * d0); a1 = pack_bf16((ak0[2] - m0 * ck[2]) * d0, (ak0[3] - m0 * ck[3]) * d0);
      b0 = pack_bf16((ak1[0] - m1 * ck[0]) * d1v, (ak1[1] - m1 * ck[1]) * d1v); b1 = pack_bf16((ak1[2] - m1 * ck[2]) * d1v, (ak1[3] - m1 * ck[3]) * d1v);
      { auto r0 = __builtin_amdgcn_permlane16_swap(a0, b0, false, false); auto r1 = __builtin_amdgcn_permlane16_swap(a1, b1, false, false);
        ho_st(rs, go_k, u32x4{r0[0], r1[0], r0[1], r1[1]}); }
      const float e0 = d0 * QSCALE, e1 = d1v * QSCALE;
      a0 = pack_bf16((aq0[0] - m0 * cq[0]) * e0, (aq0[1] - m0 * cq[1]) * e0); a1 = pack_bf16((aq0[2] - m0 * cq[2]) * e0, (aq0[3] - m0 * cq[3]) * e0);
      b0 = pack_bf16((aq1[0] - m1 * cq[0]) * e1, (aq1[1] - m1 * cq[1]) * e1); b1 = pack_bf16((aq1[2] - m1 * cq[2]) * e1, (aq1[3] - m1 * cq[3]) * e1);
      { auto r0 = __builtin_amdgcn_permlane16_swap(a0, b0, false, false); auto r1 = __builtin_amdgcn_permlane16_swap(a1, b1, false, false);
        ho_st(rs, go_q, u32x4{r0[0], r1[0], r0[1], r1[1]}); }
      a0 = pack_bf16((av0[0] - mv0[0] * cv) * dv0[0], (av0[1] - mv0[1] * cv) * dv0[1]); a1 = pack_bf16((av0[2] - mv0[2] * cv) * dv0[2], (av0[3] - mv0[3] * cv) * dv0[3]);
      b0 = pack_bf16((av1[0] - mv1[0] * cv) * dv1[0], (av1[1] - mv1[1] * cv) * dv1[1]); b1 = pack_bf16((av1[2] - mv1[2] * cv) * dv1[2], (av1[3] - mv1[3] * cv) * dv1[3]);
      { auto r0 = __builtin_amdgcn_permlane16_swap(a0, b0, false, false); auto r1 = __builtin_amdgcn_permlane16_swap(a1, b1, false, false);
        ho_st(rs, go_v, u32x4{r0[0], r1[0], r0[1], r1[1]}); }
    }
    tag = __builtin_amdgcn_readfirstlane(tag) + 1u;
    STAMP(3);
    asm volatile("s_waitcnt vmcnt(0)" : "+v"(code_pf) :: "memory");
    if (lane == 0) {
      __builtin_amdgcn_raw_buffer_store_b64(u32x2{tag, ~tag}, rs, WS_FA + (ahead * 64 + tile) * 128, 0, 16);
      __hip_atomic_fetch_add((LAS unsigned*)(uintptr_t)(L_RDY + 36), 1u, __ATOMIC_RELAXED, __HIP_MEMORY_SCOPE_WORKGROUP);
    }
    STAMP(4);
  }

  const int qg = wid & 3, kh = wid >> 2;
  u32x4 fw0 = {}, fw1 = {}, fw2 = {}, fw3 = {}, fw4 = {}, fw5 = {};
  const char* fb0 = isd1 ? (const char*)wg + wid * (64 * F * 2) + d1 * 32 : (const char*)wd + wid * (32 * D * 2) + sub * 32;
  const char* fb2 = isd1 ? (const char*)wu + wid * (64 * F * 2) + d1 * 32 : fb0 + 2 * (256 * D * 2);
  const unsigned fst = isd1 ? 32 * F * 2 : 256 * D * 2;
  const unsigned fs4 = isd1 ? 0u : 2 * fst, fs5 = (isd1 || wid >= 3) ? 0u : 3 * fst;
  unsigned fvo = (lane >> 1) * (isd1 ? F * 2 : D * 2) + (lane & 1) * 16;
  const int fit = (isd1 || isd2) ? 6 : -1;
  f32x16 oa = {}, ob = {};
  int sidx = (int)0xDDDDDDDDu;
  bf16x8 qa, qb2;
  float mref;
  {
    unsigned ka = kh * 32 * KVT + lane * 16;
    unsigned va0 = kh * 32 * KVT + (r32 < 16 ? 1024 + (hh * 16 + r32) * 16 : (r32 == 16 ? 2048 : 2064));
    unsigned va1 = va0 + (r32 < 16 ? 512 : 0);
    unsigned qad = L_STG + qg * 2048 + kh * 1024 + lane * 16;
    PIN(ka); PIN(va0); PIN(va1); PIN(qad); PIN(fvo); PIN(oa); PIN(ob); PIN(sidx);
    PIN(fw0); PIN(fw1); PIN(fw2); PIN(fw3); PIN(fw4); PIN(fw5);
    __builtin_amdgcn_s_setprio(2);
    wait_rdy(1u);
    STAMP(6);
    bf16x8 k0 = lds_ld128(ka), k1;
    qa = lds_ld128(qad); qb2 = lds_ld128(qad ^ 1024);
    bf16x8 v00 = lds_ld128(va0), v01 = lds_ld128(va1), v10, v11 = lds_ld128(va0);
    f32x16 ng, sA, sB;
    {
      const f32x16 z = {};
      sA = MFMA32(k0, qa, z); sB = MFMA32(k0, qb2, z);
      mref = max_halves(fmaxf(tile_max(sA), tile_max(sB)));
    }
#pragma unroll
    for (int j = 0; j < 16; ++j) { ng[j] = -mref; sA[j] -= mref; if (j >= 8) sB[j] = 0.f; }
    PIN(ng); PIN(sA);
    for (int it = 0; it < 8; ++it) {
      FSTAMP(32 + 2 * it);
      const unsigned need = (0x8042u >> (4 * it)) & 15u;
      if (need) {
        wait_rdy(need);
        if (it == 0 && fresh_lane() == 0) __hip_atomic_fetch_add((LAS unsigned*)(uintptr_t)(L_RDY + 16 + qg * 4), 1u, __ATOMIC_RELAXED, __HIP_MEMORY_SCOPE_WORKGROUP);
      }
      { unsigned tv_;
        asm volatile("s_cmp_lg_u32 %[it], %[fit]\n\ts_cbranch_scc1 .Lff%=\n\t"
          "global_load_dwordx4 %[a0], %[vo], %[b0]\n\tglobal_load_dwordx4 %[a2], %[vo], %[b2]\n\tv_add_u32 %[tv], %[st], %[vo]\n\t"
          "global_load_dwordx4 %[a1], %[tv], %[b0]\n\tglobal_load_dwordx4 %[a3], %[tv], %[b2]\n\tv_add_u32 %[tv], %[s4], %[vo]\n\t"
          "global_load_dwordx4 %[a4], %[tv], %[b2]\n\tv_add_u32 %[tv], %[s5], %[vo]\n\t"
          "global_load_dwordx4 %[a5], %[tv], %[b2]\n.Lff%=:\n\t"
          "s_cmp_lt_u32 %[it], 5\n\ts_cbranch_scc1 .Lpr%=\n\ts_setprio 1\n\ts_cmp_lt_u32 %[it], 7\n\ts_cbranch_scc1 .Lpr%=\n\ts_setprio 0\n.Lpr%=:"
          : [a0] "+v"(fw0), [a1] "+v"(fw1), [a2] "+v"(fw2), [a3] "+v"(fw3), [a4] "+v"(fw4), [a5] "+v"(fw5), [tv] "=&v"(tv_)
          : [it] "s"(it), [fit] "s"(fit), [vo] "v"(fvo), [b0] "s"(fb0), [b2] "s"(fb2), [st] "s"(fst), [s4] "s"(fs4), [s5] "s"(fs5) : "memory", "scc"); }
      SBAR();
      TWO_TILES(0);
      TWO_TILES(2);
      ka += 4 * KVT; va0 += 4 * KVT; va1 += 4 * KVT;
    }
    ob = SMF(v11, hi8(sB), ob);
  }
  asm volatile("s_waitcnt vmcnt(0)" : "+v"(fw0), "+v"(fw1), "+v"(fw2), "+v"(fw3), "+v"(fw4), "+v"(fw5) :: "memory");
  {
  RELANE();
  const unsigned priv = L_PRIV + wid * PRIV;
  float ma = mref, mb = mref;
  float la = sum_halves(oa[8]), lb = sum_halves(ob[8]);
  {
    float t = 0.f;
#pragma unroll
    for (int j = 0; j < 8; ++j) t += fabsf(oa[j]) + fabsf(ob[j]);
    const bool ok = (la <= 16777216.f) && (lb <= 16777216.f) && (la >= 5.9604645e-8f) && (lb >= 5.9604645e-8f) && (t <= 3.0e38f);
    if (__builtin_expect(!__all(ok), 0)) {
      const unsigned kaddr = kh * 32 * KVT + lane * 16;
      const unsigned vaddr0 = kh * 32 * KVT + (r32 < 16 ? 1024 + (hh * 16 + r32) * 16 : (r32 == 16 ? 2048 : 2064));
      const unsigned vaddr1 = vaddr0 + (r32 < 16 ? 512 : 0);
      const f32x16 z = {};
      ma = -INFINITY; mb = -INFINITY;
      for (int t2 = 0; t2 < 32; ++t2) {
        const bf16x8 kf = lds_ld128(kaddr + t2 * KVT);
        ma = fmaxf(ma, tile_max(MFMA32(kf, qa, z)));
        mb = fmaxf(mb, tile_max(MFMA32(kf, qb2, z)));
      }
      ma = max_halves(ma); mb = max_halves(mb);
      oa = z; ob = z;
      for (int t2 = 0; t2 < 32; ++t2) {
        const bf16x8 kf = lds_ld128(kaddr + t2 * KVT), v0 = lds_ld128(vaddr0 + t2 * KVT), v1 = lds_ld128(vaddr1 + t2 * KVT);
        f32x16 sa = MFMA32(kf, qa, z), sb = MFMA32(kf, qb2, z);
#pragma unroll
        for (int j = 0; j < 16; ++j) { sa[j] = EX(sa[j] - ma); sb[j] = EX(sb[j] - mb); }
        oa = SMF(v0, lo8(sa), oa); oa = SMF(v1, hi8(sa), oa);
        ob = SMF(v0, lo8(sb), ob); ob = SMF(v1, hi8(sb), ob);
      }
      la = sum_halves(oa[8]); lb = sum_halves(ob[8]);
    }
  }
  STAMP(7);
  const int row = tid >> 4, c8 = tid & 15;
  const int goff = ((tt * 32 + row) * D + cbk * 128 + c8 * 8) * 2;
  const int po_probe = HO_O + (tt * H + wid * 4 + (lane & 3)) * 1024 + 1020;
  unsigned probe;
  {
    lds_stf4(priv + lane * 16, f32x4{ob[0], ob[1], ob[2], ob[3]}); lds_stf4(priv + 1024 + lane * 16, f32x4{ob[4], ob[5], ob[6], ob[7]});
    *(LAS f32x2*)(uintptr_t)(priv + 2048 + lane * 8) = f32x2{lb, mb};
    BARRIER();
    probe = __builtin_amdgcn_raw_buffer_load_b32(rs, po_probe, 0, 16);
    SBAR();
    const unsigned yp = L_PRIV + (wid ^ 4) * PRIV;
    const f32x2 y2 = *(const LAS f32x2*)(uintptr_t)(yp + 2048 + lane * 8);
    const f32x4 y0 = lds_ldf4(yp + lane * 16), y1 = lds_ldf4(yp + 1024 + lane * 16);
    const float M = fmaxf(ma, y2[1]);
    float f1 = EX(ma - M), f2 = EX(y2[1] - M);
    const float inv = RCP(la * f1 + y2[0] * f2);
    f1 *= inv; f2 *= inv;
    u32x4 po;
    po[0] = san(pack_bf16(oa[0] * f1 + y0[0] * f2, oa[1] * f1 + y0[1] * f2));
    po[1] = san(pack_bf16(oa[2] * f1 + y0[2] * f2, oa[3] * f1 + y0[3] * f2));
    po[2] = san(pack_bf16(oa[4] * f1 + y1[0] * f2, oa[5] * f1 + y1[1] * f2));
    po[3] = san(pack_bf16(oa[6] * f1 + y1[2] * f2, oa[7] * f1 + y1[3] * f2));
    ho_st(rs, HO_O + ((qb * 8 + qg * 2 + kh) * H + head) * 1024 + lane * 16, po);
  }
  STAMP(8);

  {
    const int oo = HO_O + (tt * H + wid * 4) * 1024 + lane * 16;
    for (unsigned spin = 0;; ++spin) {
      if (__builtin_amdgcn_ballot_w64(probe == SENT) == 0 || spin > SPIN_CAP) break;
      POLL_PAUSE(1);
      probe = __builtin_amdgcn_raw_buffer_load_b32(rs, po_probe, 0, 16);
    }
    u32x4 o0 = ho_ld(rs, oo), o1 = ho_ld(rs, oo + 1024), o2 = ho_ld(rs, oo + 2048), o3 = ho_ld(rs, oo + 3072);
    for (unsigned spin = 0;; ++spin) {
      const unsigned long long p0 = __builtin_amdgcn_ballot_w64(pend(o0)), p1 = __builtin_amdgcn_ballot_w64(pend(o1));
      const unsigned long long p2 = __builtin_amdgcn_ballot_w64(pend(o2)), p3 = __builtin_amdgcn_ballot_w64(pend(o3));
      if ((p0 | p1 | p2 | p3) == 0 || spin > SPIN_CAP) break;
      POLL_PAUSE(1);
      if (p0) o0 = ho_ld(rs, oo);
      if (p1) o1 = ho_ld(rs, oo + 1024);
      if (p2) o2 = ho_ld(rs, oo + 2048);
      if (p3) o3 = ho_ld(rs, oo + 3072);
    }
    const unsigned oa2 = wid * 4096 + lane * 16;
    lds_stu4(oa2, o0); lds_stu4(oa2 + 1024, o1); lds_stu4(oa2 + 2048, o2); lds_stu4(oa2 + 3072, o3);
    WAIT_LGKM0();
    if (lane == 0) __hip_atomic_fetch_or((LAS unsigned*)(uintptr_t)(L_RDY + 32), 1u << wid, __ATOMIC_RELAXED, __HIP_MEMORY_SCOPE_WORKGROUP);
  }
  STAMP(9);
  if (isd1) {
    const unsigned fa = F_OFF + wid * 2048 + lane * 16;
    lds_stu4(fa, fw0); lds_stu4(fa + 1024, fw1); lds_stu4(fa + 16384, fw2); lds_stu4(fa + 17408, fw3);
  } else if (isd2) {
    const unsigned fa = F_OFF + wid * 1024 + lane * 16;
    lds_stu4(fa, fw0); lds_stu4(fa + 8192, fw1); lds_stu4(fa + 16384, fw2); lds_stu4(fa + 24576, fw3); lds_stu4(fa + 32768, fw4); lds_stu4(fa + 40960, fw5);
  }
  WAIT_LGKM0();
  bf16x8 ff[6];
  {
    const unsigned ft = (8 * g + (i16 >> 2)) * 32 + (i16 & 3) * 8;
    const unsigned fl = F_OFF + wid * (isd1 ? 2048 : 1024) + ft, s1 = isd1 ? 1024 : 8192;
    ff[0] = cat8(tr_read(fl), tr_read(fl + 128)); ff[1] = cat8(tr_read(fl + s1), tr_read(fl + s1 + 128));
    ff[2] = cat8(tr_read(fl + 16384), tr_read(fl + 16384 + 128)); ff[3] = cat8(tr_read(fl + 16384 + s1), tr_read(fl + 16384 + s1 + 128));
    ff[4] = cat8(tr_read(fl + 32768), tr_read(fl + 32768 + 128)); ff[5] = cat8(tr_read(fl + 40960), tr_read(fl + 40960 + 128));
  }
  if (wid < 6) {
    const int rec = ((cbk + 1 + (wid >> 1)) & 3) * 4 + (wid & 1) * 2 + hh;
    u32x4 sv[1];
    poll_all<1>(rs, HO_ST + (tt * 16 + rec) * 512 + r32 * 16, 0, sv);
    lds_stu4(S_OFF + rec * 528 + r32 * 16, sv[0]);
    STAMP(10);
  }
  BARRIER();
  STAMP(11);
  const u32x4 hv = lds_ldu4(X_OFF + row * 272 + c8 * 16);
  u32x4 ov;
  {
    const f32x2 pr = *(const LAS f32x2*)(uintptr_t)(S_OFF + c8 * 528 + row * 16);
    f32x2 st;
    { float mbv, rden; ln_stats(row16_sum(pr[0]), row16_sum(pr[1]), mbv, rden); st = f32x2{mbv, rden}; }
    u32x4 lv;
#pragma unroll
    for (int j = 0; j < 4; ++j) {
      const float h0 = bf_lo(hv[j]), h1 = bf_hi(hv[j]);
      const unsigned d = pack_bf16(h0 - st[0], h1 - st[0]);
      lv[j] = pack_bf16(bf_lo(d) * st[1], bf_hi(d) * st[1]);
      ov[j] = pack_bf16(h0 + bf_lo(lv[j]), h1 + bf_hi(lv[j]));
    }
    lv = u32x4{san(lv[0]), san(lv[1]), san(lv[2]), san(lv[3])};
    PIN(lv);
    BARRIER();
    ho_st(rs, HO_LN + goff, lv);
  }
  BARRIER(); BARRIER(); BARRIER();
  STAMP(13);
  const unsigned mykey = lds_ldu(T_OFF + 640 + (lane & 7) * 4);
  const int myrow = 2047 - (int)(mykey & 0x7ffu);
  if (isd1) {
    u32x4 ax[2];
    poll_all<2>(rs, HO_LN + (myrow * D + wid * 64 + g * 8) * 2, 64, ax);
    f32x4 ag = {}, au = {};
    ag = MFMA16(BC8(ax[0]), ff[0], ag); au = MFMA16(BC8(ax[0]), ff[2], au);
    ag = MFMA16(BC8(ax[1]), ff[1], ag); au = MFMA16(BC8(ax[1]), ff[3], au);
    lds_stf4(R_OFF + wid * 1024 + lane * 16, hh ? au : ag);
    BARRIER();
    if (wid == 0) {
      f32x4 sum = lds_ldf4(R_OFF + lane * 16);
#pragma unroll
      for (int w2 = 1; w2 < 8; ++w2) sum += lds_ldf4(R_OFF + w2 * 1024 + lane * 16);
      const auto x0 = __builtin_amdgcn_permlane32_swap(__float_as_uint(sum[0]), __float_as_uint(sum[2]), false, false);
      const auto x1 = __builtin_amdgcn_permlane32_swap(__float_as_uint(sum[1]), __float_as_uint(sum[3]), false, false);
      const float g0 = rbf(__uint_as_float(x0[0])), u0 = rbf(__uint_as_float(x0[1])), g1 = rbf(__uint_as_float(x1[0])), u1 = rbf(__uint_as_float(x1[1]));
      const float a0f = rbf(g0 * sigmoid(g0)) * u0, a1f = rbf(g1 * sigmoid(g1)) * u1;
      const float n0 = dpp_f<DPP_X1>(a0f), n1 = dpp_f<DPP_X1>(a1f);
      const bool odd = lane & 1;
      const int tok = 4 * (g & 1) + 2 * hh + (odd ? 1 : 0);
      ho_st1(rs, HO_ACT + d1 * 256 + tok * 32 + (i16 >> 1) * 4, san(odd ? pack_bf16(n1, a1f) : pack_bf16(a0f, n0)));
    }
    STAMP(14);
  }
  {
    const int grow = tt * 32 + row;
    int sel = -1;
#pragma unroll
    for (int c = 0; c < 8; ++c) if (__builtin_amdgcn_readlane(myrow, c) == grow) sel = c;
    if (sel < 0) __builtin_amdgcn_raw_buffer_store_b128(ov, __builtin_amdgcn_make_buffer_rsrc(out, 0, S * D * 2, 0x00020000), goff, 0, 16);
    else ho_st(rs, HO_HS + (sel * D + cbk * 128 + c8 * 8) * 2, u32x4{san(hv[0]), san(hv[1]), san(hv[2]), san(hv[3])});
    if (b == 0 && tid == 0) __builtin_amdgcn_raw_buffer_store_b32(tag, rs, WS_EP, 0, 16);
  }
  if (isd2) {
    const int n0 = sub * 16;
    unsigned hw = SENT, lw = SENT, fkey = 0;
    const bool odd = lane & 1;
    const int tok = 4 * (g & 1) + 2 * hh + (odd ? 1 : 0), cp = i16 >> 1;
    float sig = 0.f;
    if (wid == 0) {
      fkey = lds_ldu(T_OFF + 640 + tok * 4);
      const int frow = 2047 - (int)(fkey & 0x7ffu);
      sig = rbf(sigmoid(key_score(fkey)));
      for (unsigned spin = 0;; ++spin) {
        if (hw == SENT) hw = __builtin_amdgcn_raw_buffer_load_b32(rs, HO_HS + (tok * D + n0 + cp * 2) * 2, 0, 16);
        if (lw == SENT) lw = __builtin_amdgcn_raw_buffer_load_b32(rs, HO_LN + (frow * D + n0 + cp * 2) * 2, 0, 16);
        if (!__any(hw == SENT || lw == SENT) || spin > SPIN_CAP) break;
        POLL_PAUSE(1);
      }
    }
    u32x4 av[6];
    const int ao = HO_ACT + ((wid * 2 + (g >> 1)) * 8 + (lane & 7)) * 32 + (g & 1) * 16;
    poll_all<6>(rs, ao, 4096, av, wid < 3 ? 0 : 4096);
    STAMP(14);
    f32x4 y = {}, y2 = {};
    y = MFMA16(BC8(av[0]), ff[0], y); y2 = MFMA16(BC8(av[1]), ff[1], y2);
    y = MFMA16(BC8(av[2]), ff[2], y); y2 = MFMA16(BC8(av[3]), ff[3], y2);
    y = MFMA16(BC8(av[4]), ff[4], y);
    if (wid < 3) y2 = MFMA16(BC8(av[5]), ff[5], y2);
    lds_stf4(R_OFF + wid * 1024 + lane * 16, hh ? y2 : y);
    BARRIER();
    if (wid == 0) {
      f32x4 sum = lds_ldf4(R_OFF + lane * 16);
#pragma unroll
      for (int w2 = 1; w2 < 8; ++w2) sum += lds_ldf4(R_OFF + w2 * 1024 + lane * 16);
      const auto x0 = __builtin_amdgcn_permlane32_swap(__float_as_uint(sum[0]), __float_as_uint(sum[2]), false, false);
      const auto x1 = __builtin_amdgcn_permlane32_swap(__float_as_uint(sum[1]), __float_as_uint(sum[3]), false, false);
      const float t0 = __uint_as_float(x0[0]) + __uint_as_float(x0[1]), t1 = __uint_as_float(x1[0]) + __uint_as_float(x1[1]);
      const float n0f = dpp_f<DPP_X1>(t0), n1f = dpp_f<DPP_X1>(t1);
      const float ylo = rbf(rbf(odd ? n1f : t0) * sig), yhi = rbf(rbf(odd ? t1 : n0f) * sig);
      *(unsigned*)((char*)out + ((size_t)(2047 - (int)(fkey & 0x7ffu)) * D + n0 + cp * 2) * 2) =
          pack_bf16(bf_lo(hw) + rbf(bf_lo(lw) + ylo), bf_hi(hw) + rbf(bf_hi(lw) + yhi));
    }
  }
  }
  STAMP(15); STAMP_OUT(0);
}

extern "C" void kernel_launch(void* const* d_in, const int* in_sizes, int n_in, void* d_out, int out_size, void* d_ws, size_t ws_size,
                              hipStream_t stream) {
  (void)hipFuncSetAttribute((const void*)k_fused, hipFuncAttributeMaxDynamicSharedMemorySize, LDS_BYTES);
  k_fused<<<256, 768, LDS_BYTES, stream>>>((const uint16_t*)d_in[0], (const uint16_t*)d_in[1], (const uint16_t*)d_in[2], (const uint16_t*)d_in[4],
                                           (const uint16_t*)d_in[5], (const uint16_t*)d_in[6], (const uint16_t*)d_in[7], (char*)d_ws, (uint16_t*)d_out);
}
```

```cpp
#include <hip/hip_runtime.h>
#include <hip/hip_bf16.h>
#include <stdint.h>

typedef short bf16x8 __attribute__((ext_vector_type(8)));
typedef short bf16x4 __attribute__((ext_vector_type(4)));
typedef float f32x16 __attribute__((ext_vector_type(16)));
typedef float f32x8 __attribute__((ext_vector_type(8)));
typedef float f32x4 __attribute__((ext_vector_type(4)));
typedef __bf16 b16x8 __attribute__((ext_vector_type(8)));
typedef __bf16 b16x16 __attribute__((ext_vector_type(16)));
typedef float f32x2 __attribute__((ext_vector_type(2)));
typedef __bf16 bf16x2_t __attribute__((ext_vector_type(2)));
typedef unsigned u32x4 __attribute__((ext_vector_type(4)));
typedef unsigned u32x2 __attribute__((ext_vector_type(2)));
#define LAS __attribute__((address_space(3)))
#define GAS __attribute__((address_space(1)))
#define DI __device__ __forceinline__
#define MFMA32(a, b, c) __builtin_amdgcn_mfma_f32_32x32x16_bf16((a), (b), (c), 0, 0, 0)
#define MFMA16(a, b, c) __builtin_amdgcn_mfma_f32_16x16x32_bf16((a), (b), (c), 0, 0, 0)
#define SBAR() __builtin_amdgcn_sched_barrier(0)
#define PIN(x) asm volatile("" : "+v"(x))
#define BC8(x) __builtin_bit_cast(bf16x8, x)

constexpr int S = 2048, D = 512, H = 32, F = 1376;
constexpr float LN_EPS = 1e-5f;
constexpr float QSCALE = 0.25f * 1.4426950408889634f;
constexpr unsigned SENT = 0xFFFFFFFFu;
constexpr unsigned SPIN_CAP = 1u << 16;

constexpr int WS_Q = 0;
constexpr int WS_KV = WS_Q + H * 64 * 1024;
constexpr int WS_FA = WS_KV + H * 64 * 2048;
constexpr int WS_FQ = WS_FA + H * 64 * 128;
constexpr int WS_FR = WS_FQ + H * 64 * 128;
constexpr int WS_EP = WS_FR + 4096;
constexpr int WS_HO = WS_EP + 4096;
constexpr int HO_O = WS_HO;
constexpr int HO_LN = HO_O + 64 * H * 1024;
constexpr int HO_ST = HO_LN + S * D * 2;
constexpr int HO_KEY = HO_ST + 64 * 16 * 512;
constexpr int HO_ACT = HO_KEY + 8 * S * 4;
constexpr int HO_HS = HO_ACT + 24576;
constexpr int HO_END = HO_HS + 8192;
constexpr int HO_PER_WG = 19 * 1024;
static_assert(WS_HO + 256 * HO_PER_WG >= HO_END, "reset covers the hand-off words");
constexpr int WS_BYTES = WS_HO + 256 * HO_PER_WG;

#define STAMP_DECL do {} while (0)
#define STAMP(i) do {} while (0)
#define STAMP_OUT(k) do {} while (0)

#define FSTAMP(i) do {} while (0)

DI unsigned pack_bf16(float lo, float hi) {
  f32x2 v = {lo, hi};
  bf16x2_t r = __builtin_convertvector(v, bf16x2_t);
  return __builtin_bit_cast(unsigned, r);
}
DI float bf_lo(unsigned v) { return __uint_as_float(v << 16); }
DI float bf_hi(unsigned v) { return __uint_as_float(v & 0xffff0000u); }
DI float rbf(float x) { return bf_lo(pack_bf16(x, x)); }
DI float sum_halves(float v) {
  auto r = __builtin_amdgcn_permlane32_swap(__float_as_uint(v), __float_as_uint(v), false, false);
  return __uint_as_float(r[0]) + __uint_as_float(r[1]);
}
DI float sum_rows(float v) {
  auto r = __builtin_amdgcn_permlane16_swap(__float_as_uint(v), __float_as_uint(v), false, false);
  return sum_halves(__uint_as_float(r[0]) + __uint_as_float(r[1]));
}
DI float max_halves(float v) {
  auto r = __builtin_amdgcn_permlane32_swap(__float_as_uint(v), __float_as_uint(v), false, false);
  return fmaxf(__uint_as_float(r[0]), __uint_as_float(r[1]));
}
template <int CTRL> DI float dpp_f(float v) { return __uint_as_float(__builtin_amdgcn_update_dpp(0u, __float_as_uint(v), CTRL, 0xf, 0xf, true)); }
template <int CTRL> DI unsigned dpp_u(unsigned v) { return __builtin_amdgcn_update_dpp(0u, v, CTRL, 0xf, 0xf, true); }
constexpr int DPP_X1 = 0xB1, DPP_X2 = 0x4E, DPP_HMIR = 0x141, DPP_MIR = 0x140;
DI float quad_sum(float v) { v += dpp_f<DPP_X1>(v); v += dpp_f<DPP_X2>(v); return v; }
DI float row16_sum(float v) { v = quad_sum(v); v += dpp_f<DPP_HMIR>(v); v += dpp_f<DPP_MIR>(v); return v; }
DI void glds16(const void* sbase, unsigned voff, unsigned lds_base) {
  unsigned sv;
  asm volatile("s_mov_b32 %0, m0\n\ts_mov_b32 m0, %3\n\ts_nop 0\n\tglobal_load_lds_dwordx4 %1, %2\n\ts_mov_b32 m0, %0"
               : "=&s"(sv) : "v"(voff), "s"(sbase), "s"(lds_base) : "memory");
}
DI void glds16s(const void* sbase, unsigned voff, unsigned lds_base) {
  unsigned sv;
  asm volatile("s_mov_b32 %0, m0\n\ts_mov_b32 m0, %3\n\ts_nop 0\n\tglobal_load_lds_dwordx4 %1, %2 sc1\n\ts_mov_b32 m0, %0"
               : "=&s"(sv) : "v"(voff), "s"(sbase), "s"(lds_base) : "memory");
}
DI void glds4(const void* sbase, unsigned voff, unsigned lds_base) {
  unsigned sv;
  asm volatile("s_mov_b32 %0, m0\n\ts_mov_b32 m0, %3\n\ts_nop 0\n\tglobal_load_lds_dword %1, %2\n\ts_mov_b32 m0, %0"
               : "=&s"(sv) : "v"(voff), "s"(sbase), "s"(lds_base) : "memory");
}
#define WAIT_VM(N) asm volatile("s_waitcnt vmcnt(" #N ")" ::: "memory")
#define WAIT_LGKM0() asm volatile("s_waitcnt lgkmcnt(0)" ::: "memory")
#define BARRIER() asm volatile("s_waitcnt lgkmcnt(0)\n\ts_barrier" ::: "memory")
typedef short v4i16_t __attribute__((ext_vector_type(4)));
DI bf16x4 tr_read(unsigned addr) {
  return __builtin_bit_cast(bf16x4, __builtin_amdgcn_ds_read_tr16_b64_v4i16((LAS v4i16_t*)(uintptr_t)addr));
}
DI bf16x8 lds_ld128(unsigned addr) { return *(const LAS bf16x8*)(uintptr_t)addr; }
DI f32x4 lds_ldf4(unsigned addr) { return *(const LAS f32x4*)(uintptr_t)addr; }
DI u32x4 lds_ldu4(unsigned addr) { return *(const LAS u32x4*)(uintptr_t)addr; }
DI void lds_stf4(unsigned addr, f32x4 v) { *(LAS f32x4*)(uintptr_t)addr = v; }
DI void lds_stu4(unsigned addr, u32x4 v) { *(LAS u32x4*)(uintptr_t)addr = v; }
DI void lds_stf(unsigned addr, float v) { *(LAS float*)(uintptr_t)addr = v; }
DI void lds_stu(unsigned addr, unsigned v) { *(LAS unsigned*)(uintptr_t)addr = v; }
DI float lds_ldf(unsigned addr) { return *(const LAS float*)(uintptr_t)addr; }
DI unsigned lds_ldu(unsigned addr) { return *(const LAS unsigned*)(uintptr_t)addr; }
DI bf16x8 cat8(bf16x4 a, bf16x4 b) { return __builtin_shufflevector(a, b, 0, 1, 2, 3, 4, 5, 6, 7); }
typedef __amdgpu_buffer_rsrc_t rsrc_t;
DI u32x4 ho_ld(rsrc_t rs, int off) { return __builtin_amdgcn_raw_buffer_load_b128(rs, off, 0, 16); }
DI void ho_st(rsrc_t rs, int off, u32x4 v) { __builtin_amdgcn_raw_buffer_store_b128(v, rs, off, 0, 16); }
DI void ho_st1(rsrc_t rs, int off, unsigned v) { __builtin_amdgcn_raw_buffer_store_b32(v, rs, off, 0, 16); }
DI unsigned san(unsigned v) { return min(v, SENT - 1u); }
#define POLL_PAUSE(n) do { __builtin_amdgcn_s_sleep(n); asm volatile("" ::: "memory"); } while (0)
DI bool pend(u32x4 v) { return max(max(v[0], v[1]), max(v[2], v[3])) == SENT; }

constexpr int LDS_BYTES = 163840;
constexpr int KVT = 2080;
constexpr int L_PRIV = 64 * KVT, PRIV = 2560;
constexpr int L_STG = L_PRIV + 8 * PRIV;
constexpr int L_RDY = L_STG + 4 * 2048;
constexpr int L_RS = L_RDY + 64;
static_assert(L_RS + 1024 <= LDS_BYTES, "lds map");
constexpr unsigned X_OFF = 32768, S_OFF = 45056, T_OFF = 55296, R_OFF = 57344, F_OFF = 81920;
#define PREW 0
#define PREKV 8
constexpr int N_D1 = 86;
constexpr int CODE_PF_BYTES = 25 * 1024;
struct AFrag { bf16x8 x0, x1, q, k, v; };

#define EX(x) __builtin_amdgcn_exp2f(x)
#define RCP(x) __builtin_amdgcn_rcpf(x)
#define SMF(a, b, c) __builtin_amdgcn_smfmac_f32_32x32x32_bf16(__builtin_bit_cast(b16x8, a), __builtin_bit_cast(b16x16, b), (c), sidx, 0, 0)
DI f32x8 lo8(const f32x16& s) { return __builtin_shufflevector(s, s, 0, 1, 2, 3, 4, 5, 6, 7); }
DI f32x8 hi8(const f32x16& s) { return __builtin_shufflevector(s, s, 8, 9, 10, 11, 12, 13, 14, 15); }
#define UNIT(SC, SN, KN, QN, NEGN, OPREV, VPREV, OCUR, VCUR0, LOADS) do { \
    SC[0] = EX(SC[0]); SC[1] = EX(SC[1]); SBAR(); \
    OPREV = SMF(VPREV, hi8(SN), OPREV); \
    LOADS; \
    SBAR(); \
    SC[2] = EX(SC[2]); SC[3] = EX(SC[3]); SC[4] = EX(SC[4]); SC[5] = EX(SC[5]); SBAR(); \
    SN = MFMA32(KN, QN, NEGN); SBAR(); \
    SC[6] = EX(SC[6]); SC[7] = EX(SC[7]); SC[8] = EX(SC[8]); SC[9] = EX(SC[9]); SC[10] = EX(SC[10]); SBAR(); \
    OCUR = SMF(VCUR0, lo8(SC), OCUR); SBAR(); \
    SC[11] = EX(SC[11]); SC[12] = EX(SC[12]); SC[13] = EX(SC[13]); SC[14] = EX(SC[14]); SC[15] = EX(SC[15]); SBAR(); \
  } while (0)
#define TWO_TILES(U) do { \
    UNIT(sA, sB, k0, qb2, ng, ob, v11, oa, v00, k1 = lds_ld128(ka + (U + 1) * KVT); v10 = lds_ld128(va0 + (U + 1) * KVT)); \
    UNIT(sB, sA, k1, qa, ng, oa, v01, ob, v00, v11 = lds_ld128(va1 + (U + 1) * KVT)); \
    UNIT(sA, sB, k1, qb2, ng, ob, v01, oa, v10, k0 = lds_ld128(ka + (U + 2) * KVT); v00 = lds_ld128(va0 + (U + 2) * KVT)); \
    UNIT(sB, sA, k0, qa, ng, oa, v11, ob, v10, v01 = lds_ld128(va1 + (U + 2) * KVT)); \
  } while (0)

DI int fresh_lane() {
  int l;
  asm volatile("v_mbcnt_lo_u32_b32 %0, -1, 0\n\tv_mbcnt_hi_u32_b32 %0, -1, %0" : "=v"(l));
  return l;
}
#define RELANE() const int lane = fresh_lane(), tid = wid * 64 + lane, r32 = lane & 31, hh = lane >> 5, g = lane >> 4, i16 = lane & 15; \
  (void)tid; (void)r32; (void)hh; (void)g; (void)i16
DI float tile_max(const f32x16& s) {
  float m = fmaxf(fmaxf(s[0], s[1]), s[2]);
#pragma unroll
  for (int j = 3; j < 15; j += 2) m = fmaxf(fmaxf(m, s[j]), s[j + 1]);
  return fmaxf(m, s[15]);
}
DI unsigned sort_key(float r, int idx) {
  unsigned u = pack_bf16(r, r) & 0xffffu;
  if ((u & 0x7fffu) > 0x7f80u) u = 0xffffu;
  u = (u & 0x8000u) ? (~u & 0xffffu) : (u | 0x8000u);
  return (u << 11) | (unsigned)(2047 - idx);
}
DI float key_score(unsigned key) {
  unsigned u = (key >> 11) & 0xffffu;
  u = (u & 0x8000u) ? (u & 0x7fffu) : (~u & 0xffffu);
  return __uint_as_float(u << 16);
}
DI void ln_stats(float s1, float s2, float& mb, float& rden) {
  const float mean = s1 * (1.f / D);
  const float var = fmaxf(s2 * (1.f / D) - mean * mean, 0.f);
  mb = rbf(mean);
  rden = RCP(rbf(__builtin_amdgcn_sqrtf(rbf(rbf(var) + LN_EPS))));
}
DI float sigmoid(float v) { return RCP(1.f + EX(v * -1.4426950408889634f)); }
DI void wait_rdy(unsigned need) {
  for (unsigned spin = 0;; ++spin) {
    const u32x4 r = *(volatile const LAS u32x4*)(uintptr_t)L_RDY;
    const unsigned m = min(min(r[0], r[1]), min(r[2], r[3]));
    if (__builtin_amdgcn_readfirstlane(m) >= need || spin > (SPIN_CAP << 4)) break;
    __builtin_amdgcn_s_sleep(1);
  }
  asm volatile("" ::: "memory");
}
DI void wait_word(unsigned addr, unsigned need) {
  for (unsigned spin = 0;; ++spin) {
    const unsigned r = *(volatile const LAS unsigned*)(uintptr_t)addr;
    if (__builtin_amdgcn_readfirstlane(r) >= need || spin > (SPIN_CAP << 4)) break;
    __builtin_amdgcn_s_sleep(1);
  }
  asm volatile("" ::: "memory");
}
DI unsigned gt_bit(unsigned m, unsigned a, unsigned c) { return __builtin_amdgcn_alignbit(m, c - a, 31); }
DI unsigned count_gt(u32x4 a, u32x4 b, unsigned c) {
  unsigned m = 0, n = 0;
#pragma unroll
  for (int j = 0; j < 4; ++j) { m = gt_bit(m, a[j], c); n = gt_bit(n, b[j], c); }
  return __builtin_popcount(m) + __builtin_popcount(n);
}
DI unsigned max4(u32x4 v) { return max(max(v[0], v[1]), max(v[2], v[3])); }
template <int N> DI void poll_all(rsrc_t rs, int off, int stride, u32x4 (&v)[N], int back = 0) {
  for (unsigned spin = 0;; ++spin) {
#pragma unroll
    for (int j = 0; j < N; ++j) v[j] = ho_ld(rs, off + j * stride - (j == N - 1 ? back : 0));
    unsigned m = 0;
#pragma unroll
    for (int j = 0; j < N; ++j) m = max(m, max4(v[j]));
    if (__builtin_amdgcn_ballot_w64(m == SENT) == 0 || spin > SPIN_CAP) break;
    POLL_PAUSE(1);
  }
}
DI unsigned sum8_u(unsigned v) { v += dpp_u<DPP_X1>(v); v += dpp_u<DPP_X2>(v); v += dpp_u<DPP_HMIR>(v); return v; }

__global__ __launch_bounds__(768, 1) void k_fused(const uint16_t* __restrict__ x, const uint16_t* __restrict__ wqkv, const uint16_t* __restrict__ wo,
                                                   const uint16_t* __restrict__ wr, const uint16_t* __restrict__ wg, const uint16_t* __restrict__ wu,
                                                   const uint16_t* __restrict__ wd, char* __restrict__ ws, uint16_t* __restrict__ out) {
  extern __shared__ __attribute__((aligned(16))) char smem[];
  asm volatile("" :: "s"((unsigned)(uintptr_t)smem));
  const int tid = threadIdx.x, lane = tid & 63, wid = __builtin_amdgcn_readfirstlane(tid >> 6);
  const int b = blockIdx.x, xcd = b & 7, sub = b >> 3;
  const int r32 = lane & 31, hh = lane >> 5, g = lane >> 4, i16 = lane & 15;
  const rsrc_t rs = __builtin_amdgcn_make_buffer_rsrc(ws, 0, WS_BYTES, 0x00020000);
  const int rb = (xcd >> 1) * 8 + (sub & 7), hq = (xcd & 1) * 4 + (sub >> 3);
  const int head = xcd * 4 + (sub & 3), qb = sub >> 2;
  const int tt = xcd * 8 + (sub >> 2), cbk = sub & 3;
  const int d1 = (xcd < 7 && sub < 13) ? xcd * 13 + sub : 999;
  const bool isd1 = d1 < N_D1, isd2 = xcd == 7;
  STAMP_DECL; STAMP(0);
  unsigned code_pf = 0;
  if (wid == 7) {
    const unsigned long long pc = __builtin_amdgcn_s_getpc() & ~0xFFull;
    const unsigned off = min((unsigned)((sub & 3) * 8192 + lane * 128), (unsigned)(CODE_PF_BYTES - 128));
    asm volatile("global_load_dword %0, %1, %2" : "=v"(code_pf) : "v"(off), "s"(pc) : "memory");
  }

  if (__builtin_expect(wid >= 8, 1)) {
    const int hw = wid - 8;
    __builtin_amdgcn_s_setprio(3);
    {
      const int rot = ((sub & 7) + 2 * (sub >> 3)) & 7;
      const char* base; unsigned vo0, vo1, cstr, pstr;
      if (hw < 3) {
        base = (const char*)wqkv + hw * 1024 + hq * 128; cstr = 64 * 3072; pstr = 8 * 3072;
        vo0 = (lane >> 3) * 3072 + (((lane & 7) ^ ((g & 1) << 1)) * 16); vo1 = (lane >> 3) * 3072 + (((lane & 7) ^ (((g & 1) | 2) << 1)) * 16);
      } else {
        base = (const char*)x + (size_t)rb * 65536; cstr = 128; pstr = 8192;
        vo0 = (lane >> 3) * 1024 + (((lane & 7) ^ g) * 16); vo1 = (lane >> 3) * 1024 + (((lane & 7) ^ (4 + g)) * 16);
      }
      auto issue = [&](int c) {
        const char* src = base + (size_t)((c + rot) & 7) * cstr;
        const unsigned dst = (c & 3) * 32768 + hw * 8192;
#pragma unroll
        for (int p = 0; p < 8; ++p) glds16(src + (size_t)p * pstr, (p & 1) ? vo1 : vo0, dst + p * 1024);
      };
#pragma unroll 1
      for (int c = -3; c < 7; ++c) {
        if (c >= -1) {
          if (c <= 5) { WAIT_VM(8); } else { WAIT_VM(0); }
          BARRIER();
        }
        if (c + 3 < 8) issue(c + 3);
      }
    }
    unsigned tag = __builtin_amdgcn_raw_buffer_load_b32(rs, WS_EP, 0, 16);
    tag = __builtin_amdgcn_readfirstlane(tag) + 1u;
    BARRIER();
    STAMP(4);
    if (lane < 32) {
      const unsigned o = (lane & 1) ? 0u : 0x3f803f80u;
      lds_stu4((hw * 16 + (lane >> 1)) * KVT + 2048 + (lane & 1) * 16, u32x4{o, o, o, o});
    }
    {
      const int fo = WS_FA + (head * 64 + ((lane >> 1) & 7) * 8 + hw * 2 + (lane & 1)) * 128;
      const int fo0 = ((lane & 2) ? WS_FQ + qb * 8 * 128 : WS_FA) + (head * 64 + hw * 2 + (lane & 1)) * 128;
      unsigned okm = 0;
      auto need = [&](unsigned bits) {
        for (unsigned spin = 0; (okm & bits) != bits && spin <= SPIN_CAP; ++spin) {
          const u32x2 f = __builtin_amdgcn_raw_buffer_load_b64(rs, fo, 0, 16);
          okm = (unsigned)__builtin_amdgcn_ballot_w64(f[0] == tag && f[1] == ~tag);
          if ((okm & bits) != bits) POLL_PAUSE(1);
        }
        asm volatile("" ::: "memory");
      };
      const char* kvg = ws + WS_KV + (size_t)head * (64 * 2048) + hw * 4096;
      const char* qsrc = ws + WS_Q + (size_t)(head * 64 + qb * 8 + hw * 2) * 1024;
      auto kv_step = [&](int i) {
        const char* src = kvg + i * 16384;
#pragma unroll
        for (int p = 0; p < 4; ++p) glds16s(src + p * 1024, lane * 16, (hw + i * 4 + (p >> 1) * 32) * KVT + (p & 1) * 1024);
      };
      wait_word(L_RDY + 36, 8u);
      for (unsigned spin = 0; spin <= SPIN_CAP; ++spin) {
        const u32x2 f = __builtin_amdgcn_raw_buffer_load_b64(rs, fo0, 0, 16);
        if (__builtin_amdgcn_ballot_w64(f[0] != tag || f[1] != ~tag) == 0) break;
        POLL_PAUSE(1);
      }
      asm volatile("" ::: "memory");
      STAMP(5);
      glds16s(qsrc, lane * 16, L_STG + hw * 2048); glds16s(qsrc + 1024, lane * 16, L_STG + hw * 2048 + 1024);
      kv_step(0);
      {
        const u32x2 f = __builtin_amdgcn_raw_buffer_load_b64(rs, fo, 0, 16);
        asm volatile("s_waitcnt vmcnt(1) lgkmcnt(0)" ::: "memory");
        lds_stu(L_RDY + hw * 4, 1u);
        okm = (unsigned)__builtin_amdgcn_ballot_w64(f[0] == tag && f[1] == ~tag);
      }
      need(0xCu);
      kv_step(1);
      if ((okm & 0xF0u) != 0xF0u) { WAIT_VM(0); lds_stu(L_RDY + hw * 4, 2u); need(0xF0u); }
      kv_step(2); kv_step(3);
      WAIT_VM(8);
      lds_stu(L_RDY + hw * 4, 2u);
      if ((okm & 0xFF00u) != 0xFF00u) { WAIT_VM(0); lds_stu(L_RDY + hw * 4, 4u); need(0xFF00u); }
      kv_step(4); kv_step(5);
      WAIT_VM(8);
      lds_stu(L_RDY + hw * 4, 4u);
      kv_step(6); kv_step(7);
      WAIT_VM(0);
      lds_stu(L_RDY + hw * 4, 8u);
    }
    STAMP(6);
    if (hw == 0) {
      const u32x4 sv = {SENT, SENT, SENT, SENT};
#pragma unroll 1
      for (int j = 0; j < 19; ++j) __builtin_amdgcn_raw_buffer_store_b128(sv, rs, lane * 16, WS_HO + b * HO_PER_WG + j * 1024, 16);
      WAIT_VM(0);
      if (lane == 0) __builtin_amdgcn_raw_buffer_store_b64(u32x2{tag, ~tag}, rs, WS_FR + b * 8, 0, 16);
    }
    bf16x8 wf[32];
    float swr = 0.f;
    f32x16 acc;
    {
      const char* wsrc = (const char*)wo + (lane >> 2) * 1024 + cbk * 256 + hw * 64 + (lane & 3) * 16;
#pragma unroll
      for (int i = 0; i < 32; ++i) wf[i] = *(const bf16x8*)(wsrc + i * 16384);
      const unsigned stg = L_STG + hw * 2048;
      const unsigned wor = stg + (4 * (g >> 1) + (i16 >> 2)) * 64 + (g & 1) * 32 + (i16 & 3) * 8;
      wait_word(L_RDY + 16 + hw * 4, 2u);
#pragma unroll
      for (int i = 0; i < 32; ++i) {
        *(LAS bf16x8*)(uintptr_t)(stg + (i & 1) * 1024 + lane * 16) = wf[i];
        WAIT_LGKM0();
        wf[i] = cat8(tr_read(wor + (i & 1) * 1024), tr_read(wor + (i & 1) * 1024 + 512));
      }
      WAIT_LGKM0();
      const char* xs = (const char*)x + ((tt * 32 + r32) * D + cbk * 128 + hw * 32 + 4 * hh) * 2;
#pragma unroll
      for (int gq = 0; gq < 4; ++gq) {
        const u32x2 xw = *(const u32x2*)(xs + gq * 16);
        acc[4 * gq] = bf_lo(xw[0]); acc[4 * gq + 1] = bf_hi(xw[0]); acc[4 * gq + 2] = bf_lo(xw[1]); acc[4 * gq + 3] = bf_hi(xw[1]);
      }
      lds_stf(stg + r32 * 4, bf_lo(wr[cbk * 128 + hw * 32 + r32]));
      const u32x4 wra = *(const u32x4*)((const char*)wr + lane * 16);
#pragma unroll
      for (int j = 0; j < 4; ++j) swr += bf_lo(wra[j]) + bf_hi(wra[j]);
      swr = __uint_as_float(__builtin_amdgcn_readfirstlane(__float_as_uint(sum_rows(row16_sum(swr)))));
      PIN(acc);
      if (isd1) {
#pragma unroll 1
        for (int p = 0; p < 4; ++p) glds4(hw < 2 ? wg : wu, (((hw & 1) * 4 + p) * 64 + lane) * (F * 2) + d1 * 32, stg + 1024);
      } else if (isd2) {
#pragma unroll 1
        for (int p = hw; p < 22; p += 4) glds4(wd, min(p * 64 + lane, F - 1) * (D * 2) + sub * 32, stg + 1024);
      }
      WAIT_VM(0);
    }
    STAMP(7);
    if (hw == 0) {
      for (unsigned spin = 0;; ++spin) {
        const u32x4 f0 = ho_ld(rs, WS_FR + lane * 16), f1 = ho_ld(rs, WS_FR + 1024 + lane * 16);
        const bool okf = f0[0] == tag && f0[1] == ~tag && f0[2] == tag && f0[3] == ~tag && f1[0] == tag && f1[1] == ~tag && f1[2] == tag && f1[3] == ~tag;
        if (__all(okf) || spin > SPIN_CAP) break;
        POLL_PAUSE(8);
      }
    }
    STAMP(8);
    BARRIER();
    {
      RELANE();
      const unsigned ol = lane * 16;
      const int sto = HO_ST + (tt * 16 + cbk * 4 + hw) * 512 + r32 * 16;
      const unsigned ha = X_OFF + r32 * 272 + (hw * 32 + 4 * hh) * 2, wa = L_STG + hw * 2048 + 4 * hh * 4;
      f32x4 wv[4];
      auto wait_bit = [&](int bit) {
        for (unsigned spin = 0;; ++spin) {
          const unsigned m = __builtin_amdgcn_readfirstlane(*(volatile const LAS unsigned*)(uintptr_t)(L_RDY + 32));
          if (((m >> bit) & 1u) || spin > (SPIN_CAP << 4)) break;
          __builtin_amdgcn_s_sleep(1);
        }
        asm volatile("" ::: "memory");
      };
      bf16x8 of[4], nf[4];
      wait_bit(0);
#pragma unroll
      for (int i = 0; i < 4; ++i) of[i] = lds_ld128(ol + i * 1024);
#pragma unroll
      for (int w2 = 0; w2 < 8; ++w2) {
        unsigned mk = 0;
        if (w2 >= 1 && w2 < 7) {
          mk = *(volatile const LAS unsigned*)(uintptr_t)(L_RDY + 32);
          asm volatile("" ::: "memory");
#pragma unroll
          for (int i = 0; i < 4; ++i) nf[i] = lds_ld128(ol + (w2 * 4 + 4 + i) * 1024);
        }
        if (w2 == 6) {
#pragma unroll
          for (int gq = 0; gq < 4; ++gq) wv[gq] = lds_ldf4(wa + gq * 32);
        }
        SBAR();
#pragma unroll
        for (int i = 0; i < 4; ++i) acc = MFMA32(wf[w2 * 4 + i], of[i], acc);
        SBAR();
        if (w2 < 7 && !((__builtin_amdgcn_readfirstlane(mk) >> (w2 + 1)) & 1u)) {
          wait_bit(w2 + 1);
#pragma unroll
          for (int i = 0; i < 4; ++i) nf[i] = lds_ld128(ol + (w2 * 4 + 4 + i) * 1024);
        }
#pragma unroll
        for (int i = 0; i < 4; ++i) of[i] = nf[i];
      }
      STAMP(9);
      f32x2 t1 = {0.f, 0.f}, t2 = t1, t3 = t1;
#pragma unroll
      for (int j = 0; j < 8; ++j) {
        const f32x2 v = {acc[2 * j], acc[2 * j + 1]}, w = {wv[j >> 1][2 * (j & 1)], wv[j >> 1][2 * (j & 1) + 1]};
        t1 += v; t2 += v * v; t3 += v * w;
      }
      const float s1 = sum_halves(t1[0] + t1[1]), s2 = sum_halves(t2[0] + t2[1]), s3 = sum_halves(t3[0] + t3[1]);
      const u32x4 rec = {san(__float_as_uint(s1)), san(__float_as_uint(s2)), san(__float_as_uint(s3)), 0u};
      if (lane < 32) ho_st(rs, sto, rec);
      lds_stu4(S_OFF + (cbk * 4 + hw) * 528 + r32 * 16, rec);
#pragma unroll
      for (int gq = 0; gq < 4; ++gq)
        *(LAS u32x2*)(uintptr_t)(ha + gq * 16) = u32x2{pack_bf16(acc[4 * gq], acc[4 * gq + 1]), pack_bf16(acc[4 * gq + 2], acc[4 * gq + 3])};
    }
    BARRIER();
    if (hw == 0) {
      RELANE();
      f32x4 t = lds_ldf4(S_OFF + hh * 8 * 528 + r32 * 16);
#pragma unroll
      for (int j = 1; j < 8; ++j) t += lds_ldf4(S_OFF + (hh * 8 + j) * 528 + r32 * 16);
      const float s1 = sum_halves(t[0]), s2 = sum_halves(t[1]), s3 = sum_halves(t[2]);
      float mbv, rden;
      ln_stats(s1, s2, mbv, rden);
      ho_st1(rs, HO_KEY + (2 * cbk + hh) * (S * 4) + (tt * 32 + r32) * 4, sort_key((s3 - mbv * swr) * rden, tt * 32 + r32));
    }
    STAMP(11);
    {
      constexpr unsigned G = T_OFF, M1 = T_OFF + 256, C2 = T_OFF + 512, SEL = T_OFF + 640, C1 = T_OFF + 1024;
      const int ht = hw * 64 + fresh_lane(), t3 = ht & 3, t4 = ht >> 2;
      u32x4 kv[2];
      poll_all<2>(rs, HO_KEY + xcd * (S * 4) + ht * 32, 16, kv);
      STAMP(12);
      const unsigned m0 = max4(kv[0]), m1 = max4(kv[1]);
      unsigned gm = max(m0, m1); gm = max(gm, dpp_u<DPP_X1>(gm)); gm = max(gm, dpp_u<DPP_X2>(gm));
      lds_stu(G + t4 * 4, gm);
      BARRIER();
      {
        unsigned rk = count_gt(lds_ldu4(G + t3 * 64), lds_ldu4(G + t3 * 64 + 16), gm) + count_gt(lds_ldu4(G + t3 * 64 + 32), lds_ldu4(G + t3 * 64 + 48), gm);
        rk += dpp_u<DPP_X1>(rk); rk += dpp_u<DPP_X2>(rk);
        if (rk < 8) {
          lds_stu4(C1 + (rk * 8 + t3 * 2) * 16, kv[0]); lds_stu4(C1 + (rk * 8 + t3 * 2 + 1) * 16, kv[1]);
          *(LAS u32x2*)(uintptr_t)(M1 + (rk * 8 + t3 * 2) * 4) = u32x2{m0, m1};
        }
      }
      BARRIER();
      {
        const unsigned cm = lds_ldu(M1 + t4 * 4), key = lds_ldu(C1 + ht * 4);
        unsigned rk = count_gt(lds_ldu4(M1 + t3 * 64), lds_ldu4(M1 + t3 * 64 + 16), cm) + count_gt(lds_ldu4(M1 + t3 * 64 + 32), lds_ldu4(M1 + t3 * 64 + 48), cm);
        rk += dpp_u<DPP_X1>(rk); rk += dpp_u<DPP_X2>(rk);
        if (rk < 8) lds_stu(C2 + rk * 16 + t3 * 4, key);
      }
      BARRIER();
      {
        const unsigned key = lds_ldu(C2 + (ht >> 3) * 4);
        const u32x4 o = lds_ldu4(C2 + (ht & 7) * 16);
        const unsigned rk = sum8_u(((key - o[0]) >> 31) + ((key - o[1]) >> 31) + ((key - o[2]) >> 31) + ((key - o[3]) >> 31));
        if (rk < 8) lds_stu(SEL + rk * 4, key);
      }
      BARRIER();
    }
    STAMP(15); STAMP_OUT(0);
    return;
  }

  unsigned tag;
  {
    const int rt = wid & 1, hd = wid >> 1, sw = hd >> 1, par = rt ^ (hd & 1);
    tag = __builtin_amdgcn_raw_buffer_load_b32(rs, WS_EP, 0, 16);
    const int xr0 = rt * 32 + sw * 16 + i16, xr1 = xr0 ^ 16;
    const int xf = (i16 >> 1) & 7;
    const unsigned xb0 = 24576 + (xr0 >> 3) * 1024 + (xr0 & 7) * 128, xb1 = 24576 + (xr1 >> 3) * 1024 + (xr1 & 7) * 128;
    const unsigned xoA = ((4 * par + g) ^ xf) * 16, xoB = ((4 * (par ^ 1) + g) ^ xf) * 16;
    const unsigned wl = (8 * g + (i16 >> 2)) * 128 + ((hd ^ (((i16 >> 3) & 1) | ((g & 1) << 1))) * 32) + (i16 & 3) * 8;
    unsigned x0A = xb0 + xoA, x1A = xb1 + xoA, x0B = xb0 + xoB, x1B = xb1 + xoB, wAa = wl + par * 4096, wBa = wl + (par ^ 1) * 4096;
#define RDX(a, so) lds_ld128((a) + (so))
#define RDW(a, so, t) cat8(tr_read((a) + (so) + (t) * 8192), tr_read((a) + (so) + (t) * 8192 + 512))
    f32x4 aq0 = {}, aq1 = {}, ak0 = {}, ak1 = {}, av0 = {}, av1 = {};
    f32x4 rsum = {}, rsq = {}, cs0 = {}, cs1 = {}, cs2 = {};
    bf16x8 ones8 = {0x3f80, 0x3f80, 0x3f80, 0x3f80, 0x3f80, 0x3f80, 0x3f80, 0x3f80};
    PIN(ones8);
    if (tid < 16) lds_stu(L_RDY + tid * 4, 0u);
    const unsigned pv = L_PRIV + wid * PRIV, po = L_PRIV + (wid ^ 1) * PRIV;
    unsigned ad_rs = (g == (i16 >> 2)) ? L_RS + ((rt * 4 + hd) * 16 + i16) * 8 : pv + lane * 16;
    unsigned ad_cq = (i16 == 0) ? po + 1280 + g * 16 : pv + lane * 16, ad_cv = po + 1280 + 128 + i16 * 4;
    unsigned rd_rs = L_RS + ((rt * 4 + (g & 1) * 2) * 16 + i16) * 8, rd_cq = pv + 1280 + g * 16, rd_cv = pv + 1280 + 128 + i16 * 4;
    unsigned ad_mr = pv + 2048 + r32 * 4, rd_mr = pv + 2048 + (sw * 16 + 4 * g) * 4;
    const int ahead = hq * 4 + hd, tile = rb * 2 + rt;
    unsigned go_k = WS_KV + (ahead * 64 + tile) * 2048 + (lane ^ (sw * 16)) * 16, go_q = WS_Q + (ahead * 64 + tile) * 1024 + (lane ^ (sw * 16)) * 16;
    unsigned go_v = WS_KV + (ahead * 64 + tile) * 2048 + 1024 + ((g & 1) ^ sw) * 512 + (hh * 16 + i16) * 16;
    PIN(ad_rs); PIN(ad_cq); PIN(ad_cv); PIN(rd_rs); PIN(rd_cq); PIN(rd_cv); PIN(ad_mr); PIN(rd_mr); PIN(go_k); PIN(go_q); PIN(go_v);
    PIN(x0A); PIN(x1A); PIN(x0B); PIN(x1B); PIN(wAa); PIN(wBa);
#define PREWRITE() do { const u32x4 z4 = {0u, 0u, 0u, 0u}; ho_st(rs, go_q, z4); if (rb < PREKV) { ho_st(rs, go_k, z4); ho_st(rs, go_v, z4); } \
      if (lane < 16) ho_st(rs, ((lane & 8) ? WS_FQ : WS_FA) + (ahead * 64 + tile) * 128 + (lane & 7) * 16, z4); } while (0)
    BARRIER();
    STAMP(1);
    AFrag fa, fb;
    fa.q = RDW(wAa, 0, 0); fa.x0 = RDX(x0A, 0); fa.x1 = RDX(x1A, 0); fa.k = RDW(wAa, 0, 1); fa.v = RDW(wAa, 0, 2);
    fb.q = RDW(wBa, 0, 0); fb.x0 = RDX(x0B, 0); fb.x1 = RDX(x1B, 0); fb.k = RDW(wBa, 0, 1); fb.v = RDW(wBa, 0, 2);
#pragma unroll
    for (int c = 0; c < 7; ++c) {
      asm volatile("s_barrier" ::: "memory");
      FSTAMP(16 + c);
      if (((c + 1) & 1) == 0) {
        const unsigned d = ((c + 1) & 2) ? 65536u : (unsigned)-65536;
        x0A += d; x1A += d; x0B += d; x1B += d; wAa += d; wBa += d;
        PIN(x0A); PIN(x1A); PIN(x0B); PIN(x1B); PIN(wAa); PIN(wBa);
      }
      const unsigned so = ((c + 1) & 1) * 32768;
      SBAR();
      aq0 = MFMA16(fa.q, fa.x0, aq0); aq1 = MFMA16(fa.q, fa.x1, aq1);
      cs0 = MFMA16(fa.q, ones8, cs0);
      SBAR(); fa.q = RDW(wAa, so, 0); SBAR();
      ak0 = MFMA16(fa.k, fa.x0, ak0); ak1 = MFMA16(fa.k, fa.x1, ak1);
      cs1 = MFMA16(fa.k, ones8, cs1);
      SBAR(); fa.k = RDW(wAa, so, 1); SBAR();
      av0 = MFMA16(fa.x0, fa.v, av0); av1 = MFMA16(fa.x1, fa.v, av1);
      SBAR(); fa.x1 = RDX(x1A, so); SBAR();
      cs2 = MFMA16(ones8, fa.v, cs2); rsum = MFMA16(ones8, fa.x0, rsum); rsq = MFMA16(fa.x0, fa.x0, rsq);
      SBAR(); fa.x0 = RDX(x0A, so); fa.v = RDW(wAa, so, 2); SBAR();
      aq0 = MFMA16(fb.q, fb.x0, aq0); aq1 = MFMA16(fb.q, fb.x1, aq1);
      SBAR(); fb.q = RDW(wBa, so, 0); SBAR();
      ak0 = MFMA16(fb.k, fb.x0, ak0); ak1 = MFMA16(fb.k, fb.x1, ak1);
      SBAR(); fb.k = RDW(wBa, so, 1); SBAR();
      av0 = MFMA16(fb.x0, fb.v, av0); av1 = MFMA16(fb.x1, fb.v, av1);
      SBAR(); fb.x0 = RDX(x0B, so); fb.x1 = RDX(x1B, so); fb.v = RDW(wBa, so, 2); SBAR();
    }
    rsum = MFMA16(ones8, fa.x0, rsum); rsq = MFMA16(fa.x0, fa.x0, rsq);
    cs0 = MFMA16(fa.q, ones8, cs0); cs1 = MFMA16(fa.k, ones8, cs1); cs2 = MFMA16(ones8, fa.v, cs2);
    SBAR();
    aq0 = MFMA16(fa.q, fa.x0, aq0); aq1 = MFMA16(fa.q, fa.x1, aq1);
    SBAR();
    {
      const bool b0 = lane & 1, b1 = lane & 2;
      const float dlo = b0 ? rsq[1] : rsq[0], dhi = b0 ? rsq[3] : rsq[2];
      *(LAS f32x2*)(uintptr_t)ad_rs = f32x2{rsum[0], b1 ? dhi : dlo};
      lds_stf4(ad_cq, cs0); lds_stf4(ad_cq + 64, cs1); lds_stf(ad_cv, cs2[0]);
    }
    STAMP(2);
    BARRIER();
    {
      const f32x2 pa = *(const LAS f32x2*)(uintptr_t)rd_rs, pb = *(const LAS f32x2*)(uintptr_t)(rd_rs + 128);
      const f32x4 oq = lds_ldf4(rd_cq), ok = lds_ldf4(rd_cq + 64);
      const float ov = lds_ldf(rd_cv);
      SBAR();
      aq0 = MFMA16(fb.q, fb.x0, aq0); aq1 = MFMA16(fb.q, fb.x1, aq1);
      ak0 = MFMA16(fa.k, fa.x0, ak0); ak1 = MFMA16(fa.k, fa.x1, ak1);
      SBAR();
      float mb, rden;
      ln_stats(pa[0] + pb[0], pa[1] + pb[1], mb, rden);
      lds_stf(ad_mr, mb); lds_stf(ad_mr + 128, rden);
      const f32x4 cq = cs0 + oq, ck = cs1 + ok;
      const float cv = cs2[0] + ov;
      const auto sm = __builtin_amdgcn_permlane16_swap(__float_as_uint(mb), __float_as_uint(mb), false, false);
      const auto sd = __builtin_amdgcn_permlane16_swap(__float_as_uint(rden), __float_as_uint(rden), false, false);
      const float m0 = __uint_as_float(sw ? sm[1] : sm[0]), m1 = __uint_as_float(sw ? sm[0] : sm[1]);
      const float d0 = __uint_as_float(sw ? sd[1] : sd[0]), d1v = __uint_as_float(sw ? sd[0] : sd[1]);
      unsigned a0, a1, b0, b1;
      const float e0 = d0 * QSCALE, e1 = d1v * QSCALE;
      a0 = pack_bf16((aq0[0] - m0 * cq[0]) * e0, (aq0[1] - m0 * cq[1]) * e0); a1 = pack_bf16((aq0[2] - m0 * cq[2]) * e0, (aq0[3] - m0 * cq[3]) * e0);
      b0 = pack_bf16((aq1[0] - m1 * cq[0]) * e1, (aq1[1] - m1 * cq[1]) * e1); b1 = pack_bf16((aq1[2] - m1 * cq[2]) * e1, (aq1[3] - m1 * cq[3]) * e1);
      { auto r0 = __builtin_amdgcn_permlane16_swap(a0, b0, false, false); auto r1 = __builtin_amdgcn_permlane16_swap(a1, b1, false, false);
        ho_st(rs, go_q, u32x4{r0[0], r1[0], r0[1], r1[1]}); }
      SBAR();
      ak0 = MFMA16(fb.k, fb.x0, ak0); ak1 = MFMA16(fb.k, fb.x1, ak1);
      av0 = MFMA16(fa.x0, fa.v, av0); av1 = MFMA16(fa.x1, fa.v, av1);
      av0 = MFMA16(fb.x0, fb.v, av0); av1 = MFMA16(fb.x1, fb.v, av1);
      WAIT_LGKM0();
      const f32x4 mv0 = lds_ldf4(rd_mr), mv1 = lds_ldf4(rd_mr ^ 64), dv0 = lds_ldf4(rd_mr + 128), dv1 = lds_ldf4((rd_mr ^ 64) + 128);
      u32x4 pk, pvv;
      a0 = pack_bf16((ak0[0] - m0 * ck[0]) * d0, (ak0[1] - m0 * ck[1]) * d0); a1 = pack_bf16((ak0[2] - m0 * ck[2]) * d0, (ak0[3] - m0 * ck[3]) * d0);
      b0 = pack_bf16((ak1[0] - m1 * ck[0]) * d1v, (ak1[1] - m1 * ck[1]) * d1v); b1 = pack_bf16((ak1[2] - m1 * ck[2]) * d1v, (ak1[3] - m1 * ck[3]) * d1v);
      { auto r0 = __builtin_amdgcn_permlane16_swap(a0, b0, false, false); auto r1 = __builtin_amdgcn_permlane16_swap(a1, b1, false, false);
        pk = u32x4{r0[0], r1[0], r0[1], r1[1]}; }
      a0 = pack_bf16((av0[0] - mv0[0] * cv) * dv0[0], (av0[1] - mv0[1] * cv) * dv0[1]); a1 = pack_bf16((av0[2] - mv0[2] * cv) * dv0[2], (av0[3] - mv0[3] * cv) * dv0[3]);
      b0 = pack_bf16((av1[0] - mv1[0] * cv) * dv1[0], (av1[1] - mv1[1] * cv) * dv1[1]); b1 = pack_bf16((av1[2] - mv1[2] * cv) * dv1[2], (av1[3] - mv1[3] * cv) * dv1[3]);
      { auto r0 = __builtin_amdgcn_permlane16_swap(a0, b0, false, false); auto r1 = __builtin_amdgcn_permlane16_swap(a1, b1, false, false);
        pvv = u32x4{r0[0], r1[0], r0[1], r1[1]}; }
      tag = __builtin_amdgcn_readfirstlane(tag) + 1u;
      const u32x4 fl = {tag, ~tag, tag, ~tag};
      PIN(pk); PIN(pvv);
      STAMP(3);
      if (rb < 4) { ho_st(rs, go_k, pk); ho_st(rs, go_v, pvv); asm volatile("s_waitcnt vmcnt(2)" : "+v"(code_pf) :: "memory"); }
      else asm volatile("s_waitcnt vmcnt(0)" : "+v"(code_pf) :: "memory");
      if (lane < 8) ho_st(rs, WS_FQ + (ahead * 64 + tile) * 128 + lane * 16, fl);
      if (lane == 0) __hip_atomic_fetch_add((LAS unsigned*)(uintptr_t)(L_RDY + 36), 1u, __ATOMIC_RELAXED, __HIP_MEMORY_SCOPE_WORKGROUP);
      if (rb >= 4) {
        ho_st(rs, go_k, pk); ho_st(rs, go_v, pvv); WAIT_VM(0);
      } else WAIT_VM(1);
      if (lane < 8) ho_st(rs, WS_FA + (ahead * 64 + tile) * 128 + lane * 16, fl);
    }
    STAMP(4);
  }

  const int qg = wid & 3, kh = wid >> 2;
  u32x4 fw0 = {}, fw1 = {}, fw2 = {}, fw3 = {}, fw4 = {}, fw5 = {};
  const char* fb0 = isd1 ? (const char*)wg + wid * (64 * F * 2) + d1 * 32 : (const char*)wd + wid * (32 * D * 2) + sub * 32;
  const char* fb2 = isd1 ? (const char*)wu + wid * (64 * F * 2) + d1 * 32 : fb0 + 2 * (256 * D * 2);
  const unsigned fst = isd1 ? 32 * F * 2 : 256 * D * 2;
  const unsigned fs4 = isd1 ? 0u : 2 * fst, fs5 = (isd1 || wid >= 3) ? 0u : 3 * fst;
  unsigned fvo = (lane >> 1) * (isd1 ? F * 2 : D * 2) + (lane & 1) * 16;
  const int fit = (isd1 || isd2) ? 6 : -1;
  f32x16 oa = {}, ob = {};
  int sidx = (int)0xDDDDDDDDu;
  bf16x8 qa, qb2;
  float mref;
  {
    unsigned ka = kh * 32 * KVT + lane * 16;
    unsigned va0 = kh * 32 * KVT + (r32 < 16 ? 1024 + (hh * 16 + r32) * 16 : (r32 == 16 ? 2048 : 2064));
    unsigned va1 = va0 + (r32 < 16 ? 512 : 0);
    unsigned qad = L_STG + qg * 2048 + kh * 1024 + lane * 16;
    PIN(ka); PIN(va0); PIN(va1); PIN(qad); PIN(fvo); PIN(oa); PIN(ob); PIN(sidx);
    PIN(fw0); PIN(fw1); PIN(fw2); PIN(fw3); PIN(fw4); PIN(fw5);
    __builtin_amdgcn_s_setprio(2);
    wait_rdy(1u);
    STAMP(6);
    bf16x8 k0 = lds_ld128(ka), k1;
    qa = lds_ld128(qad); qb2 = lds_ld128(qad ^ 1024);
    bf16x8 v00 = lds_ld128(va0), v01 = lds_ld128(va1), v10, v11 = lds_ld128(va0);
    f32x16 ng, sA, sB;
    {
      const f32x16 z = {};
      sA = MFMA32(k0, qa, z); sB = MFMA32(k0, qb2, z);
      mref = max_halves(fmaxf(tile_max(sA), tile_max(sB)));
    }
#pragma unroll
    for (int j = 0; j < 16; ++j) { ng[j] = -mref; sA[j] -= mref; if (j >= 8) sB[j] = 0.f; }
    PIN(ng); PIN(sA);
    for (int it = 0; it < 8; ++it) {
      FSTAMP(32 + 2 * it);
      { unsigned tv_;
        asm volatile("s_cmp_lg_u32 %[it], %[fit]\n\ts_cbranch_scc1 .Lff%=\n\t"
          "global_load_dwordx4 %[a0], %[vo], %[b0]\n\tglobal_load_dwordx4 %[a2], %[vo], %[b2]\n\tv_add_u32 %[tv], %[st], %[vo]\n\t"
          "global_load_dwordx4 %[a1], %[tv], %[b0]\n\tglobal_load_dwordx4 %[a3], %[tv], %[b2]\n\tv_add_u32 %[tv], %[s4], %[vo]\n\t"
          "global_load_dwordx4 %[a4], %[tv], %[b2]\n\tv_add_u32 %[tv], %[s5], %[vo]\n\t"
          "global_load_dwordx4 %[a5], %[tv], %[b2]\n.Lff%=:\n\t"
          "s_cmp_lt_u32 %[it], 5\n\ts_cbranch_scc1 .Lpr%=\n\ts_setprio 1\n\ts_cmp_lt_u32 %[it], 7\n\ts_cbranch_scc1 .Lpr%=\n\ts_setprio 0\n.Lpr%=:"
          : [a0] "+v"(fw0), [a1] "+v"(fw1), [a2] "+v"(fw2), [a3] "+v"(fw3), [a4] "+v"(fw4), [a5] "+v"(fw5), [tv] "=&v"(tv_)
          : [it] "s"(it), [fit] "s"(fit), [vo] "v"(fvo), [b0] "s"(fb0), [b2] "s"(fb2), [st] "s"(fst), [s4] "s"(fs4), [s5] "s"(fs5) : "memory", "scc"); }
      SBAR();
      TWO_TILES(0);
      PIN(sA); PIN(sB);
      const unsigned need = (0x8042u >> (4 * it)) & 15u;
      if (need) {
        wait_rdy(need);
        if (it == 0 && fresh_lane() == 0) __hip_atomic_fetch_add((LAS unsigned*)(uintptr_t)(L_RDY + 16 + qg * 4), 1u, __ATOMIC_RELAXED, __HIP_MEMORY_SCOPE_WORKGROUP);
      }
      SBAR();
      TWO_TILES(2);
      ka += 4 * KVT; va0 += 4 * KVT; va1 += 4 * KVT;
    }
    ob = SMF(v11, hi8(sB), ob);
  }
  asm volatile("s_waitcnt vmcnt(0)" : "+v"(fw0), "+v"(fw1), "+v"(fw2), "+v"(fw3), "+v"(fw4), "+v"(fw5) :: "memory");
  {
  RELANE();
  const unsigned priv = L_PRIV + wid * PRIV;
  float ma = mref, mb = mref;
  float la = sum_halves(oa[8]), lb = sum_halves(ob[8]);
  {
    float t = 0.f;
#pragma unroll
    for (int j = 0; j < 8; ++j) t += fabsf(oa[j]) + fabsf(ob[j]);
    const bool ok = (la <= 16777216.f) && (lb <= 16777216.f) && (la >= 5.9604645e-8f) && (lb >= 5.9604645e-8f) && (t <= 3.0e38f);
    if (__builtin_expect(!__all(ok), 0)) {
      const unsigned kaddr = kh * 32 * KVT + lane * 16;
      const unsigned vaddr0 = kh * 32 * KVT + (r32 < 16 ? 1024 + (hh * 16 + r32) * 16 : (r32 == 16 ? 2048 : 2064));
      const unsigned vaddr1 = vaddr0 + (r32 < 16 ? 512 : 0);
      const f32x16 z = {};
      ma = -INFINITY; mb = -INFINITY;
      for (int t2 = 0; t2 < 32; ++t2) {
        const bf16x8 kf = lds_ld128(kaddr + t2 * KVT);
        ma = fmaxf(ma, tile_max(MFMA32(kf, qa, z)));
        mb = fmaxf(mb, tile_max(MFMA32(kf, qb2, z)));
      }
      ma = max_halves(ma); mb = max_halves(mb);
      oa = z; ob = z;
      for (int t2 = 0; t2 < 32; ++t2) {
        const bf16x8 kf = lds_ld128(kaddr + t2 * KVT), v0 = lds_ld128(vaddr0 + t2 * KVT), v1 = lds_ld128(vaddr1 + t2 * KVT);
        f32x16 sa = MFMA32(kf, qa, z), sb = MFMA32(kf, qb2, z);
#pragma unroll
        for (int j = 0; j < 16; ++j) { sa[j] = EX(sa[j] - ma); sb[j] = EX(sb[j] - mb); }
        oa = SMF(v0, lo8(sa), oa); oa = SMF(v1, hi8(sa), oa);
        ob = SMF(v0, lo8(sb), ob); ob = SMF(v1, hi8(sb), ob);
      }
      la = sum_halves(oa[8]); lb = sum_halves(ob[8]);
    }
  }
  STAMP(7);
  const int row = tid >> 4, c8 = tid & 15;
  const int goff = ((tt * 32 + row) * D + cbk * 128 + c8 * 8) * 2;
  const int po_probe = HO_O + (tt * H + wid * 4 + (lane & 3)) * 1024 + 1020;
  unsigned probe;
  {
    lds_stf4(priv + lane * 16, f32x4{ob[0], ob[1], ob[2], ob[3]}); lds_stf4(priv + 1024 + lane * 16, f32x4{ob[4], ob[5], ob[6], ob[7]});
    *(LAS f32x2*)(uintptr_t)(priv + 2048 + lane * 8) = f32x2{lb, mb};
    BARRIER();
    probe = __builtin_amdgcn_raw_buffer_load_b32(rs, po_probe, 0, 16);
    SBAR();
    const unsigned yp = L_PRIV + (wid ^ 4) * PRIV;
    const f32x2 y2 = *(const LAS f32x2*)(uintptr_t)(yp + 2048 + lane * 8);
    const f32x4 y0 = lds_ldf4(yp + lane * 16), y1 = lds_ldf4(yp + 1024 + lane * 16);
    const float M = fmaxf(ma, y2[1]);
    float f1 = EX(ma - M), f2 = EX(y2[1] - M);
    const float inv = RCP(la * f1 + y2[0] * f2);
    f1 *= inv; f2 *= inv;
    u32x4 po;
    po[0] = san(pack_bf16(oa[0] * f1 + y0[0] * f2, oa[1] * f1 + y0[1] * f2));
    po[1] = san(pack_bf16(oa[2] * f1 + y0[2] * f2, oa[3] * f1 + y0[3] * f2));
    po[2] = san(pack_bf16(oa[4] * f1 + y1[0] * f2, oa[5] * f1 + y1[1] * f2));
    po[3] = san(pack_bf16(oa[6] * f1 + y1[2] * f2, oa[7] * f1 + y1[3] * f2));
    ho_st(rs, HO_O + ((qb * 8 + qg * 2 + kh) * H + head) * 1024 + lane * 16, po);
  }
  STAMP(8);

  {
    const int oo = HO_O + (tt * H + wid * 4) * 1024 + lane * 16;
    for (unsigned spin = 0;; ++spin) {
      if (__builtin_amdgcn_ballot_w64(probe == SENT) == 0 || spin > SPIN_CAP) break;
      POLL_PAUSE(1);
      probe = __builtin_amdgcn_raw_buffer_load_b32(rs, po_probe, 0, 16);
    }
    u32x4 o0 = ho_ld(rs, oo), o1 = ho_ld(rs, oo + 1024), o2 = ho_ld(rs, oo + 2048), o3 = ho_ld(rs, oo + 3072);
    for (unsigned spin = 0;; ++spin) {
      const unsigned long long p0 = __builtin_amdgcn_ballot_w64(pend(o0)), p1 = __builtin_amdgcn_ballot_w64(pend(o1));
      const unsigned long long p2 = __builtin_amdgcn_ballot_w64(pend(o2)), p3 = __builtin_amdgcn_ballot_w64(pend(o3));
      if ((p0 | p1 | p2 | p3) == 0 || spin > SPIN_CAP) break;
      POLL_PAUSE(1);
      if (p0) o0 = ho_ld(rs, oo);
      if (p1) o1 = ho_ld(rs, oo + 1024);
      if (p2) o2 = ho_ld(rs, oo + 2048);
      if (p3) o3 = ho_ld(rs, oo + 3072);
    }
    const unsigned oa2 = wid * 4096 + lane * 16;
    lds_stu4(oa2, o0); lds_stu4(oa2 + 1024, o1); lds_stu4(oa2 + 2048, o2); lds_stu4(oa2 + 3072, o3);
    WAIT_LGKM0();
    if (lane == 0) __hip_atomic_fetch_or((LAS unsigned*)(uintptr_t)(L_RDY + 32), 1u << wid, __ATOMIC_RELAXED, __HIP_MEMORY_SCOPE_WORKGROUP);
  }
  STAMP(9);
  if (isd1) {
    const unsigned fa = F_OFF + wid * 2048 + lane * 16;
    lds_stu4(fa, fw0); lds_stu4(fa + 1024, fw1); lds_stu4(fa + 16384, fw2); lds_stu4(fa + 17408, fw3);
  } else if (isd2) {
    const unsigned fa = F_OFF + wid * 1024 + lane * 16;
    lds_stu4(fa, fw0); lds_stu4(fa + 8192, fw1); lds_stu4(fa + 16384, fw2); lds_stu4(fa + 24576, fw3); lds_stu4(fa + 32768, fw4); lds_stu4(fa + 40960, fw5);
  }
  WAIT_LGKM0();
  bf16x8 ff[6];
  {
    const unsigned ft = (8 * g + (i16 >> 2)) * 32 + (i16 & 3) * 8;
    const unsigned fl = F_OFF + wid * (isd1 ? 2048 : 1024) + ft, s1 = isd1 ? 1024 : 8192;
    ff[0] = cat8(tr_read(fl), tr_read(fl + 128)); ff[1] = cat8(tr_read(fl + s1), tr_read(fl + s1 + 128));
    ff[2] = cat8(tr_read(fl + 16384), tr_read(fl + 16384 + 128)); ff[3] = cat8(tr_read(fl + 16384 + s1), tr_read(fl + 16384 + s1 + 128));
    ff[4] = cat8(tr_read(fl + 32768), tr_read(fl + 32768 + 128)); ff[5] = cat8(tr_read(fl + 40960), tr_read(fl + 40960 + 128));
  }
  if (wid < 6) {
    const int rec = ((cbk + 1 + (wid >> 1)) & 3) * 4 + (wid & 1) * 2 + hh;
    u32x4 sv[1];
    poll_all<1>(rs, HO_ST + (tt * 16 + rec) * 512 + r32 * 16, 0, sv);
    lds_stu4(S_OFF + rec * 528 + r32 * 16, sv[0]);
    STAMP(10);
  }
  BARRIER();
  STAMP(11);
  const u32x4 hv = lds_ldu4(X_OFF + row * 272 + c8 * 16);
  u32x4 ov;
  {
    const f32x2 pr = *(const LAS f32x2*)(uintptr_t)(S_OFF + c8 * 528 + row * 16);
    f32x2 st;
    { float mbv, rden; ln_stats(row16_sum(pr[0]), row16_sum(pr[1]), mbv, rden); st = f32x2{mbv, rden}; }
    u32x4 lv;
#pragma unroll
    for (int j = 0; j < 4; ++j) {
      const float h0 = bf_lo(hv[j]), h1 = bf_hi(hv[j]);
      const unsigned d = pack_bf16(h0 - st[0], h1 - st[0]);
      lv[j] = pack_bf16(bf_lo(d) * st[1], bf_hi(d) * st[1]);
      ov[j] = pack_bf16(h0 + bf_lo(lv[j]), h1 + bf_hi(lv[j]));
    }
    lv = u32x4{san(lv[0]), san(lv[1]), san(lv[2]), san(lv[3])};
    PIN(lv);
    BARRIER();
    ho_st(rs, HO_LN + goff, lv);
  }
  BARRIER(); BARRIER(); BARRIER();
  STAMP(13);
  const unsigned mykey = lds_ldu(T_OFF + 640 + (lane & 7) * 4);
  const int myrow = 2047 - (int)(mykey & 0x7ffu);
  if (isd1) {
    u32x4 ax[2];
    poll_all<2>(rs, HO_LN + (myrow * D + wid * 64 + g * 8) * 2, 64, ax);
    f32x4 ag = {}, au = {};
    ag = MFMA16(BC8(ax[0]), ff[0], ag); au = MFMA16(BC8(ax[0]), ff[2], au);
    ag = MFMA16(BC8(ax[1]), ff[1], ag); au = MFMA16(BC8(ax[1]), ff[3], au);
    lds_stf4(R_OFF + wid * 1024 + lane * 16, hh ? au : ag);
    BARRIER();
    if (wid == 0) {
      f32x4 sum = lds_ldf4(R_OFF + lane * 16);
#pragma unroll
      for (int w2 = 1; w2 < 8; ++w2) sum += lds_ldf4(R_OFF + w2 * 1024 + lane * 16);
      const auto x0 = __builtin_amdgcn_permlane32_swap(__float_as_uint(sum[0]), __float_as_uint(sum[2]), false, false);
      const auto x1 = __builtin_amdgcn_permlane32_swap(__float_as_uint(sum[1]), __float_as_uint(sum[3]), false, false);
      const float g0 = rbf(__uint_as_float(x0[0])), u0 = rbf(__uint_as_float(x0[1])), g1 = rbf(__uint_as_float(x1[0])), u1 = rbf(__uint_as_float(x1[1]));
      const float a0f = rbf(g0 * sigmoid(g0)) * u0, a1f = rbf(g1 * sigmoid(g1)) * u1;
      const float n0 = dpp_f<DPP_X1>(a0f), n1 = dpp_f<DPP_X1>(a1f);
      const bool odd = lane & 1;
      const int tok = 4 * (g & 1) + 2 * hh + (odd ? 1 : 0);
      ho_st1(rs, HO_ACT + d1 * 256 + tok * 32 + (i16 >> 1) * 4, san(odd ? pack_bf16(n1, a1f) : pack_bf16(a0f, n0)));
    }
    STAMP(14);
  }
  {
    const int grow = tt * 32 + row;
    int sel = -1;
#pragma unroll
    for (int c = 0; c < 8; ++c) if (__builtin_amdgcn_readlane(myrow, c) == grow) sel = c;
    if (sel < 0) __builtin_amdgcn_raw_buffer_store_b128(ov, __builtin_amdgcn_make_buffer_rsrc(out, 0, S * D * 2, 0x00020000), goff, 0, 16);
    else ho_st(rs, HO_HS + (sel * D + cbk * 128 + c8 * 8) * 2, u32x4{san(hv[0]), san(hv[1]), san(hv[2]), san(hv[3])});
    if (b == 0 && tid == 0) __builtin_amdgcn_raw_buffer_store_b32(tag, rs, WS_EP, 0, 16);
  }
  if (isd2) {
    const int n0 = sub * 16;
    unsigned hw = SENT, lw = SENT, fkey = 0;
    const bool odd = lane & 1;
    const int tok = 4 * (g & 1) + 2 * hh + (odd ? 1 : 0), cp = i16 >> 1;
    float sig = 0.f;
    if (wid == 0) {
      fkey = lds_ldu(T_OFF + 640 + tok * 4);
      const int frow = 2047 - (int)(fkey & 0x7ffu);
      sig = rbf(sigmoid(key_score(fkey)));
      for (unsigned spin = 0;; ++spin) {
        if (hw == SENT) hw = __builtin_amdgcn_raw_buffer_load_b32(rs, HO_HS + (tok * D + n0 + cp * 2) * 2, 0, 16);
        if (lw == SENT) lw = __builtin_amdgcn_raw_buffer_load_b32(rs, HO_LN + (frow * D + n0 + cp * 2) * 2, 0, 16);
        if (!__any(hw == SENT || lw == SENT) || spin > SPIN_CAP) break;
        POLL_PAUSE(1);
      }
    }
    u32x4 av[6];
    const int ao = HO_ACT + ((wid * 2 + (g >> 1)) * 8 + (lane & 7)) * 32 + (g & 1) * 16;
    poll_all<6>(rs, ao, 4096, av, wid < 3 ? 0 : 4096);
    STAMP(14);
    f32x4 y = {}, y2 = {};
    y = MFMA16(BC8(av[0]), ff[0], y); y2 = MFMA16(BC8(av[1]), ff[1], y2);
    y = MFMA16(BC8(av[2]), ff[2], y); y2 = MFMA16(BC8(av[3]), ff[3], y2);
    y = MFMA16(BC8(av[4]), ff[4], y);
    if (wid < 3) y2 = MFMA16(BC8(av[5]), ff[5], y2);
    lds_stf4(R_OFF + wid * 1024 + lane * 16, hh ? y2 : y);
    BARRIER();
    if (wid == 0) {
      f32x4 sum = lds_ldf4(R_OFF + lane * 16);
#pragma unroll
      for (int w2 = 1; w2 < 8; ++w2) sum += lds_ldf4(R_OFF + w2 * 1024 + lane * 16);
      const auto x0 = __builtin_amdgcn_permlane32_swap(__float_as_uint(sum[0]), __float_as_uint(sum[2]), false, false);
      const auto x1 = __builtin_amdgcn_permlane32_swap(__float_as_uint(sum[1]), __float_as_uint(sum[3]), false, false);
      const float t0 = __uint_as_float(x0[0]) + __uint_as_float(x0[1]), t1 = __uint_as_float(x1[0]) + __uint_as_float(x1[1]);
      const float n0f = dpp_f<DPP_X1>(t0), n1f = dpp_f<DPP_X1>(t1);
      const float ylo = rbf(rbf(odd ? n1f : t0) * sig), yhi = rbf(rbf(odd ? t1 : n0f) * sig);
      *(unsigned*)((char*)out + ((size_t)(2047 - (int)(fkey & 0x7ffu)) * D + n0 + cp * 2) * 2) =
          pack_bf16(bf_lo(hw) + rbf(bf_lo(lw) + ylo), bf_hi(hw) + rbf(bf_hi(lw) + yhi));
    }
  }
  }
  STAMP(15); STAMP_OUT(0);
}

extern "C" void kernel_launch(void* const* d_in, const int* in_sizes, int n_in, void* d_out, int out_size, void* d_ws, size_t ws_size,
                              hipStream_t stream) {
  (void)hipFuncSetAttribute((const void*)k_fused, hipFuncAttributeMaxDynamicSharedMemorySize, LDS_BYTES);
  k_fused<<<256, 768, LDS_BYTES, stream>>>((const uint16_t*)d_in[0], (const uint16_t*)d_in[1], (const uint16_t*)d_in[2], (const uint16_t*)d_in[4],
                                           (const uint16_t*)d_in[5], (const uint16_t*)d_in[6], (const uint16_t*)d_in[7], (char*)d_ws, (uint16_t*)d_out);
}
```

```cpp
#include <hip/hip_runtime.h>
#include <hip/hip_bf16.h>
#include <stdint.h>

typedef short bf16x8 __attribute__((ext_vector_type(8)));
typedef short bf16x4 __attribute__((ext_vector_type(4)));
typedef float f32x16 __attribute__((ext_vector_type(16)));
typedef float f32x8 __attribute__((ext_vector_type(8)));
typedef float f32x4 __attribute__((ext_vector_type(4)));
typedef __bf16 b16x8 __attribute__((ext_vector_type(8)));
typedef __bf16 b16x16 __attribute__((ext_vector_type(16)));
typedef float f32x2 __attribute__((ext_vector_type(2)));
typedef __bf16 bf16x2_t __attribute__((ext_vector_type(2)));
typedef unsigned u32x4 __attribute__((ext_vector_type(4)));
typedef unsigned u32x2 __attribute__((ext_vector_type(2)));
#define LAS __attribute__((address_space(3)))
#define GAS __attribute__((address_space(1)))
#define DI __device__ __forceinline__
#define MFMA32(a, b, c) __builtin_amdgcn_mfma_f32_32x32x16_bf16((a), (b), (c), 0, 0, 0)
#define MFMA16(a, b, c) __builtin_amdgcn_mfma_f32_16x16x32_bf16((a), (b), (c), 0, 0, 0)
#define SBAR() __builtin_amdgcn_sched_barrier(0)
#define PIN(x) asm volatile("" : "+v"(x))
#define BC8(x) __builtin_bit_cast(bf16x8, x)

constexpr int S = 2048, D = 512, H = 32, F = 1376;
constexpr float LN_EPS = 1e-5f;
constexpr float QSCALE = 0.25f * 1.4426950408889634f;
constexpr unsigned SENT = 0xFFFFFFFFu;
constexpr unsigned SPIN_CAP = 1u << 16;

constexpr int WS_Q = 0;
constexpr int WS_KV = WS_Q + H * 64 * 1024;
constexpr int WS_FA = WS_KV + H * 64 * 2048;
constexpr int WS_FQ = WS_FA + H * 64 * 128;
constexpr int WS_FR = WS_FQ + H * 64 * 128;
constexpr int WS_EP = WS_FR + 4096;
constexpr int WS_HO = WS_EP + 4096;
constexpr int HO_O = WS_HO;
constexpr int HO_LN = HO_O + 64 * H * 1024;
constexpr int HO_ST = HO_LN + S * D * 2;
constexpr int HO_KEY = HO_ST + 64 * 16 * 512;
constexpr int HO_ACT = HO_KEY + 8 * S * 4;
constexpr int HO_HS = HO_ACT + 24576;
constexpr int HO_END = HO_HS + 8192;
constexpr int HO_PER_WG = 19 * 1024;
static_assert(WS_HO + 256 * HO_PER_WG >= HO_END, "reset covers the hand-off words");
constexpr int WS_BYTES = WS_HO + 256 * HO_PER_WG;

#define STAMP_DECL do {} while (0)
#define STAMP(i) do {} while (0)
#define STAMP_OUT(k) do {} while (0)

#define FSTAMP(i) do {} while (0)

DI unsigned pack_bf16(float lo, float hi) {
  f32x2 v = {lo, hi};
  bf16x2_t r = __builtin_convertvector(v, bf16x2_t);
  return __builtin_bit_cast(unsigned, r);
}
DI float bf_lo(unsigned v) { return __uint_as_float(v << 16); }
DI float bf_hi(unsigned v) { return __uint_as_float(v & 0xffff0000u); }
DI float rbf(float x) { return bf_lo(pack_bf16(x, x)); }
DI float sum_halves(float v) {
  auto r = __builtin_amdgcn_permlane32_swap(__float_as_uint(v), __float_as_uint(v), false, false);
  return __uint_as_float(r[0]) + __uint_as_float(r[1]);
}
DI float sum_rows(float v) {
  auto r = __builtin_amdgcn_permlane16_swap(__float_as_uint(v), __float_as_uint(v), false, false);
  return sum_halves(__uint_as_float(r[0]) + __uint_as_float(r[1]));
}
DI float max_halves(float v) {
  auto r = __builtin_amdgcn_permlane32_swap(__float_as_uint(v), __float_as_uint(v), false, false);
  return fmaxf(__uint_as_float(r[0]), __uint_as_float(r[1]));
}
template <int CTRL> DI float dpp_f(float v) { return __uint_as_float(__builtin_amdgcn_update_dpp(0u, __float_as_uint(v), CTRL, 0xf, 0xf, true)); }
template <int CTRL> DI unsigned dpp_u(unsigned v) { return __builtin_amdgcn_update_dpp(0u, v, CTRL, 0xf, 0xf, true); }
constexpr int DPP_X1 = 0xB1, DPP_X2 = 0x4E, DPP_HMIR = 0x141, DPP_MIR = 0x140;
DI float quad_sum(float v) { v += dpp_f<DPP_X1>(v); v += dpp_f<DPP_X2>(v); return v; }
DI float row16_sum(float v) { v = quad_sum(v); v += dpp_f<DPP_HMIR>(v); v += dpp_f<DPP_MIR>(v); return v; }
DI void glds16(const void* sbase, unsigned voff, unsigned lds_base) {
  unsigned sv;
  asm volatile("s_mov_b32 %0, m0\n\ts_mov_b32 m0, %3\n\ts_nop 0\n\tglobal_load_lds_dwordx4 %1, %2\n\ts_mov_b32 m0, %0"
               : "=&s"(sv) : "v"(voff), "s"(sbase), "s"(lds_base) : "memory");
}
DI void glds16s(const void* sbase, unsigned voff, unsigned lds_base) {
  unsigned sv;
  asm volatile("s_mov_b32 %0, m0\n\ts_mov_b32 m0, %3\n\ts_nop 0\n\tglobal_load_lds_dwordx4 %1, %2 sc1\n\ts_mov_b32 m0, %0"
               : "=&s"(sv) : "v"(voff), "s"(sbase), "s"(lds_base) : "memory");
}
DI void glds4(const void* sbase, unsigned voff, unsigned lds_base) {
  unsigned sv;
  asm volatile("s_mov_b32 %0, m0\n\ts_mov_b32 m0, %3\n\ts_nop 0\n\tglobal_load_lds_dword %1, %2\n\ts_mov_b32 m0, %0"
               : "=&s"(sv) : "v"(voff), "s"(sbase), "s"(lds_base) : "memory");
}
#define WAIT_VM(N) asm volatile("s_waitcnt vmcnt(" #N ")" ::: "memory")
#define WAIT_LGKM0() asm volatile("s_waitcnt lgkmcnt(0)" ::: "memory")
#define BARRIER() asm volatile("s_waitcnt lgkmcnt(0)\n\ts_barrier" ::: "memory")
typedef short v4i16_t __attribute__((ext_vector_type(4)));
DI bf16x4 tr_read(unsigned addr) {
  return __builtin_bit_cast(bf16x4, __builtin_amdgcn_ds_read_tr16_b64_v4i16((LAS v4i16_t*)(uintptr_t)addr));
}
DI bf16x8 lds_ld128(unsigned addr) { return *(const LAS bf16x8*)(uintptr_t)addr; }
DI f32x4 lds_ldf4(unsigned addr) { return *(const LAS f32x4*)(uintptr_t)addr; }
DI u32x4 lds_ldu4(unsigned addr) { return *(const LAS u32x4*)(uintptr_t)addr; }
DI void lds_stf4(unsigned addr, f32x4 v) { *(LAS f32x4*)(uintptr_t)addr = v; }
DI void lds_stu4(unsigned addr, u32x4 v) { *(LAS u32x4*)(uintptr_t)addr = v; }
DI void lds_stf(unsigned addr, float v) { *(LAS float*)(uintptr_t)addr = v; }
DI void lds_stu(unsigned addr, unsigned v) { *(LAS unsigned*)(uintptr_t)addr = v; }
DI float lds_ldf(unsigned addr) { return *(const LAS float*)(uintptr_t)addr; }
DI unsigned lds_ldu(unsigned addr) { return *(const LAS unsigned*)(uintptr_t)addr; }
DI bf16x8 cat8(bf16x4 a, bf16x4 b) { return __builtin_shufflevector(a, b, 0, 1, 2, 3, 4, 5, 6, 7); }
typedef __amdgpu_buffer_rsrc_t rsrc_t;
DI u32x4 ho_ld(rsrc_t rs, int off) { return __builtin_amdgcn_raw_buffer_load_b128(rs, off, 0, 16); }
DI void ho_st(rsrc_t rs, int off, u32x4 v) { __builtin_amdgcn_raw_buffer_store_b128(v, rs, off, 0, 16); }
DI void ho_st1(rsrc_t rs, int off, unsigned v) { __builtin_amdgcn_raw_buffer_store_b32(v, rs, off, 0, 16); }
DI unsigned san(unsigned v) { return min(v, SENT - 1u); }
#define POLL_PAUSE(n) do { __builtin_amdgcn_s_sleep(n); asm volatile("" ::: "memory"); } while (0)
DI bool pend(u32x4 v) { return max(max(v[0], v[1]), max(v[2], v[3])) == SENT; }

constexpr int LDS_BYTES = 163840;
constexpr int KVT = 2080;
constexpr int L_PRIV = 64 * KVT, PRIV = 2560;
constexpr int L_STG = L_PRIV + 8 * PRIV;
constexpr int L_RDY = L_STG + 4 * 2048;
constexpr int L_RS = L_RDY + 64;
static_assert(L_RS + 1024 <= LDS_BYTES, "lds map");
constexpr unsigned X_OFF = 32768, S_OFF = 45056, T_OFF = 55296, R_OFF = 57344, F_OFF = 81920;
#define DLY_FLAG 22
#define DLY_O 0
#define DLY_ST 3
#define DLY_KEY 13
#define DLY_ACT 2800
constexpr int N_D1 = 86;
constexpr int CODE_PF_BYTES = 25 * 1024;
struct AFrag { bf16x8 x0, x1, q, k, v; };

#define EX(x) __builtin_amdgcn_exp2f(x)
#define RCP(x) __builtin_amdgcn_rcpf(x)
#define SMF(a, b, c) __builtin_amdgcn_smfmac_f32_32x32x32_bf16(__builtin_bit_cast(b16x8, a), __builtin_bit_cast(b16x16, b), (c), sidx, 0, 0)
DI f32x8 lo8(const f32x16& s) { return __builtin_shufflevector(s, s, 0, 1, 2, 3, 4, 5, 6, 7); }
DI f32x8 hi8(const f32x16& s) { return __builtin_shufflevector(s, s, 8, 9, 10, 11, 12, 13, 14, 15); }
#define UNIT(SC, SN, KN, QN, NEGN, OPREV, VPREV, OCUR, VCUR0, LOADS) do { \
    SC[0] = EX(SC[0]); SC[1] = EX(SC[1]); SBAR(); \
    OPREV = SMF(VPREV, hi8(SN), OPREV); \
    LOADS; \
    SBAR(); \
    SC[2] = EX(SC[2]); SC[3] = EX(SC[3]); SC[4] = EX(SC[4]); SC[5] = EX(SC[5]); SBAR(); \
    SN = MFMA32(KN, QN, NEGN); SBAR(); \
    SC[6] = EX(SC[6]); SC[7] = EX(SC[7]); SC[8] = EX(SC[8]); SC[9] = EX(SC[9]); SC[10] = EX(SC[10]); SBAR(); \
    OCUR = SMF(VCUR0, lo8(SC), OCUR); SBAR(); \
    SC[11] = EX(SC[11]); SC[12] = EX(SC[12]); SC[13] = EX(SC[13]); SC[14] = EX(SC[14]); SC[15] = EX(SC[15]); SBAR(); \
  } while (0)
#define TWO_TILES(U) do { \
    UNIT(sA, sB, k0, qb2, ng, ob, v11, oa, v00, k1 = lds_ld128(ka + (U + 1) * KVT); v10 = lds_ld128(va0 + (U + 1) * KVT)); \
    UNIT(sB, sA, k1, qa, ng, oa, v01, ob, v00, v11 = lds_ld128(va1 + (U + 1) * KVT)); \
    UNIT(sA, sB, k1, qb2, ng, ob, v01, oa, v10, k0 = lds_ld128(ka + (U + 2) * KVT); v00 = lds_ld128(va0 + (U + 2) * KVT)); \
    UNIT(sB, sA, k0, qa, ng, oa, v11, ob, v10, v01 = lds_ld128(va1 + (U + 2) * KVT)); \
  } while (0)

DI int fresh_lane() {
  int l;
  asm volatile("v_mbcnt_lo_u32_b32 %0, -1, 0\n\tv_mbcnt_hi_u32_b32 %0, -1, %0" : "=v"(l));
  return l;
}
#define RELANE() const int lane = fresh_lane(), tid = wid * 64 + lane, r32 = lane & 31, hh = lane >> 5, g = lane >> 4, i16 = lane & 15; \
  (void)tid; (void)r32; (void)hh; (void)g; (void)i16
DI float tile_max(const f32x16& s) {
  float m = fmaxf(fmaxf(s[0], s[1]), s[2]);
#pragma unroll
  for (int j = 3; j < 15; j += 2) m = fmaxf(fmaxf(m, s[j]), s[j + 1]);
  return fmaxf(m, s[15]);
}
DI unsigned sort_key(float r, int idx) {
  unsigned u = pack_bf16(r, r) & 0xffffu;
  if ((u & 0x7fffu) > 0x7f80u) u = 0xffffu;
  u = (u & 0x8000u) ? (~u & 0xffffu) : (u | 0x8000u);
  return (u << 11) | (unsigned)(2047 - idx);
}
DI float key_score(unsigned key) {
  unsigned u = (key >> 11) & 0xffffu;
  u = (u & 0x8000u) ? (u & 0x7fffu) : (~u & 0xffffu);
  return __uint_as_float(u << 16);
}
DI void ln_stats(float s1, float s2, float& mb, float& rden) {
  const float mean = s1 * (1.f / D);
  const float var = fmaxf(s2 * (1.f / D) - mean * mean, 0.f);
  mb = rbf(mean);
  rden = RCP(rbf(__builtin_amdgcn_sqrtf(rbf(rbf(var) + LN_EPS))));
}
DI float sigmoid(float v) { return RCP(1.f + EX(v * -1.4426950408889634f)); }
DI void wait_rdy(unsigned need) {
  for (unsigned spin = 0;; ++spin) {
    const u32x4 r = *(volatile const LAS u32x4*)(uintptr_t)L_RDY;
    const unsigned m = min(min(r[0], r[1]), min(r[2], r[3]));
    if (__builtin_amdgcn_readfirstlane(m) >= need || spin > (SPIN_CAP << 4)) break;
    __builtin_amdgcn_s_sleep(1);
  }
  asm volatile("" ::: "memory");
}
DI void wait_word(unsigned addr, unsigned need) {
  for (unsigned spin = 0;; ++spin) {
    const unsigned r = *(volatile const LAS unsigned*)(uintptr_t)addr;
    if (__builtin_amdgcn_readfirstlane(r) >= need || spin > (SPIN_CAP << 4)) break;
    __builtin_amdgcn_s_sleep(1);
  }
  asm volatile("" ::: "memory");
}
DI unsigned gt_bit(unsigned m, unsigned a, unsigned c) { return __builtin_amdgcn_alignbit(m, c - a, 31); }
DI unsigned count_gt(u32x4 a, u32x4 b, unsigned c) {
  unsigned m = 0, n = 0;
#pragma unroll
  for (int j = 0; j < 4; ++j) { m = gt_bit(m, a[j], c); n = gt_bit(n, b[j], c); }
  return __builtin_popcount(m) + __builtin_popcount(n);
}
DI unsigned max4(u32x4 v) { return max(max(v[0], v[1]), max(v[2], v[3])); }
template <int N> DI void poll_all(rsrc_t rs, int off, int stride, u32x4 (&v)[N], int back = 0) {
  for (unsigned spin = 0;; ++spin) {
#pragma unroll
    for (int j = 0; j < N; ++j) v[j] = ho_ld(rs, off + j * stride - (j == N - 1 ? back : 0));
    unsigned m = 0;
#pragma unroll
    for (int j = 0; j < N; ++j) m = max(m, max4(v[j]));
    if (__builtin_amdgcn_ballot_w64(m == SENT) == 0 || spin > SPIN_CAP) break;
    POLL_PAUSE(1);
  }
}
DI unsigned sum8_u(unsigned v) { v += dpp_u<DPP_X1>(v); v += dpp_u<DPP_X2>(v); v += dpp_u<DPP_HMIR>(v); return v; }

__global__ __launch_bounds__(768, 3) void k_fused(const uint16_t* __restrict__ x, const uint16_t* __restrict__ wqkv, const uint16_t* __restrict__ wo,
                                                   const uint16_t* __restrict__ wr, const uint16_t* __restrict__ wg, const uint16_t* __restrict__ wu,
                                                   const uint16_t* __restrict__ wd, char* __restrict__ ws, uint16_t* __restrict__ out) {
  extern __shared__ __attribute__((aligned(16))) char smem[];
  asm volatile("" :: "s"((unsigned)(uintptr_t)smem));
  const int tid = threadIdx.x, lane = tid & 63, wid = __builtin_amdgcn_readfirstlane(tid >> 6);
  const int b = blockIdx.x, xcd = b & 7, sub = b >> 3;
  const int r32 = lane & 31, hh = lane >> 5, g = lane >> 4, i16 = lane & 15;
  const rsrc_t rs = __builtin_amdgcn_make_buffer_rsrc(ws, 0, WS_BYTES, 0x00020000);
  const int rb = (xcd >> 1) * 8 + (sub & 7), hq = (xcd & 1) * 4 + (sub >> 3);
  const int head = xcd * 4 + (sub & 3), qb = sub >> 2;
  const int tt = xcd * 8 + (sub >> 2), cbk = sub & 3;
  const int d1 = (xcd < 7 && sub < 13) ? xcd * 13 + sub : 999;
  const bool isd1 = d1 < N_D1, isd2 = xcd == 7;
  STAMP_DECL; STAMP(0);
  unsigned code_pf = 0;
  if (wid == 7) {
    const unsigned long long pc = __builtin_amdgcn_s_getpc() & ~0xFFull;
    const unsigned off = min((unsigned)((sub & 3) * 8192 + lane * 128), (unsigned)(CODE_PF_BYTES - 128));
    asm volatile("global_load_dword %0, %1, %2" : "=v"(code_pf) : "v"(off), "s"(pc) : "memory");
  }

  if (__builtin_expect(wid >= 8, 1)) {
    const int hw = wid - 8;
    __builtin_amdgcn_s_setprio(3);
    {
      const int rot = ((sub & 7) + 2 * (sub >> 3)) & 7;
      const char* base; unsigned vo0, vo1, cstr, pstr;
      if (hw < 3) {
        base = (const char*)wqkv + hw * 1024 + hq * 128; cstr = 64 * 3072; pstr = 8 * 3072;
        vo0 = (lane >> 3) * 3072 + (((lane & 7) ^ ((g & 1) << 1)) * 16); vo1 = (lane >> 3) * 3072 + (((lane & 7) ^ (((g & 1) | 2) << 1)) * 16);
      } else {
        base = (const char*)x + (size_t)rb * 65536; cstr = 128; pstr = 8192;
        vo0 = (lane >> 3) * 1024 + (((lane & 7) ^ g) * 16); vo1 = (lane >> 3) * 1024 + (((lane & 7) ^ (4 + g)) * 16);
      }
      auto issue = [&](int c) {
        const char* src = base + (size_t)((c + rot) & 7) * cstr;
        const unsigned dst = (c & 3) * 32768 + hw * 8192;
#pragma unroll
        for (int p = 0; p < 8; ++p) glds16(src + (size_t)p * pstr, (p & 1) ? vo1 : vo0, dst + p * 1024);
      };
#pragma unroll 1
      for (int c = -3; c < 7; ++c) {
        if (c >= -1) {
          if (c <= 5) { WAIT_VM(8); } else { WAIT_VM(0); }
          BARRIER();
        }
        if (c + 3 < 8) issue(c + 3);
      }
    }
    unsigned tag = __builtin_amdgcn_raw_buffer_load_b32(rs, WS_EP, 0, 16);
    tag = __builtin_amdgcn_readfirstlane(tag) + 1u;
    BARRIER();
    STAMP(4);
    if (lane < 32) {
      const unsigned o = (lane & 1) ? 0u : 0x3f803f80u;
      lds_stu4((hw * 16 + (lane >> 1)) * KVT + 2048 + (lane & 1) * 16, u32x4{o, o, o, o});
    }
    {
      const int fo = WS_FA + (head * 64 + ((lane >> 1) & 7) * 8 + hw * 2 + (lane & 1)) * 128;
      const int fo0 = ((lane & 2) ? WS_FQ + qb * 8 * 128 : WS_FA) + (head * 64 + hw * 2 + (lane & 1)) * 128;
      unsigned okm = 0;
      auto need = [&](unsigned bits) {
        for (unsigned spin = 0; (okm & bits) != bits && spin <= SPIN_CAP; ++spin) {
          const u32x2 f = __builtin_amdgcn_raw_buffer_load_b64(rs, fo, 0, 16);
          okm = (unsigned)__builtin_amdgcn_ballot_w64(f[0] == tag && f[1] == ~tag);
          if ((okm & bits) != bits) POLL_PAUSE(1);
        }
        asm volatile("" ::: "memory");
      };
      const char* kvg = ws + WS_KV + (size_t)head * (64 * 2048) + hw * 4096;
      const char* qsrc = ws + WS_Q + (size_t)(head * 64 + qb * 8 + hw * 2) * 1024;
      auto kv_step = [&](int i) {
        const char* src = kvg + i * 16384;
#pragma unroll
        for (int p = 0; p < 4; ++p) glds16s(src + p * 1024, lane * 16, (hw + i * 4 + (p >> 1) * 32) * KVT + (p & 1) * 1024);
      };
      wait_word(L_RDY + 36, 8u);
      __builtin_amdgcn_s_sleep(DLY_FLAG);
      for (unsigned spin = 0; spin <= SPIN_CAP; ++spin) {
        const u32x2 f = __builtin_amdgcn_raw_buffer_load_b64(rs, fo0, 0, 16);
        if (__builtin_amdgcn_ballot_w64(f[0] != tag || f[1] != ~tag) == 0) break;
        POLL_PAUSE(1);
      }
      asm volatile("" ::: "memory");
      STAMP(5);
      glds16s(qsrc, lane * 16, L_STG + hw * 2048); glds16s(qsrc + 1024, lane * 16, L_STG + hw * 2048 + 1024);
      kv_step(0);
      {
        const u32x2 f = __builtin_amdgcn_raw_buffer_load_b64(rs, fo, 0, 16);
        asm volatile("s_waitcnt vmcnt(1) lgkmcnt(0)" ::: "memory");
        lds_stu(L_RDY + hw * 4, 1u);
        okm = (unsigned)__builtin_amdgcn_ballot_w64(f[0] == tag && f[1] == ~tag);
      }
      need(0xCu);
      kv_step(1);
      if ((okm & 0xF0u) != 0xF0u) { WAIT_VM(0); lds_stu(L_RDY + hw * 4, 2u); need(0xF0u); }
      kv_step(2); kv_step(3);
      WAIT_VM(8);
      lds_stu(L_RDY + hw * 4, 2u);
      if ((okm & 0xFF00u) != 0xFF00u) { WAIT_VM(0); lds_stu(L_RDY + hw * 4, 4u); need(0xFF00u); }
      kv_step(4); kv_step(5);
      WAIT_VM(8);
      lds_stu(L_RDY + hw * 4, 4u);
      kv_step(6); kv_step(7);
      WAIT_VM(0);
      lds_stu(L_RDY + hw * 4, 8u);
    }
    STAMP(6);
    if (hw == 0) {
      const u32x4 sv = {SENT, SENT, SENT, SENT};
#pragma unroll 1
      for (int j = 0; j < 19; ++j) __builtin_amdgcn_raw_buffer_store_b128(sv, rs, lane * 16, WS_HO + b * HO_PER_WG + j * 1024, 16);
      WAIT_VM(0);
      if (lane == 0) __builtin_amdgcn_raw_buffer_store_b64(u32x2{tag, ~tag}, rs, WS_FR + b * 8, 0, 16);
    }
    bf16x8 wf[32];
    float swr = 0.f;
    f32x16 acc;
    {
      const char* wsrc = (const char*)wo + (lane >> 2) * 1024 + cbk * 256 + hw * 64 + (lane & 3) * 16;
#pragma unroll
      for (int i = 0; i < 32; ++i) wf[i] = *(const bf16x8*)(wsrc + i * 16384);
      const unsigned stg = L_STG + hw * 2048;
      const unsigned wor = stg + (4 * (g >> 1) + (i16 >> 2)) * 64 + (g & 1) * 32 + (i16 & 3) * 8;
      wait_word(L_RDY + 16 + hw * 4, 2u);
#pragma unroll
      for (int i = 0; i < 32; ++i) {
        *(LAS bf16x8*)(uintptr_t)(stg + (i & 1) * 1024 + lane * 16) = wf[i];
        WAIT_LGKM0();
        wf[i] = cat8(tr_read(wor + (i & 1) * 1024), tr_read(wor + (i & 1) * 1024 + 512));
      }
      WAIT_LGKM0();
      const char* xs = (const char*)x + ((tt * 32 + r32) * D + cbk * 128 + hw * 32 + 4 * hh) * 2;
#pragma unroll
      for (int gq = 0; gq < 4; ++gq) {
        const u32x2 xw = *(const u32x2*)(xs + gq * 16);
        acc[4 * gq] = bf_lo(xw[0]); acc[4 * gq + 1] = bf_hi(xw[0]); acc[4 * gq + 2] = bf_lo(xw[1]); acc[4 * gq + 3] = bf_hi(xw[1]);
      }
      lds_stf(stg + r32 * 4, bf_lo(wr[cbk * 128 + hw * 32 + r32]));
      const u32x4 wra = *(const u32x4*)((const char*)wr + lane * 16);
#pragma unroll
      for (int j = 0; j < 4; ++j) swr += bf_lo(wra[j]) + bf_hi(wra[j]);
      swr = __uint_as_float(__builtin_amdgcn_readfirstlane(__float_as_uint(sum_rows(row16_sum(swr)))));
      PIN(acc);
      if (isd1) {
#pragma unroll 1
        for (int p = 0; p < 4; ++p) glds4(hw < 2 ? wg : wu, (((hw & 1) * 4 + p) * 64 + lane) * (F * 2) + d1 * 32, stg + 1024);
      } else if (isd2) {
#pragma unroll 1
        for (int p = hw; p < 22; p += 4) glds4(wd, min(p * 64 + lane, F - 1) * (D * 2) + sub * 32, stg + 1024);
      }
      WAIT_VM(0);
    }
    STAMP(7);
    if (hw == 0) {
      for (unsigned spin = 0;; ++spin) {
        const u32x4 f0 = ho_ld(rs, WS_FR + lane * 16), f1 = ho_ld(rs, WS_FR + 1024 + lane * 16);
        const bool okf = f0[0] == tag && f0[1] == ~tag && f0[2] == tag && f0[3] == ~tag && f1[0] == tag && f1[1] == ~tag && f1[2] == tag && f1[3] == ~tag;
        if (__all(okf) || spin > SPIN_CAP) break;
        POLL_PAUSE(8);
      }
    }
    STAMP(8);
    BARRIER();
    {
      RELANE();
      const unsigned ol = lane * 16;
      const int sto = HO_ST + (tt * 16 + cbk * 4 + hw) * 512 + r32 * 16;
      const unsigned ha = X_OFF + r32 * 272 + (hw * 32 + 4 * hh) * 2, wa = L_STG + hw * 2048 + 4 * hh * 4;
      f32x4 wv[4];
      auto wait_bit = [&](int bit) {
        for (unsigned spin = 0;; ++spin) {
          const unsigned m = __builtin_amdgcn_readfirstlane(*(volatile const LAS unsigned*)(uintptr_t)(L_RDY + 32));
          if (((m >> bit) & 1u) || spin > (SPIN_CAP << 4)) break;
          __builtin_amdgcn_s_sleep(1);
        }
        asm volatile("" ::: "memory");
      };
      bf16x8 of[4], nf[4];
      wait_bit(0);
#pragma unroll
      for (int i = 0; i < 4; ++i) of[i] = lds_ld128(ol + i * 1024);
#pragma unroll
      for (int w2 = 0; w2 < 8; ++w2) {
        unsigned mk = 0;
        if (w2 >= 1 && w2 < 7) {
          mk = *(volatile const LAS unsigned*)(uintptr_t)(L_RDY + 32);
          asm volatile("" ::: "memory");
#pragma unroll
          for (int i = 0; i < 4; ++i) nf[i] = lds_ld128(ol + (w2 * 4 + 4 + i) * 1024);
        }
        if (w2 == 6) {
#pragma unroll
          for (int gq = 0; gq < 4; ++gq) wv[gq] = lds_ldf4(wa + gq * 32);
        }
        SBAR();
#pragma unroll
        for (int i = 0; i < 4; ++i) acc = MFMA32(wf[w2 * 4 + i], of[i], acc);
        SBAR();
        if (w2 < 7 && !((__builtin_amdgcn_readfirstlane(mk) >> (w2 + 1)) & 1u)) {
          wait_bit(w2 + 1);
#pragma unroll
          for (int i = 0; i < 4; ++i) nf[i] = lds_ld128(ol + (w2 * 4 + 4 + i) * 1024);
        }
#pragma unroll
        for (int i = 0; i < 4; ++i) of[i] = nf[i];
      }
      STAMP(9);
      f32x2 t1 = {0.f, 0.f}, t2 = t1, t3 = t1;
#pragma unroll
      for (int j = 0; j < 8; ++j) {
        const f32x2 v = {acc[2 * j], acc[2 * j + 1]}, w = {wv[j >> 1][2 * (j & 1)], wv[j >> 1][2 * (j & 1) + 1]};
        t1 += v; t2 += v * v; t3 += v * w;
      }
      const float s1 = sum_halves(t1[0] + t1[1]), s2 = sum_halves(t2[0] + t2[1]), s3 = sum_halves(t3[0] + t3[1]);
      const u32x4 rec = {san(__float_as_uint(s1)), san(__float_as_uint(s2)), san(__float_as_uint(s3)), 0u};
      if (lane < 32) ho_st(rs, sto, rec);
      if (lane == 0) __hip_atomic_fetch_add((LAS unsigned*)(uintptr_t)(L_RDY + 40), 1u, __ATOMIC_RELAXED, __HIP_MEMORY_SCOPE_WORKGROUP);
      lds_stu4(S_OFF + (cbk * 4 + hw) * 528 + r32 * 16, rec);
#pragma unroll
      for (int gq = 0; gq < 4; ++gq)
        *(LAS u32x2*)(uintptr_t)(ha + gq * 16) = u32x2{pack_bf16(acc[4 * gq], acc[4 * gq + 1]), pack_bf16(acc[4 * gq + 2], acc[4 * gq + 3])};
    }
    BARRIER();
    if (hw == 0) {
      RELANE();
      f32x4 t = lds_ldf4(S_OFF + hh * 8 * 528 + r32 * 16);
#pragma unroll
      for (int j = 1; j < 8; ++j) t += lds_ldf4(S_OFF + (hh * 8 + j) * 528 + r32 * 16);
      const float s1 = sum_halves(t[0]), s2 = sum_halves(t[1]), s3 = sum_halves(t[2]);
      float mbv, rden;
      ln_stats(s1, s2, mbv, rden);
      ho_st1(rs, HO_KEY + (2 * cbk + hh) * (S * 4) + (tt * 32 + r32) * 4, sort_key((s3 - mbv * swr) * rden, tt * 32 + r32));
    }
    STAMP(11);
    {
      constexpr unsigned G = T_OFF, M1 = T_OFF + 256, C2 = T_OFF + 512, SEL = T_OFF + 640, C1 = T_OFF + 1024;
      const int ht = hw * 64 + fresh_lane(), t3 = ht & 3, t4 = ht >> 2;
      u32x4 kv[2];
      if (hw == 0) __builtin_amdgcn_s_sleep(DLY_KEY); else __builtin_amdgcn_s_sleep(DLY_KEY + 8);
      poll_all<2>(rs, HO_KEY + xcd * (S * 4) + ht * 32, 16, kv);
      STAMP(12);
      const unsigned m0 = max4(kv[0]), m1 = max4(kv[1]);
      unsigned gm = max(m0, m1); gm = max(gm, dpp_u<DPP_X1>(gm)); gm = max(gm, dpp_u<DPP_X2>(gm));
      lds_stu(G + t4 * 4, gm);
      BARRIER();
      {
        unsigned rk = count_gt(lds_ldu4(G + t3 * 64), lds_ldu4(G + t3 * 64 + 16), gm) + count_gt(lds_ldu4(G + t3 * 64 + 32), lds_ldu4(G + t3 * 64 + 48), gm);
        rk += dpp_u<DPP_X1>(rk); rk += dpp_u<DPP_X2>(rk);
        if (rk < 8) {
          lds_stu4(C1 + (rk * 8 + t3 * 2) * 16, kv[0]); lds_stu4(C1 + (rk * 8 + t3 * 2 + 1) * 16, kv[1]);
          *(LAS u32x2*)(uintptr_t)(M1 + (rk * 8 + t3 * 2) * 4) = u32x2{m0, m1};
        }
      }
      BARRIER();
      {
        const unsigned cm = lds_ldu(M1 + t4 * 4), key = lds_ldu(C1 + ht * 4);
        unsigned rk = count_gt(lds_ldu4(M1 + t3 * 64), lds_ldu4(M1 + t3 * 64 + 16), cm) + count_gt(lds_ldu4(M1 + t3 * 64 + 32), lds_ldu4(M1 + t3 * 64 + 48), cm);
        rk += dpp_u<DPP_X1>(rk); rk += dpp_u<DPP_X2>(rk);
        if (rk < 8) lds_stu(C2 + rk * 16 + t3 * 4, key);
      }
      BARRIER();
      {
        const unsigned key = lds_ldu(C2 + (ht >> 3) * 4);
        const u32x4 o = lds_ldu4(C2 + (ht & 7) * 16);
        const unsigned rk = sum8_u(((key - o[0]) >> 31) + ((key - o[1]) >> 31) + ((key - o[2]) >> 31) + ((key - o[3]) >> 31));
        if (rk < 8) lds_stu(SEL + rk * 4, key);
      }
      BARRIER();
    }
    STAMP(15); STAMP_OUT(0);
    return;
  }

  unsigned tag;
  {
    const int rt = wid & 1, hd = wid >> 1, sw = hd >> 1, par = rt ^ (hd & 1);
    tag = __builtin_amdgcn_raw_buffer_load_b32(rs, WS_EP, 0, 16);
    const int xr0 = rt * 32 + sw * 16 + i16, xr1 = xr0 ^ 16;
    const int xf = (i16 >> 1) & 7;
    const unsigned xb0 = 24576 + (xr0 >> 3) * 1024 + (xr0 & 7) * 128, xb1 = 24576 + (xr1 >> 3) * 1024 + (xr1 & 7) * 128;
    const unsigned xoA = ((4 * par + g) ^ xf) * 16, xoB = ((4 * (par ^ 1) + g) ^ xf) * 16;
    const unsigned wl = (8 * g + (i16 >> 2)) * 128 + ((hd ^ (((i16 >> 3) & 1) | ((g & 1) << 1))) * 32) + (i16 & 3) * 8;
    unsigned x0A = xb0 + xoA, x1A = xb1 + xoA, x0B = xb0 + xoB, x1B = xb1 + xoB, wAa = wl + par * 4096, wBa = wl + (par ^ 1) * 4096;
#define RDX(a, so) lds_ld128((a) + (so))
#define RDW(a, so, t) cat8(tr_read((a) + (so) + (t) * 8192), tr_read((a) + (so) + (t) * 8192 + 512))
    f32x4 aq0 = {}, aq1 = {}, ak0 = {}, ak1 = {}, av0 = {}, av1 = {};
    f32x4 rsum = {}, rsq = {}, cs0 = {}, cs1 = {}, cs2 = {};
    bf16x8 ones8 = {0x3f80, 0x3f80, 0x3f80, 0x3f80, 0x3f80, 0x3f80, 0x3f80, 0x3f80};
    PIN(ones8);
    if (tid < 16) lds_stu(L_RDY + tid * 4, 0u);
    const unsigned pv = L_PRIV + wid * PRIV, po = L_PRIV + (wid ^ 1) * PRIV;
    unsigned ad_rs = (g == (i16 >> 2)) ? L_RS + ((rt * 4 + hd) * 16 + i16) * 8 : pv + lane * 16;
    unsigned ad_cq = (i16 == 0) ? po + 1280 + g * 16 : pv + lane * 16, ad_cv = po + 1280 + 128 + i16 * 4;
    unsigned rd_rs = L_RS + ((rt * 4 + (g & 1) * 2) * 16 + i16) * 8, rd_cq = pv + 1280 + g * 16, rd_cv = pv + 1280 + 128 + i16 * 4;
    unsigned ad_mr = pv + 2048 + r32 * 4, rd_mr = pv + 2048 + (sw * 16 + 4 * g) * 4;
    const int ahead = hq * 4 + hd, tile = rb * 2 + rt;
    unsigned go_k = WS_KV + (ahead * 64 + tile) * 2048 + (lane ^ (sw * 16)) * 16, go_q = WS_Q + (ahead * 64 + tile) * 1024 + (lane ^ (sw * 16)) * 16;
    unsigned go_v = WS_KV + (ahead * 64 + tile) * 2048 + 1024 + ((g & 1) ^ sw) * 512 + (hh * 16 + i16) * 16;
    PIN(ad_rs); PIN(ad_cq); PIN(ad_cv); PIN(rd_rs); PIN(rd_cq); PIN(rd_cv); PIN(ad_mr); PIN(rd_mr); PIN(go_k); PIN(go_q); PIN(go_v);
    PIN(x0A); PIN(x1A); PIN(x0B); PIN(x1B); PIN(wAa); PIN(wBa);
    BARRIER();
    STAMP(1);
    AFrag fa, fb;
    fa.q = RDW(wAa, 0, 0); fa.x0 = RDX(x0A, 0); fa.x1 = RDX(x1A, 0); fa.k = RDW(wAa, 0, 1); fa.v = RDW(wAa, 0, 2);
    fb.q = RDW(wBa, 0, 0); fb.x0 = RDX(x0B, 0); fb.x1 = RDX(x1B, 0); fb.k = RDW(wBa, 0, 1); fb.v = RDW(wBa, 0, 2);
#pragma unroll
    for (int c = 0; c < 7; ++c) {
      asm volatile("s_barrier" ::: "memory");
      FSTAMP(16 + c);
      if (((c + 1) & 1) == 0) {
        const unsigned d = ((c + 1) & 2) ? 65536u : (unsigned)-65536;
        x0A += d; x1A += d; x0B += d; x1B += d; wAa += d; wBa += d;
        PIN(x0A); PIN(x1A); PIN(x0B); PIN(x1B); PIN(wAa); PIN(wBa);
      }
      const unsigned so = ((c + 1) & 1) * 32768;
      SBAR();
      aq0 = MFMA16(fa.q, fa.x0, aq0); aq1 = MFMA16(fa.q, fa.x1, aq1);
      cs0 = MFMA16(fa.q, ones8, cs0);
      SBAR(); fa.q = RDW(wAa, so, 0); SBAR();
      ak0 = MFMA16(fa.k, fa.x0, ak0); ak1 = MFMA16(fa.k, fa.x1, ak1);
      cs1 = MFMA16(fa.k, ones8, cs1);
      SBAR(); fa.k = RDW(wAa, so, 1); SBAR();
      av0 = MFMA16(fa.x0, fa.v, av0); av1 = MFMA16(fa.x1, fa.v, av1);
      SBAR(); fa.x1 = RDX(x1A, so); SBAR();
      cs2 = MFMA16(ones8, fa.v, cs2); rsum = MFMA16(ones8, fa.x0, rsum); rsq = MFMA16(fa.x0, fa.x0, rsq);
      SBAR(); fa.x0 = RDX(x0A, so); fa.v = RDW(wAa, so, 2); SBAR();
      aq0 = MFMA16(fb.q, fb.x0, aq0); aq1 = MFMA16(fb.q, fb.x1, aq1);
      SBAR(); fb.q = RDW(wBa, so, 0); SBAR();
      ak0 = MFMA16(fb.k, fb.x0, ak0); ak1 = MFMA16(fb.k, fb.x1, ak1);
      SBAR(); fb.k = RDW(wBa, so, 1); SBAR();
      av0 = MFMA16(fb.x0, fb.v, av0); av1 = MFMA16(fb.x1, fb.v, av1);
      SBAR(); fb.x0 = RDX(x0B, so); fb.x1 = RDX(x1B, so); fb.v = RDW(wBa, so, 2); SBAR();
    }
    rsum = MFMA16(ones8, fa.x0, rsum); rsq = MFMA16(fa.x0, fa.x0, rsq);
    cs0 = MFMA16(fa.q, ones8, cs0); cs1 = MFMA16(fa.k, ones8, cs1); cs2 = MFMA16(ones8, fa.v, cs2);
    SBAR();
    aq0 = MFMA16(fa.q, fa.x0, aq0); aq1 = MFMA16(fa.q, fa.x1, aq1);
    SBAR();
    {
      const bool b0 = lane & 1, b1 = lane & 2;
      const float dlo = b0 ? rsq[1] : rsq[0], dhi = b0 ? rsq[3] : rsq[2];
      *(LAS f32x2*)(uintptr_t)ad_rs = f32x2{rsum[0], b1 ? dhi : dlo};
      lds_stf4(ad_cq, cs0); lds_stf4(ad_cq + 64, cs1); lds_stf(ad_cv, cs2[0]);
    }
    STAMP(2);
    BARRIER();
    {
      const f32x2 pa = *(const LAS f32x2*)(uintptr_t)rd_rs, pb = *(const LAS f32x2*)(uintptr_t)(rd_rs + 128);
      const f32x4 oq = lds_ldf4(rd_cq), ok = lds_ldf4(rd_cq + 64);
      const float ov = lds_ldf(rd_cv);
      SBAR();
      aq0 = MFMA16(fb.q, fb.x0, aq0); aq1 = MFMA16(fb.q, fb.x1, aq1);
      ak0 = MFMA16(fa.k, fa.x0, ak0); ak1 = MFMA16(fa.k, fa.x1, ak1);
      SBAR();
      float mb, rden;
      ln_stats(pa[0] + pb[0], pa[1] + pb[1], mb, rden);
      lds_stf(ad_mr, mb); lds_stf(ad_mr + 128, rden);
      const f32x4 cq = cs0 + oq, ck = cs1 + ok;
      const float cv = cs2[0] + ov;
      const auto sm = __builtin_amdgcn_permlane16_swap(__float_as_uint(mb), __float_as_uint(mb), false, false);
      const auto sd = __builtin_amdgcn_permlane16_swap(__float_as_uint(rden), __float_as_uint(rden), false, false);
      const float m0 = __uint_as_float(sw ? sm[1] : sm[0]), m1 = __uint_as_float(sw ? sm[0] : sm[1]);
      const float d0 = __uint_as_float(sw ? sd[1] : sd[0]), d1v = __uint_as_float(sw ? sd[0] : sd[1]);
      unsigned a0, a1, b0, b1;
      const float e0 = d0 * QSCALE, e1 = d1v * QSCALE;
      a0 = pack_bf16((aq0[0] - m0 * cq[0]) * e0, (aq0[1] - m0 * cq[1]) * e0); a1 = pack_bf16((aq0[2] - m0 * cq[2]) * e0, (aq0[3] - m0 * cq[3]) * e0);
      b0 = pack_bf16((aq1[0] - m1 * cq[0]) * e1, (aq1[1] - m1 * cq[1]) * e1); b1 = pack_bf16((aq1[2] - m1 * cq[2]) * e1, (aq1[3] - m1 * cq[3]) * e1);
      { auto r0 = __builtin_amdgcn_permlane16_swap(a0, b0, false, false); auto r1 = __builtin_amdgcn_permlane16_swap(a1, b1, false, false);
        ho_st(rs, go_q, u32x4{r0[0], r1[0], r0[1], r1[1]}); }
      SBAR();
      ak0 = MFMA16(fb.k, fb.x0, ak0); ak1 = MFMA16(fb.k, fb.x1, ak1);
      av0 = MFMA16(fa.x0, fa.v, av0); av1 = MFMA16(fa.x1, fa.v, av1);
      av0 = MFMA16(fb.x0, fb.v, av0); av1 = MFMA16(fb.x1, fb.v, av1);
      WAIT_LGKM0();
      const f32x4 mv0 = lds_ldf4(rd_mr), mv1 = lds_ldf4(rd_mr ^ 64), dv0 = lds_ldf4(rd_mr + 128), dv1 = lds_ldf4((rd_mr ^ 64) + 128);
      u32x4 pk, pvv;
      a0 = pack_bf16((ak0[0] - m0 * ck[0]) * d0, (ak0[1] - m0 * ck[1]) * d0); a1 = pack_bf16((ak0[2] - m0 * ck[2]) * d0, (ak0[3] - m0 * ck[3]) * d0);
      b0 = pack_bf16((ak1[0] - m1 * ck[0]) * d1v, (ak1[1] - m1 * ck[1]) * d1v); b1 = pack_bf16((ak1[2] - m1 * ck[2]) * d1v, (ak1[3] - m1 * ck[3]) * d1v);
      { auto r0 = __builtin_amdgcn_permlane16_swap(a0, b0, false, false); auto r1 = __builtin_amdgcn_permlane16_swap(a1, b1, false, false);
        pk = u32x4{r0[0], r1[0], r0[1], r1[1]}; }
      a0 = pack_bf16((av0[0] - mv0[0] * cv) * dv0[0], (av0[1] - mv0[1] * cv) * dv0[1]); a1 = pack_bf16((av0[2] - mv0[2] * cv) * dv0[2], (av0[3] - mv0[3] * cv) * dv0[3]);
      b0 = pack_bf16((av1[0] - mv1[0] * cv) * dv1[0], (av1[1] - mv1[1] * cv) * dv1[1]); b1 = pack_bf16((av1[2] - mv1[2] * cv) * dv1[2], (av1[3] - mv1[3] * cv) * dv1[3]);
      { auto r0 = __builtin_amdgcn_permlane16_swap(a0, b0, false, false); auto r1 = __builtin_amdgcn_permlane16_swap(a1, b1, false, false);
        pvv = u32x4{r0[0], r1[0], r0[1], r1[1]}; }
      tag = __builtin_amdgcn_readfirstlane(tag) + 1u;
      const u32x4 fl = {tag, ~tag, tag, ~tag};
      PIN(pk); PIN(pvv);
      STAMP(3);
      if (rb < 4) { ho_st(rs, go_k, pk); ho_st(rs, go_v, pvv); asm volatile("s_waitcnt vmcnt(2)" : "+v"(code_pf) :: "memory"); }
      else asm volatile("s_waitcnt vmcnt(0)" : "+v"(code_pf) :: "memory");
      if (lane < 8) ho_st(rs, WS_FQ + (ahead * 64 + tile) * 128 + lane * 16, fl);
      if (lane == 0) __hip_atomic_fetch_add((LAS unsigned*)(uintptr_t)(L_RDY + 36), 1u, __ATOMIC_RELAXED, __HIP_MEMORY_SCOPE_WORKGROUP);
      if (rb >= 4) {
        ho_st(rs, go_k, pk); ho_st(rs, go_v, pvv); WAIT_VM(0);
      } else WAIT_VM(1);
      if (lane < 8) ho_st(rs, WS_FA + (ahead * 64 + tile) * 128 + lane * 16, fl);
    }
    STAMP(4);
  }

  const int qg = wid & 3, kh = wid >> 2;
  u32x4 fw0 = {}, fw1 = {}, fw2 = {}, fw3 = {}, fw4 = {}, fw5 = {};
  const char* fb0 = isd1 ? (const char*)wg + wid * (64 * F * 2) + d1 * 32 : (const char*)wd + wid * (32 * D * 2) + sub * 32;
  const char* fb2 = isd1 ? (const char*)wu + wid * (64 * F * 2) + d1 * 32 : fb0 + 2 * (256 * D * 2);
  const unsigned fst = isd1 ? 32 * F * 2 : 256 * D * 2;
  const unsigned fs4 = isd1 ? 0u : 2 * fst, fs5 = (isd1 || wid >= 3) ? 0u : 3 * fst;
  unsigned fvo = (lane >> 1) * (isd1 ? F * 2 : D * 2) + (lane & 1) * 16;
  const int fit = (isd1 || isd2) ? 6 : -1;
  f32x16 oa = {}, ob = {};
  int sidx = (int)0xDDDDDDDDu;
  bf16x8 qa, qb2;
  float mref;
  {
    unsigned ka = kh * 32 * KVT + lane * 16;
    unsigned va0 = kh * 32 * KVT + (r32 < 16 ? 1024 + (hh * 16 + r32) * 16 : (r32 == 16 ? 2048 : 2064));
    unsigned va1 = va0 + (r32 < 16 ? 512 : 0);
    unsigned qad = L_STG + qg * 2048 + kh * 1024 + lane * 16;
    PIN(ka); PIN(va0); PIN(va1); PIN(qad); PIN(fvo); PIN(oa); PIN(ob); PIN(sidx);
    PIN(fw0); PIN(fw1); PIN(fw2); PIN(fw3); PIN(fw4); PIN(fw5);
    __builtin_amdgcn_s_setprio(2);
    wait_rdy(1u);
    STAMP(6);
    bf16x8 k0 = lds_ld128(ka), k1;
    qa = lds_ld128(qad); qb2 = lds_ld128(qad ^ 1024);
    bf16x8 v00 = lds_ld128(va0), v01 = lds_ld128(va1), v10, v11 = lds_ld128(va0);
    f32x16 ng, sA, sB;
    {
      const f32x16 z = {};
      sA = MFMA32(k0, qa, z); sB = MFMA32(k0, qb2, z);
      mref = max_halves(fmaxf(tile_max(sA), tile_max(sB)));
    }
#pragma unroll
    for (int j = 0; j < 16; ++j) { ng[j] = -mref; sA[j] -= mref; if (j >= 8) sB[j] = 0.f; }
    PIN(ng); PIN(sA);
    for (int it = 0; it < 8; ++it) {
      FSTAMP(32 + 2 * it);
      { unsigned tv_;
        asm volatile("s_cmp_lg_u32 %[it], %[fit]\n\ts_cbranch_scc1 .Lff%=\n\t"
          "global_load_dwordx4 %[a0], %[vo], %[b0]\n\tglobal_load_dwordx4 %[a2], %[vo], %[b2]\n\tv_add_u32 %[tv], %[st], %[vo]\n\t"
          "global_load_dwordx4 %[a1], %[tv], %[b0]\n\tglobal_load_dwordx4 %[a3], %[tv], %[b2]\n\tv_add_u32 %[tv], %[s4], %[vo]\n\t"
          "global_load_dwordx4 %[a4], %[tv], %[b2]\n\tv_add_u32 %[tv], %[s5], %[vo]\n\t"
          "global_load_dwordx4 %[a5], %[tv], %[b2]\n.Lff%=:\n\t"
          "s_cmp_lt_u32 %[it], 5\n\ts_cbranch_scc1 .Lpr%=\n\ts_setprio 1\n\ts_cmp_lt_u32 %[it], 7\n\ts_cbranch_scc1 .Lpr%=\n\ts_setprio 0\n.Lpr%=:"
          : [a0] "+v"(fw0), [a1] "+v"(fw1), [a2] "+v"(fw2), [a3] "+v"(fw3), [a4] "+v"(fw4), [a5] "+v"(fw5), [tv] "=&v"(tv_)
          : [it] "s"(it), [fit] "s"(fit), [vo] "v"(fvo), [b0] "s"(fb0), [b2] "s"(fb2), [st] "s"(fst), [s4] "s"(fs4), [s5] "s"(fs5) : "memory", "scc"); }
      SBAR();
      TWO_TILES(0);
      PIN(sA); PIN(sB);
      const unsigned need = (0x8042u >> (4 * it)) & 15u;
      if (need) {
        wait_rdy(need);
        if (it == 0 && fresh_lane() == 0) __hip_atomic_fetch_add((LAS unsigned*)(uintptr_t)(L_RDY + 16 + qg * 4), 1u, __ATOMIC_RELAXED, __HIP_MEMORY_SCOPE_WORKGROUP);
      }
      SBAR();
      TWO_TILES(2);
      ka += 4 * KVT; va0 += 4 * KVT; va1 += 4 * KVT;
    }
    ob = SMF(v11, hi8(sB), ob);
  }
  asm volatile("s_waitcnt vmcnt(0)" : "+v"(fw0), "+v"(fw1), "+v"(fw2), "+v"(fw3), "+v"(fw4), "+v"(fw5) :: "memory");
  {
  RELANE();
  const unsigned priv = L_PRIV + wid * PRIV;
  float ma = mref, mb = mref;
  float la = sum_halves(oa[8]), lb = sum_halves(ob[8]);
  {
    float t = 0.f;
#pragma unroll
    for (int j = 0; j < 8; ++j) t += fabsf(oa[j]) + fabsf(ob[j]);
    const bool ok = (la <= 1.0e18f) && (lb <= 1.0e18f) && (la >= 1.0e-18f) && (lb >= 1.0e-18f) && (t <= 3.0e38f);
    if (__builtin_expect(!__all(ok), 0)) {
      const unsigned kaddr = kh * 32 * KVT + lane * 16;
      const unsigned vaddr0 = kh * 32 * KVT + (r32 < 16 ? 1024 + (hh * 16 + r32) * 16 : (r32 == 16 ? 2048 : 2064));
      const unsigned vaddr1 = vaddr0 + (r32 < 16 ? 512 : 0);
      const f32x16 z = {};
      ma = -INFINITY; mb = -INFINITY;
      for (int t2 = 0; t2 < 32; ++t2) {
        const bf16x8 kf = lds_ld128(kaddr + t2 * KVT);
        ma = fmaxf(ma, tile_max(MFMA32(kf, qa, z)));
        mb = fmaxf(mb, tile_max(MFMA32(kf, qb2, z)));
      }
      ma = max_halves(ma); mb = max_halves(mb);
      oa = z; ob = z;
      for (int t2 = 0; t2 < 32; ++t2) {
        const bf16x8 kf = lds_ld128(kaddr + t2 * KVT), v0 = lds_ld128(vaddr0 + t2 * KVT), v1 = lds_ld128(vaddr1 + t2 * KVT);
        f32x16 sa = MFMA32(kf, qa, z), sb = MFMA32(kf, qb2, z);
#pragma unroll
        for (int j = 0; j < 16; ++j) { sa[j] = EX(sa[j] - ma); sb[j] = EX(sb[j] - mb); }
        oa = SMF(v0, lo8(sa), oa); oa = SMF(v1, hi8(sa), oa);
        ob = SMF(v0, lo8(sb), ob); ob = SMF(v1, hi8(sb), ob);
      }
      la = sum_halves(oa[8]); lb = sum_halves(ob[8]);
    }
  }
  STAMP(7);
  const int row = tid >> 4, c8 = tid & 15;
  const int goff = ((tt * 32 + row) * D + cbk * 128 + c8 * 8) * 2;
  const int po_probe = HO_O + (tt * H + wid * 4 + (lane & 3)) * 1024 + 1020;
  unsigned probe;
  {
    lds_stf4(priv + lane * 16, f32x4{ob[0], ob[1], ob[2], ob[3]}); lds_stf4(priv + 1024 + lane * 16, f32x4{ob[4], ob[5], ob[6], ob[7]});
    *(LAS f32x2*)(uintptr_t)(priv + 2048 + lane * 8) = f32x2{lb, mb};
    BARRIER();
    probe = __builtin_amdgcn_raw_buffer_load_b32(rs, po_probe, 0, 16);
    SBAR();
    const unsigned yp = L_PRIV + (wid ^ 4) * PRIV;
    const f32x2 y2 = *(const LAS f32x2*)(uintptr_t)(yp + 2048 + lane * 8);
    const f32x4 y0 = lds_ldf4(yp + lane * 16), y1 = lds_ldf4(yp + 1024 + lane * 16);
    const float M = fmaxf(ma, y2[1]);
    float f1 = EX(ma - M), f2 = EX(y2[1] - M);
    const float inv = RCP(la * f1 + y2[0] * f2);
    f1 *= inv; f2 *= inv;
    u32x4 po;
    po[0] = san(pack_bf16(oa[0] * f1 + y0[0] * f2, oa[1] * f1 + y0[1] * f2));
    po[1] = san(pack_bf16(oa[2] * f1 + y0[2] * f2, oa[3] * f1 + y0[3] * f2));
    po[2] = san(pack_bf16(oa[4] * f1 + y1[0] * f2, oa[5] * f1 + y1[1] * f2));
    po[3] = san(pack_bf16(oa[6] * f1 + y1[2] * f2, oa[7] * f1 + y1[3] * f2));
    ho_st(rs, HO_O + ((qb * 8 + qg * 2 + kh) * H + head) * 1024 + lane * 16, po);
  }
  STAMP(8);

  {
    const int oo = HO_O + (tt * H + wid * 4) * 1024 + lane * 16;
    for (unsigned spin = 0;; ++spin) {
      if (__builtin_amdgcn_ballot_w64(probe == SENT) == 0 || spin > SPIN_CAP) break;
      POLL_PAUSE(1);
      probe = __builtin_amdgcn_raw_buffer_load_b32(rs, po_probe, 0, 16);
    }
    u32x4 o0 = ho_ld(rs, oo), o1 = ho_ld(rs, oo + 1024), o2 = ho_ld(rs, oo + 2048), o3 = ho_ld(rs, oo + 3072);
    for (unsigned spin = 0;; ++spin) {
      const unsigned long long p0 = __builtin_amdgcn_ballot_w64(pend(o0)), p1 = __builtin_amdgcn_ballot_w64(pend(o1));
      const unsigned long long p2 = __builtin_amdgcn_ballot_w64(pend(o2)), p3 = __builtin_amdgcn_ballot_w64(pend(o3));
      if ((p0 | p1 | p2 | p3) == 0 || spin > SPIN_CAP) break;
      POLL_PAUSE(1);
      if (p0) o0 = ho_ld(rs, oo);
      if (p1) o1 = ho_ld(rs, oo + 1024);
      if (p2) o2 = ho_ld(rs, oo + 2048);
      if (p3) o3 = ho_ld(rs, oo + 3072);
    }
    const unsigned oa2 = wid * 4096 + lane * 16;
    lds_stu4(oa2, o0); lds_stu4(oa2 + 1024, o1); lds_stu4(oa2 + 2048, o2); lds_stu4(oa2 + 3072, o3);
    WAIT_LGKM0();
    if (lane == 0) __hip_atomic_fetch_or((LAS unsigned*)(uintptr_t)(L_RDY + 32), 1u << wid, __ATOMIC_RELAXED, __HIP_MEMORY_SCOPE_WORKGROUP);
  }
  STAMP(9);
  if (isd1) {
    const unsigned fa = F_OFF + wid * 2048 + lane * 16;
    lds_stu4(fa, fw0); lds_stu4(fa + 1024, fw1); lds_stu4(fa + 16384, fw2); lds_stu4(fa + 17408, fw3);
  } else if (isd2) {
    const unsigned fa = F_OFF + wid * 1024 + lane * 16;
    lds_stu4(fa, fw0); lds_stu4(fa + 8192, fw1); lds_stu4(fa + 16384, fw2); lds_stu4(fa + 24576, fw3); lds_stu4(fa + 32768, fw4); lds_stu4(fa + 40960, fw5);
  }
  WAIT_LGKM0();
  bf16x8 ff[6];
  {
    const unsigned ft = (8 * g + (i16 >> 2)) * 32 + (i16 & 3) * 8;
    const unsigned fl = F_OFF + wid * (isd1 ? 2048 : 1024) + ft, s1 = isd1 ? 1024 : 8192;
    ff[0] = cat8(tr_read(fl), tr_read(fl + 128)); ff[1] = cat8(tr_read(fl + s1), tr_read(fl + s1 + 128));
    ff[2] = cat8(tr_read(fl + 16384), tr_read(fl + 16384 + 128)); ff[3] = cat8(tr_read(fl + 16384 + s1), tr_read(fl + 16384 + s1 + 128));
    ff[4] = cat8(tr_read(fl + 32768), tr_read(fl + 32768 + 128)); ff[5] = cat8(tr_read(fl + 40960), tr_read(fl + 40960 + 128));
  }
  if (wid < 6) {
    const int rec = ((cbk + 1 + (wid >> 1)) & 3) * 4 + (wid & 1) * 2 + hh;
    u32x4 sv[1];
    wait_word(L_RDY + 40, 4u);
    __builtin_amdgcn_s_sleep(DLY_ST);
    poll_all<1>(rs, HO_ST + (tt * 16 + rec) * 512 + r32 * 16, 0, sv);
    lds_stu4(S_OFF + rec * 528 + r32 * 16, sv[0]);
    STAMP(10);
  }
  BARRIER();
  STAMP(11);
  const u32x4 hv = lds_ldu4(X_OFF + row * 272 + c8 * 16);
  u32x4 ov;
  {
    const f32x2 pr = *(const LAS f32x2*)(uintptr_t)(S_OFF + c8 * 528 + row * 16);
    f32x2 st;
    { float mbv, rden; ln_stats(row16_sum(pr[0]), row16_sum(pr[1]), mbv, rden); st = f32x2{mbv, rden}; }
    u32x4 lv;
#pragma unroll
    for (int j = 0; j < 4; ++j) {
      const float h0 = bf_lo(hv[j]), h1 = bf_hi(hv[j]);
      const unsigned d = pack_bf16(h0 - st[0], h1 - st[0]);
      lv[j] = pack_bf16(bf_lo(d) * st[1], bf_hi(d) * st[1]);
      ov[j] = pack_bf16(h0 + bf_lo(lv[j]), h1 + bf_hi(lv[j]));
    }
    lv = u32x4{san(lv[0]), san(lv[1]), san(lv[2]), san(lv[3])};
    PIN(lv);
    BARRIER();
    ho_st(rs, HO_LN + goff, lv);
  }
  BARRIER(); BARRIER(); BARRIER();
  STAMP(13);
  const unsigned long long tsel = __builtin_amdgcn_s_memtime();
  const unsigned mykey = lds_ldu(T_OFF + 640 + (lane & 7) * 4);
  const int myrow = 2047 - (int)(mykey & 0x7ffu);
  u32x4 ax[2];
  const int axo = HO_LN + (myrow * D + wid * 64 + g * 8) * 2;
  if (isd1) { ax[0] = ho_ld(rs, axo); ax[1] = ho_ld(rs, axo + 64); }
  SBAR();
  int sel = -1;
  {
    const int grow = tt * 32 + row;
#pragma unroll
    for (int c = 0; c < 8; ++c) if (__builtin_amdgcn_readlane(myrow, c) == grow) sel = c;
    if (sel >= 0) ho_st(rs, HO_HS + (sel * D + cbk * 128 + c8 * 8) * 2, u32x4{san(hv[0]), san(hv[1]), san(hv[2]), san(hv[3])});
  }
  if (isd1) {
    if (__builtin_amdgcn_ballot_w64(max(max4(ax[0]), max4(ax[1])) == SENT) != 0) poll_all<2>(rs, axo, 64, ax);
    f32x4 ag = {}, au = {};
    ag = MFMA16(BC8(ax[0]), ff[0], ag); au = MFMA16(BC8(ax[0]), ff[2], au);
    ag = MFMA16(BC8(ax[1]), ff[1], ag); au = MFMA16(BC8(ax[1]), ff[3], au);
    lds_stf4(R_OFF + wid * 1024 + lane * 16, hh ? au : ag);
    BARRIER();
    if (wid == 0) {
      f32x4 sum = lds_ldf4(R_OFF + lane * 16);
#pragma unroll
      for (int w2 = 1; w2 < 8; ++w2) sum += lds_ldf4(R_OFF + w2 * 1024 + lane * 16);
      const auto x0 = __builtin_amdgcn_permlane32_swap(__float_as_uint(sum[0]), __float_as_uint(sum[2]), false, false);
      const auto x1 = __builtin_amdgcn_permlane32_swap(__float_as_uint(sum[1]), __float_as_uint(sum[3]), false, false);
      const float g0 = rbf(__uint_as_float(x0[0])), u0 = rbf(__uint_as_float(x0[1])), g1 = rbf(__uint_as_float(x1[0])), u1 = rbf(__uint_as_float(x1[1]));
      const float a0f = rbf(g0 * sigmoid(g0)) * u0, a1f = rbf(g1 * sigmoid(g1)) * u1;
      const float n0 = dpp_f<DPP_X1>(a0f), n1 = dpp_f<DPP_X1>(a1f);
      const bool odd = lane & 1;
      const int tok = 4 * (g & 1) + 2 * hh + (odd ? 1 : 0);
      ho_st1(rs, HO_ACT + d1 * 256 + tok * 32 + (i16 >> 1) * 4, san(odd ? pack_bf16(n1, a1f) : pack_bf16(a0f, n0)));
    }
    STAMP(14);
  }
  if (sel < 0) __builtin_amdgcn_raw_buffer_store_b128(ov, __builtin_amdgcn_make_buffer_rsrc(out, 0, S * D * 2, 0x00020000), goff, 0, 16);
  if (b == 0 && tid == 0) __builtin_amdgcn_raw_buffer_store_b32(tag, rs, WS_EP, 0, 16);
  if (isd2) {
    const int n0 = sub * 16;
    unsigned hw = SENT, lw = SENT, fkey = 0;
    const bool odd = lane & 1;
    const int tok = 4 * (g & 1) + 2 * hh + (odd ? 1 : 0), cp = i16 >> 1;
    float sig = 0.f;
    fkey = lds_ldu(T_OFF + 640 + tok * 4);
    const int frow = 2047 - (int)(fkey & 0x7ffu);
    sig = rbf(sigmoid(key_score(fkey)));
    PIN(sig);
    u32x4 av[6];
    const int ao = HO_ACT + ((wid * 2 + (g >> 1)) * 8 + (lane & 7)) * 32 + (g & 1) * 16;
    if (wid == 0) {
      while (__builtin_amdgcn_s_memtime() - tsel < DLY_ACT / 2) __builtin_amdgcn_s_sleep(1);
      for (unsigned spin = 0;; ++spin) {
        hw = __builtin_amdgcn_raw_buffer_load_b32(rs, HO_HS + (tok * D + n0 + cp * 2) * 2, 0, 16);
        lw = __builtin_amdgcn_raw_buffer_load_b32(rs, HO_LN + (frow * D + n0 + cp * 2) * 2, 0, 16);
        if (__builtin_amdgcn_ballot_w64(max(hw, lw) == SENT) == 0 || spin > SPIN_CAP) break;
        POLL_PAUSE(1);
      }
    }
    while (__builtin_amdgcn_s_memtime() - tsel < DLY_ACT) __builtin_amdgcn_s_sleep(1);
    STAMP(12);
    poll_all<6>(rs, ao, 4096, av, wid < 3 ? 0 : 4096);
    STAMP(5);
    f32x4 y = {}, y2 = {};
    y = MFMA16(BC8(av[0]), ff[0], y); y2 = MFMA16(BC8(av[1]), ff[1], y2);
    y = MFMA16(BC8(av[2]), ff[2], y); y2 = MFMA16(BC8(av[3]), ff[3], y2);
    y = MFMA16(BC8(av[4]), ff[4], y);
    if (wid < 3) y2 = MFMA16(BC8(av[5]), ff[5], y2);
    lds_stf4(R_OFF + wid * 1024 + lane * 16, hh ? y2 : y);
    BARRIER();
    if (wid == 0) {
      f32x4 sum = lds_ldf4(R_OFF + lane * 16);
#pragma unroll
      for (int w2 = 1; w2 < 8; ++w2) sum += lds_ldf4(R_OFF + w2 * 1024 + lane * 16);
      const auto x0 = __builtin_amdgcn_permlane32_swap(__float_as_uint(sum[0]), __float_as_uint(sum[2]), false, false);
      const auto x1 = __builtin_amdgcn_permlane32_swap(__float_as_uint(sum[1]), __float_as_uint(sum[3]), false, false);
      const float t0 = __uint_as_float(x0[0]) + __uint_as_float(x0[1]), t1 = __uint_as_float(x1[0]) + __uint_as_float(x1[1]);
      const float n0f = dpp_f<DPP_X1>(t0), n1f = dpp_f<DPP_X1>(t1);
      const float ylo = rbf(rbf(odd ? n1f : t0) * sig), yhi = rbf(rbf(odd ? t1 : n0f) * sig);
      *(unsigned*)((char*)out + ((size_t)(2047 - (int)(fkey & 0x7ffu)) * D + n0 + cp * 2) * 2) =
          pack_bf16(bf_lo(hw) + rbf(bf_lo(lw) + ylo), bf_hi(hw) + rbf(bf_hi(lw) + yhi));
    }
  }
  }
  STAMP(15); STAMP_OUT(0);
}

extern "C" void kernel_launch(void* const* d_in, const int* in_sizes, int n_in, void* d_out, int out_size, void* d_ws, size_t ws_size,
                              hipStream_t stream) {
  (void)hipFuncSetAttribute((const void*)k_fused, hipFuncAttributeMaxDynamicSharedMemorySize, LDS_BYTES);
  k_fused<<<256, 768, LDS_BYTES, stream>>>((const uint16_t*)d_in[0], (const uint16_t*)d_in[1], (const uint16_t*)d_in[2], (const uint16_t*)d_in[4],
                                           (const uint16_t*)d_in[5], (const uint16_t*)d_in[6], (const uint16_t*)d_in[7], (char*)d_ws, (uint16_t*)d_out);
}
```

```cpp
#include <hip/hip_runtime.h>
#include <hip/hip_bf16.h>
#include <stdint.h>

typedef short bf16x8 __attribute__((ext_vector_type(8)));
typedef short bf16x4 __attribute__((ext_vector_type(4)));
typedef float f32x16 __attribute__((ext_vector_type(16)));
typedef float f32x8 __attribute__((ext_vector_type(8)));
typedef float f32x4 __attribute__((ext_vector_type(4)));
typedef __bf16 b16x8 __attribute__((ext_vector_type(8)));
typedef __bf16 b16x16 __attribute__((ext_vector_type(16)));
typedef float f32x2 __attribute__((ext_vector_type(2)));
typedef __bf16 bf16x2_t __attribute__((ext_vector_type(2)));
typedef unsigned u32x4 __attribute__((ext_vector_type(4)));
typedef unsigned u32x2 __attribute__((ext_vector_type(2)));
#define LAS __attribute__((address_space(3)))
#define GAS __attribute__((address_space(1)))
#define DI __device__ __forceinline__
#define MFMA32(a, b, c) __builtin_amdgcn_mfma_f32_32x32x16_bf16((a), (b), (c), 0, 0, 0)
#define MFMA16(a, b, c) __builtin_amdgcn_mfma_f32_16x16x32_bf16((a), (b), (c), 0, 0, 0)
#define SBAR() __builtin_amdgcn_sched_barrier(0)
#define PIN(x) asm volatile("" : "+v"(x))
#define BC8(x) __builtin_bit_cast(bf16x8, x)

constexpr int S = 2048, D = 512, H = 32, F = 1376;
constexpr float LN_EPS = 1e-5f;
constexpr float QSCALE = 0.25f * 1.4426950408889634f;
constexpr unsigned SENT = 0xFFFFFFFFu;
constexpr unsigned SPIN_CAP = 1u << 16;

constexpr int WS_Q = 0;
constexpr int WS_KV = WS_Q + H * 64 * 1024;
constexpr int WS_FA = WS_KV + H * 64 * 2048;
constexpr int WS_FQ = WS_FA + H * 64 * 128;
constexpr int WS_FR = WS_FQ + H * 64 * 128;
constexpr int WS_EP = WS_FR + 4096;
constexpr int WS_HO = WS_EP + 4096;
constexpr int HO_O = WS_HO;
constexpr int HO_LN = HO_O + 64 * H * 1024;
constexpr int HO_ST = HO_LN + S * D * 2;
constexpr int HO_KEY = HO_ST + 64 * 16 * 512;
constexpr int HO_ACT = HO_KEY + 8 * S * 4;
constexpr int HO_HS = HO_ACT + 24576;
constexpr int HO_END = HO_HS + 8192;
constexpr int HO_PER_WG = 19 * 1024;
static_assert(WS_HO + 256 * HO_PER_WG >= HO_END, "reset covers the hand-off words");
constexpr int WS_BYTES = WS_HO + 256 * HO_PER_WG;

#define STAMP_DECL do {} while (0)
#define STAMP(i) do {} while (0)
#define STAMP_OUT(k) do {} while (0)

#define FSTAMP(i) do {} while (0)

DI unsigned pack_bf16(float lo, float hi) {
  f32x2 v = {lo, hi};
  bf16x2_t r = __builtin_convertvector(v, bf16x2_t);
  return __builtin_bit_cast(unsigned, r);
}
DI float bf_lo(unsigned v) { return __uint_as_float(v << 16); }
DI float bf_hi(unsigned v) { return __uint_as_float(v & 0xffff0000u); }
DI float rbf(float x) { return bf_lo(pack_bf16(x, x)); }
DI float sum_halves(float v) {
  auto r = __builtin_amdgcn_permlane32_swap(__float_as_uint(v), __float_as_uint(v), false, false);
  return __uint_as_float(r[0]) + __uint_as_float(r[1]);
}
DI float sum_rows(float v) {
  auto r = __builtin_amdgcn_permlane16_swap(__float_as_uint(v), __float_as_uint(v), false, false);
  return sum_halves(__uint_as_float(r[0]) + __uint_as_float(r[1]));
}
DI float max_halves(float v) {
  auto r = __builtin_amdgcn_permlane32_swap(__float_as_uint(v), __float_as_uint(v), false, false);
  return fmaxf(__uint_as_float(r[0]), __uint_as_float(r[1]));
}
template <int CTRL> DI float dpp_f(float v) { return __uint_as_float(__builtin_amdgcn_update_dpp(0u, __float_as_uint(v), CTRL, 0xf, 0xf, true)); }
template <int CTRL> DI unsigned dpp_u(unsigned v) { return __builtin_amdgcn_update_dpp(0u, v, CTRL, 0xf, 0xf, true); }
constexpr int DPP_X1 = 0xB1, DPP_X2 = 0x4E, DPP_HMIR = 0x141, DPP_MIR = 0x140;
DI float quad_sum(float v) { v += dpp_f<DPP_X1>(v); v += dpp_f<DPP_X2>(v); return v; }
DI float row16_sum(float v) { v = quad_sum(v); v += dpp_f<DPP_HMIR>(v); v += dpp_f<DPP_MIR>(v); return v; }
DI void glds16(const void* sbase, unsigned voff, unsigned lds_base) {
  unsigned sv;
  asm volatile("s_mov_b32 %0, m0\n\ts_mov_b32 m0, %3\n\ts_nop 0\n\tglobal_load_lds_dwordx4 %1, %2\n\ts_mov_b32 m0, %0"
               : "=&s"(sv) : "v"(voff), "s"(sbase), "s"(lds_base) : "memory");
}
DI void glds16s(const void* sbase, unsigned voff, unsigned lds_base) {
  unsigned sv;
  asm volatile("s_mov_b32 %0, m0\n\ts_mov_b32 m0, %3\n\ts_nop 0\n\tglobal_load_lds_dwordx4 %1, %2 sc1\n\ts_mov_b32 m0, %0"
               : "=&s"(sv) : "v"(voff), "s"(sbase), "s"(lds_base) : "memory");
}
DI void glds4(const void* sbase, unsigned voff, unsigned lds_base) {
  unsigned sv;
  asm volatile("s_mov_b32 %0, m0\n\ts_mov_b32 m0, %3\n\ts_nop 0\n\tglobal_load_lds_dword %1, %2\n\ts_mov_b32 m0, %0"
               : "=&s"(sv) : "v"(voff), "s"(sbase), "s"(lds_base) : "memory");
}
#define WAIT_VM(N) asm volatile("s_waitcnt vmcnt(" #N ")" ::: "memory")
#define WAIT_LGKM0() asm volatile("s_waitcnt lgkmcnt(0)" ::: "memory")
#define BARRIER() asm volatile("s_waitcnt lgkmcnt(0)\n\ts_barrier" ::: "memory")
typedef short v4i16_t __attribute__((ext_vector_type(4)));
DI bf16x4 tr_read(unsigned addr) {
  return __builtin_bit_cast(bf16x4, __builtin_amdgcn_ds_read_tr16_b64_v4i16((LAS v4i16_t*)(uintptr_t)addr));
}
DI bf16x8 lds_ld128(unsigned addr) { return *(const LAS bf16x8*)(uintptr_t)addr; }
DI f32x4 lds_ldf4(unsigned addr) { return *(const LAS f32x4*)(uintptr_t)addr; }
DI u32x4 lds_ldu4(unsigned addr) { return *(const LAS u32x4*)(uintptr_t)addr; }
DI void lds_stf4(unsigned addr, f32x4 v) { *(LAS f32x4*)(uintptr_t)addr = v; }
DI void lds_stu4(unsigned addr, u32x4 v) { *(LAS u32x4*)(uintptr_t)addr = v; }
DI void lds_stf(unsigned addr, float v) { *(LAS float*)(uintptr_t)addr = v; }
DI void lds_stu(unsigned addr, unsigned v) { *(LAS unsigned*)(uintptr_t)addr = v; }
DI float lds_ldf(unsigned addr) { return *(const LAS float*)(uintptr_t)addr; }
DI unsigned lds_ldu(unsigned addr) { return *(const LAS unsigned*)(uintptr_t)addr; }
DI bf16x8 cat8(bf16x4 a, bf16x4 b) { return __builtin_shufflevector(a, b, 0, 1, 2, 3, 4, 5, 6, 7); }
typedef __amdgpu_buffer_rsrc_t rsrc_t;
DI u32x4 ho_ld(rsrc_t rs, int off) { return __builtin_amdgcn_raw_buffer_load_b128(rs, off, 0, 16); }
DI void ho_st(rsrc_t rs, int off, u32x4 v) { __builtin_amdgcn_raw_buffer_store_b128(v, rs, off, 0, 16); }
DI void ho_st1(rsrc_t rs, int off, unsigned v) { __builtin_amdgcn_raw_buffer_store_b32(v, rs, off, 0, 16); }
DI unsigned san(unsigned v) { return min(v, SENT - 1u); }
#define POLL_PAUSE(n) do { __builtin_amdgcn_s_sleep(n); asm volatile("" ::: "memory"); } while (0)
DI bool pend(u32x4 v) { return max(max(v[0], v[1]), max(v[2], v[3])) == SENT; }

constexpr int LDS_BYTES = 163840;
constexpr int KVT = 2080;
constexpr int L_PRIV = 64 * KVT, PRIV = 2560;
constexpr int L_STG = L_PRIV + 8 * PRIV;
constexpr int L_RDY = L_STG + 4 * 2048;
constexpr int L_RS = L_RDY + 64;
static_assert(L_RS + 1024 <= LDS_BYTES, "lds map");
constexpr unsigned X_OFF = 32768, S_OFF = 45056, T_OFF = 55296, R_OFF = 57344, F_OFF = 81920;
#define DLY_FLAG 14
#define DLY_O 0
#define DLY_ST 3
#define DLY_KEY 13
#define DLY_ACT 2800
constexpr int N_D1 = 86;
constexpr int CODE_PF_BYTES = 25 * 1024;
struct AFrag { bf16x8 x0, x1, q, k, v; };

#define EX(x) __builtin_amdgcn_exp2f(x)
#define RCP(x) __builtin_amdgcn_rcpf(x)
#define SMF(a, b, c) __builtin_amdgcn_smfmac_f32_32x32x32_bf16(__builtin_bit_cast(b16x8, a), __builtin_bit_cast(b16x16, b), (c), sidx, 0, 0)
DI f32x8 lo8(const f32x16& s) { return __builtin_shufflevector(s, s, 0, 1, 2, 3, 4, 5, 6, 7); }
DI f32x8 hi8(const f32x16& s) { return __builtin_shufflevector(s, s, 8, 9, 10, 11, 12, 13, 14, 15); }
#define UNIT(SC, SN, KN, QN, NEGN, OPREV, VPREV, OCUR, VCUR0, LOADS) do { \
    SC[0] = EX(SC[0]); SC[1] = EX(SC[1]); SBAR(); \
    OPREV = SMF(VPREV, hi8(SN), OPREV); \
    LOADS; \
    SBAR(); \
    SC[2] = EX(SC[2]); SC[3] = EX(SC[3]); SC[4] = EX(SC[4]); SC[5] = EX(SC[5]); SBAR(); \
    SN = MFMA32(KN, QN, NEGN); SBAR(); \
    SC[6] = EX(SC[6]); SC[7] = EX(SC[7]); SC[8] = EX(SC[8]); SC[9] = EX(SC[9]); SC[10] = EX(SC[10]); SBAR(); \
    OCUR = SMF(VCUR0, lo8(SC), OCUR); SBAR(); \
    SC[11] = EX(SC[11]); SC[12] = EX(SC[12]); SC[13] = EX(SC[13]); SC[14] = EX(SC[14]); SC[15] = EX(SC[15]); SBAR(); \
  } while (0)
#define TWO_TILES(U) do { \
    UNIT(sA, sB, k0, qb2, ng, ob, v11, oa, v00, k1 = lds_ld128(ka + (U + 1) * KVT); v10 = lds_ld128(va0 + (U + 1) * KVT)); \
    UNIT(sB, sA, k1, qa, ng, oa, v01, ob, v00, v11 = lds_ld128(va1 + (U + 1) * KVT)); \
    UNIT(sA, sB, k1, qb2, ng, ob, v01, oa, v10, k0 = lds_ld128(ka + (U + 2) * KVT); v00 = lds_ld128(va0 + (U + 2) * KVT)); \
    UNIT(sB, sA, k0, qa, ng, oa, v11, ob, v10, v01 = lds_ld128(va1 + (U + 2) * KVT)); \
  } while (0)

DI int fresh_lane() {
  int l;
  asm volatile("v_mbcnt_lo_u32_b32 %0, -1, 0\n\tv_mbcnt_hi_u32_b32 %0, -1, %0" : "=v"(l));
  return l;
}
#define RELANE() const int lane = fresh_lane(), tid = wid * 64 + lane, r32 = lane & 31, hh = lane >> 5, g = lane >> 4, i16 = lane & 15; \
  (void)tid; (void)r32; (void)hh; (void)g; (void)i16
DI float tile_max(const f32x16& s) {
  float m = fmaxf(fmaxf(s[0], s[1]), s[2]);
#pragma unroll
  for (int j = 3; j < 15; j += 2) m = fmaxf(fmaxf(m, s[j]), s[j + 1]);
  return fmaxf(m, s[15]);
}
DI unsigned sort_key(float r, int idx) {
  unsigned u = pack_bf16(r, r) & 0xffffu;
  if ((u & 0x7fffu) > 0x7f80u) u = 0xffffu;
  u = (u & 0x8000u) ? (~u & 0xffffu) : (u | 0x8000u);
  return (u << 11) | (unsigned)(2047 - idx);
}
DI float key_score(unsigned key) {
  unsigned u = (key >> 11) & 0xffffu;
  u = (u & 0x8000u) ? (u & 0x7fffu) : (~u & 0xffffu);
  return __uint_as_float(u << 16);
}
DI void ln_stats(float s1, float s2, float& mb, float& rden) {
  const float mean = s1 * (1.f / D);
  const float var = fmaxf(s2 * (1.f / D) - mean * mean, 0.f);
  mb = rbf(mean);
  rden = RCP(rbf(__builtin_amdgcn_sqrtf(rbf(rbf(var) + LN_EPS))));
}
DI float sigmoid(float v) { return RCP(1.f + EX(v * -1.4426950408889634f)); }
DI void wait_rdy(unsigned need) {
  for (unsigned spin = 0;; ++spin) {
    const u32x4 r = *(volatile const LAS u32x4*)(uintptr_t)L_RDY;
    const unsigned m = min(min(r[0], r[1]), min(r[2], r[3]));
    if (__builtin_amdgcn_readfirstlane(m) >= need || spin > (SPIN_CAP << 4)) break;
    __builtin_amdgcn_s_sleep(1);
  }
  asm volatile("" ::: "memory");
}
DI void wait_word(unsigned addr, unsigned need) {
  for (unsigned spin = 0;; ++spin) {
    const unsigned r = *(volatile const LAS unsigned*)(uintptr_t)addr;
    if (__builtin_amdgcn_readfirstlane(r) >= need || spin > (SPIN_CAP << 4)) break;
    __builtin_amdgcn_s_sleep(1);
  }
  asm volatile("" ::: "memory");
}
DI unsigned gt_bit(unsigned m, unsigned a, unsigned c) { return __builtin_amdgcn_alignbit(m, c - a, 31); }
DI unsigned count_gt(u32x4 a, u32x4 b, unsigned c) {
  unsigned m = 0, n = 0;
#pragma unroll
  for (int j = 0; j < 4; ++j) { m = gt_bit(m, a[j], c); n = gt_bit(n, b[j], c); }
  return __builtin_popcount(m) + __builtin_popcount(n);
}
DI unsigned max4(u32x4 v) { return max(max(v[0], v[1]), max(v[2], v[3])); }
template <int N> DI void poll_all(rsrc_t rs, int off, int stride, u32x4 (&v)[N], int back = 0) {
  for (unsigned spin = 0;; ++spin) {
#pragma unroll
    for (int j = 0; j < N; ++j) v[j] = ho_ld(rs, off + j * stride - (j == N - 1 ? back : 0));
    unsigned m = 0;
#pragma unroll
    for (int j = 0; j < N; ++j) m = max(m, max4(v[j]));
    if (__builtin_amdgcn_ballot_w64(m == SENT) == 0 || spin > SPIN_CAP) break;
    POLL_PAUSE(1);
  }
}
DI unsigned sum8_u(unsigned v) { v += dpp_u<DPP_X1>(v); v += dpp_u<DPP_X2>(v); v += dpp_u<DPP_HMIR>(v); return v; }

__global__ __launch_bounds__(768, 3) void k_fused(const uint16_t* __restrict__ x, const uint16_t* __restrict__ wqkv, const uint16_t* __restrict__ wo,
                                                   const uint16_t* __restrict__ wr, const uint16_t* __restrict__ wg, const uint16_t* __restrict__ wu,
                                                   const uint16_t* __restrict__ wd, char* __restrict__ ws, uint16_t* __restrict__ out) {
  extern __shared__ __attribute__((aligned(16))) char smem[];
  asm volatile("" :: "s"((unsigned)(uintptr_t)smem));
  const int tid = threadIdx.x, lane = tid & 63, wid = __builtin_amdgcn_readfirstlane(tid >> 6);
  const int b = blockIdx.x, xcd = b & 7, sub = b >> 3;
  const int r32 = lane & 31, hh = lane >> 5, g = lane >> 4, i16 = lane & 15;
  const rsrc_t rs = __builtin_amdgcn_make_buffer_rsrc(ws, 0, WS_BYTES, 0x00020000);
  const int rb = (xcd >> 1) * 8 + (sub & 7), hq = (xcd & 1) * 4 + (sub >> 3);
  const int head = xcd * 4 + (sub & 3), qb = sub >> 2;
  const int tt = xcd * 8 + (sub >> 2), cbk = sub & 3;
  const int d1 = (xcd < 7 && sub < 13) ? xcd * 13 + sub : 999;
  const bool isd1 = d1 < N_D1, isd2 = xcd == 7;
  STAMP_DECL; STAMP(0);
  unsigned code_pf = 0;
  if (wid == 7) {
    const unsigned long long pc = __builtin_amdgcn_s_getpc() & ~0xFFull;
    const unsigned off = min((unsigned)((sub & 3) * 8192 + lane * 128), (unsigned)(CODE_PF_BYTES - 128));
    asm volatile("global_load_dword %0, %1, %2" : "=v"(code_pf) : "v"(off), "s"(pc) : "memory");
  }

  if (__builtin_expect(wid >= 8, 1)) {
    const int hw = wid - 8;
    __builtin_amdgcn_s_setprio(3);
    {
      const int rot = ((sub & 7) + 2 * (sub >> 3)) & 7;
      const char* base; unsigned vo0, vo1, cstr, pstr;
      if (hw < 3) {
        base = (const char*)wqkv + hw * 1024 + hq * 128; cstr = 64 * 3072; pstr = 8 * 3072;
        vo0 = (lane >> 3) * 3072 + (((lane & 7) ^ ((g & 1) << 1)) * 16); vo1 = (lane >> 3) * 3072 + (((lane & 7) ^ (((g & 1) | 2) << 1)) * 16);
      } else {
        base = (const char*)x + (size_t)rb * 65536; cstr = 128; pstr = 8192;
        vo0 = (lane >> 3) * 1024 + (((lane & 7) ^ g) * 16); vo1 = (lane >> 3) * 1024 + (((lane & 7) ^ (4 + g)) * 16);
      }
      auto issue = [&](int c) {
        const char* src = base + (size_t)((c + rot) & 7) * cstr;
        const unsigned dst = (c & 3) * 32768 + hw * 8192;
#pragma unroll
        for (int p = 0; p < 8; ++p) glds16(src + (size_t)p * pstr, (p & 1) ? vo1 : vo0, dst + p * 1024);
      };
#pragma unroll 1
      for (int c = -3; c < 7; ++c) {
        if (c >= -1) {
          if (c <= 5) { WAIT_VM(8); } else { WAIT_VM(0); }
          BARRIER();
        }
        if (c + 3 < 8) issue(c + 3);
      }
    }
    unsigned tag = __builtin_amdgcn_raw_buffer_load_b32(rs, WS_EP, 0, 16);
    tag = __builtin_amdgcn_readfirstlane(tag) + 1u;
    BARRIER();
    STAMP(4);
    if (lane < 32) {
      const unsigned o = (lane & 1) ? 0u : 0x3f803f80u;
      lds_stu4((hw * 16 + (lane >> 1)) * KVT + 2048 + (lane & 1) * 16, u32x4{o, o, o, o});
    }
    {
      const int fo = WS_FA + (head * 64 + ((lane >> 1) & 7) * 8 + hw * 2 + (lane & 1)) * 128;
      const int fo0 = ((lane & 2) ? WS_FQ + qb * 8 * 128 : WS_FA) + (head * 64 + hw * 2 + (lane & 1)) * 128;
      unsigned okm = 0;
      auto need = [&](unsigned bits) {
        for (unsigned spin = 0; (okm & bits) != bits && spin <= SPIN_CAP; ++spin) {
          const u32x2 f = __builtin_amdgcn_raw_buffer_load_b64(rs, fo, 0, 16);
          okm = (unsigned)__builtin_amdgcn_ballot_w64(f[0] == tag && f[1] == ~tag);
          if ((okm & bits) != bits) POLL_PAUSE(1);
        }
        asm volatile("" ::: "memory");
      };
      const char* kvg = ws + WS_KV + (size_t)head * (64 * 2048) + hw * 4096;
      const char* qsrc = ws + WS_Q + (size_t)(head * 64 + qb * 8 + hw * 2) * 1024;
      auto kv_step = [&](int i) {
        const char* src = kvg + i * 16384;
#pragma unroll
        for (int p = 0; p < 4; ++p) glds16s(src + p * 1024, lane * 16, (hw + i * 4 + (p >> 1) * 32) * KVT + (p & 1) * 1024);
      };
      wait_word(L_RDY + 36, 8u);
      __builtin_amdgcn_s_sleep(DLY_FLAG);
      for (unsigned spin = 0; spin <= SPIN_CAP; ++spin) {
        const u32x2 f = __builtin_amdgcn_raw_buffer_load_b64(rs, fo0, 0, 16);
        if (__builtin_amdgcn_ballot_w64(f[0] != tag || f[1] != ~tag) == 0) break;
        POLL_PAUSE(1);
      }
      asm volatile("" ::: "memory");
      STAMP(5);
      glds16s(qsrc, lane * 16, L_STG + hw * 2048); glds16s(qsrc + 1024, lane * 16, L_STG + hw * 2048 + 1024);
      kv_step(0);
      {
        const u32x2 f = __builtin_amdgcn_raw_buffer_load_b64(rs, fo, 0, 16);
        asm volatile("s_waitcnt vmcnt(1) lgkmcnt(0)" ::: "memory");
        lds_stu(L_RDY + hw * 4, 1u);
        okm = (unsigned)__builtin_amdgcn_ballot_w64(f[0] == tag && f[1] == ~tag);
      }
      need(0xCu);
      kv_step(1);
      if ((okm & 0xF0u) != 0xF0u) { WAIT_VM(0); lds_stu(L_RDY + hw * 4, 2u); need(0xF0u); }
      kv_step(2); kv_step(3);
      WAIT_VM(8);
      lds_stu(L_RDY + hw * 4, 2u);
      if ((okm & 0xFF00u) != 0xFF00u) { WAIT_VM(0); lds_stu(L_RDY + hw * 4, 4u); need(0xFF00u); }
      kv_step(4); kv_step(5);
      WAIT_VM(8);
      lds_stu(L_RDY + hw * 4, 4u);
      kv_step(6); kv_step(7);
      WAIT_VM(0);
      lds_stu(L_RDY + hw * 4, 8u);
    }
    STAMP(6);
    if (hw == 0) {
      const u32x4 sv = {SENT, SENT, SENT, SENT};
#pragma unroll 1
      for (int j = 0; j < 19; ++j) __builtin_amdgcn_raw_buffer_store_b128(sv, rs, lane * 16, WS_HO + b * HO_PER_WG + j * 1024, 16);
      WAIT_VM(0);
      if (lane == 0) __builtin_amdgcn_raw_buffer_store_b64(u32x2{tag, ~tag}, rs, WS_FR + b * 8, 0, 16);
    }
    bf16x8 wf[32];
    float swr = 0.f;
    f32x16 acc;
    {
      const char* wsrc = (const char*)wo + (lane >> 2) * 1024 + cbk * 256 + hw * 64 + (lane & 3) * 16;
#pragma unroll
      for (int i = 0; i < 32; ++i) wf[i] = *(const bf16x8*)(wsrc + i * 16384);
      const unsigned stg = L_STG + hw * 2048;
      const unsigned wor = stg + (4 * (g >> 1) + (i16 >> 2)) * 64 + (g & 1) * 32 + (i16 & 3) * 8;
      wait_word(L_RDY + 16 + hw * 4, 2u);
#pragma unroll
      for (int i = 0; i < 32; ++i) {
        *(LAS bf16x8*)(uintptr_t)(stg + (i & 1) * 1024 + lane * 16) = wf[i];
        WAIT_LGKM0();
        wf[i] = cat8(tr_read(wor + (i & 1) * 1024), tr_read(wor + (i & 1) * 1024 + 512));
      }
      WAIT_LGKM0();
      const char* xs = (const char*)x + ((tt * 32 + r32) * D + cbk * 128 + hw * 32 + 4 * hh) * 2;
#pragma unroll
      for (int gq = 0; gq < 4; ++gq) {
        const u32x2 xw = *(const u32x2*)(xs + gq * 16);
        acc[4 * gq] = bf_lo(xw[0]); acc[4 * gq + 1] = bf_hi(xw[0]); acc[4 * gq + 2] = bf_lo(xw[1]); acc[4 * gq + 3] = bf_hi(xw[1]);
      }
      lds_stf(stg + r32 * 4, bf_lo(wr[cbk * 128 + hw * 32 + r32]));
      const u32x4 wra = *(const u32x4*)((const char*)wr + lane * 16);
#pragma unroll
      for (int j = 0; j < 4; ++j) swr += bf_lo(wra[j]) + bf_hi(wra[j]);
      swr = __uint_as_float(__builtin_amdgcn_readfirstlane(__float_as_uint(sum_rows(row16_sum(swr)))));
      PIN(acc);
      if (isd1) {
#pragma unroll 1
        for (int p = 0; p < 4; ++p) glds4(hw < 2 ? wg : wu, (((hw & 1) * 4 + p) * 64 + lane) * (F * 2) + d1 * 32, stg + 1024);
      } else if (isd2) {
#pragma unroll 1
        for (int p = hw; p < 22; p += 4) glds4(wd, min(p * 64 + lane, F - 1) * (D * 2) + sub * 32, stg + 1024);
      }
      WAIT_VM(0);
    }
    STAMP(7);
    if (hw == 0) {
      for (unsigned spin = 0;; ++spin) {
        const u32x4 f0 = ho_ld(rs, WS_FR + lane * 16), f1 = ho_ld(rs, WS_FR + 1024 + lane * 16);
        const bool okf = f0[0] == tag && f0[1] == ~tag && f0[2] == tag && f0[3] == ~tag && f1[0] == tag && f1[1] == ~tag && f1[2] == tag && f1[3] == ~tag;
        if (__all(okf) || spin > SPIN_CAP) break;
        POLL_PAUSE(8);
      }
    }
    STAMP(8);
    BARRIER();
    {
      RELANE();
      const unsigned ol = lane * 16;
      const int sto = HO_ST + (tt * 16 + cbk * 4 + hw) * 512 + r32 * 16;
      const unsigned ha = X_OFF + r32 * 272 + (hw * 32 + 4 * hh) * 2, wa = L_STG + hw * 2048 + 4 * hh * 4;
      f32x4 wv[4];
      auto wait_bit = [&](int bit) {
        for (unsigned spin = 0;; ++spin) {
          const unsigned m = __builtin_amdgcn_readfirstlane(*(volatile const LAS unsigned*)(uintptr_t)(L_RDY + 32));
          if (((m >> bit) & 1u) || spin > (SPIN_CAP << 4)) break;
          __builtin_amdgcn_s_sleep(1);
        }
        asm volatile("" ::: "memory");
      };
      bf16x8 of[4], nf[4];
      wait_bit(0);
#pragma unroll
      for (int i = 0; i < 4; ++i) of[i] = lds_ld128(ol + i * 1024);
#pragma unroll
      for (int w2 = 0; w2 < 8; ++w2) {
        unsigned mk = 0;
        if (w2 >= 1 && w2 < 7) {
          mk = *(volatile const LAS unsigned*)(uintptr_t)(L_RDY + 32);
          asm volatile("" ::: "memory");
#pragma unroll
          for (int i = 0; i < 4; ++i) nf[i] = lds_ld128(ol + (w2 * 4 + 4 + i) * 1024);
        }
        if (w2 == 6) {
#pragma unroll
          for (int gq = 0; gq < 4; ++gq) wv[gq] = lds_ldf4(wa + gq * 32);
        }
        SBAR();
#pragma unroll
        for (int i = 0; i < 4; ++i) acc = MFMA32(wf[w2 * 4 + i], of[i], acc);
        SBAR();
        if (w2 < 7 && !((__builtin_amdgcn_readfirstlane(mk) >> (w2 + 1)) & 1u)) {
          wait_bit(w2 + 1);
#pragma unroll
          for (int i = 0; i < 4; ++i) nf[i] = lds_ld128(ol + (w2 * 4 + 4 + i) * 1024);
        }
#pragma unroll
        for (int i = 0; i < 4; ++i) of[i] = nf[i];
      }
      STAMP(9);
      f32x2 t1 = {0.f, 0.f}, t2 = t1, t3 = t1;
#pragma unroll
      for (int j = 0; j < 8; ++j) {
        const f32x2 v = {acc[2 * j], acc[2 * j + 1]}, w = {wv[j >> 1][2 * (j & 1)], wv[j >> 1][2 * (j & 1) + 1]};
        t1 += v; t2 += v * v; t3 += v * w;
      }
      const float s1 = sum_halves(t1[0] + t1[1]), s2 = sum_halves(t2[0] + t2[1]), s3 = sum_halves(t3[0] + t3[1]);
      const u32x4 rec = {san(__float_as_uint(s1)), san(__float_as_uint(s2)), san(__float_as_uint(s3)), 0u};
      if (lane < 32) ho_st(rs, sto, rec);
      if (lane == 0) __hip_atomic_fetch_add((LAS unsigned*)(uintptr_t)(L_RDY + 40), 1u, __ATOMIC_RELAXED, __HIP_MEMORY_SCOPE_WORKGROUP);
      lds_stu4(S_OFF + (cbk * 4 + hw) * 528 + r32 * 16, rec);
#pragma unroll
      for (int gq = 0; gq < 4; ++gq)
        *(LAS u32x2*)(uintptr_t)(ha + gq * 16) = u32x2{pack_bf16(acc[4 * gq], acc[4 * gq + 1]), pack_bf16(acc[4 * gq + 2], acc[4 * gq + 3])};
    }
    BARRIER();
    if (hw == 0) {
      RELANE();
      f32x4 t = lds_ldf4(S_OFF + hh * 8 * 528 + r32 * 16);
#pragma unroll
      for (int j = 1; j < 8; ++j) t += lds_ldf4(S_OFF + (hh * 8 + j) * 528 + r32 * 16);
      const float s1 = sum_halves(t[0]), s2 = sum_halves(t[1]), s3 = sum_halves(t[2]);
      float mbv, rden;
      ln_stats(s1, s2, mbv, rden);
      ho_st1(rs, HO_KEY + (2 * cbk + hh) * (S * 4) + (tt * 32 + r32) * 4, sort_key((s3 - mbv * swr) * rden, tt * 32 + r32));
    }
    STAMP(11);
    {
      constexpr unsigned G = T_OFF, M1 = T_OFF + 256, C2 = T_OFF + 512, SEL = T_OFF + 640, C1 = T_OFF + 1024;
      const int ht = hw * 64 + fresh_lane(), t3 = ht & 3, t4 = ht >> 2;
      u32x4 kv[2];
      if (hw == 0) __builtin_amdgcn_s_sleep(DLY_KEY); else __builtin_amdgcn_s_sleep(DLY_KEY + 8);
      poll_all<2>(rs, HO_KEY + xcd * (S * 4) + ht * 32, 16, kv);
      STAMP(12);
      const unsigned m0 = max4(kv[0]), m1 = max4(kv[1]);
      unsigned gm = max(m0, m1); gm = max(gm, dpp_u<DPP_X1>(gm)); gm = max(gm, dpp_u<DPP_X2>(gm));
      lds_stu(G + t4 * 4, gm);
      BARRIER();
      {
        unsigned rk = count_gt(lds_ldu4(G + t3 * 64), lds_ldu4(G + t3 * 64 + 16), gm) + count_gt(lds_ldu4(G + t3 * 64 + 32), lds_ldu4(G + t3 * 64 + 48), gm);
        rk += dpp_u<DPP_X1>(rk); rk += dpp_u<DPP_X2>(rk);
        if (rk < 8) {
          lds_stu4(C1 + (rk * 8 + t3 * 2) * 16, kv[0]); lds_stu4(C1 + (rk * 8 + t3 * 2 + 1) * 16, kv[1]);
          *(LAS u32x2*)(uintptr_t)(M1 + (rk * 8 + t3 * 2) * 4) = u32x2{m0, m1};
        }
      }
      BARRIER();
      {
        const unsigned cm = lds_ldu(M1 + t4 * 4), key = lds_ldu(C1 + ht * 4);
        unsigned rk = count_gt(lds_ldu4(M1 + t3 * 64), lds_ldu4(M1 + t3 * 64 + 16), cm) + count_gt(lds_ldu4(M1 + t3 * 64 + 32), lds_ldu4(M1 + t3 * 64 + 48), cm);
        rk += dpp_u<DPP_X1>(rk); rk += dpp_u<DPP_X2>(rk);
        if (rk < 8) lds_stu(C2 + rk * 16 + t3 * 4, key);
      }
      BARRIER();
      {
        const unsigned key = lds_ldu(C2 + (ht >> 3) * 4);
        const u32x4 o = lds_ldu4(C2 + (ht & 7) * 16);
        const unsigned rk = sum8_u(((key - o[0]) >> 31) + ((key - o[1]) >> 31) + ((key - o[2]) >> 31) + ((key - o[3]) >> 31));
        if (rk < 8) lds_stu(SEL + rk * 4, key);
      }
      BARRIER();
    }
    STAMP(15); STAMP_OUT(0);
    return;
  }

  unsigned tag;
  {
    const int rt = wid & 1, hd = wid >> 1, sw = hd >> 1, par = rt ^ (hd & 1);
    tag = __builtin_amdgcn_raw_buffer_load_b32(rs, WS_EP, 0, 16);
    const int xr0 = rt * 32 + sw * 16 + i16, xr1 = xr0 ^ 16;
    const int xf = (i16 >> 1) & 7;
    const unsigned xb0 = 24576 + (xr0 >> 3) * 1024 + (xr0 & 7) * 128, xb1 = 24576 + (xr1 >> 3) * 1024 + (xr1 & 7) * 128;
    const unsigned xoA = ((4 * par + g) ^ xf) * 16, xoB = ((4 * (par ^ 1) + g) ^ xf) * 16;
    const unsigned wl = (8 * g + (i16 >> 2)) * 128 + ((hd ^ (((i16 >> 3) & 1) | ((g & 1) << 1))) * 32) + (i16 & 3) * 8;
    unsigned x0A = xb0 + xoA, x1A = xb1 + xoA, x0B = xb0 + xoB, x1B = xb1 + xoB, wAa = wl + par * 4096, wBa = wl + (par ^ 1) * 4096;
#define RDX(a, so) lds_ld128((a) + (so))
#define RDW(a, so, t) cat8(tr_read((a) + (so) + (t) * 8192), tr_read((a) + (so) + (t) * 8192 + 512))
    f32x4 aq0 = {}, aq1 = {}, ak0 = {}, ak1 = {}, av0 = {}, av1 = {};
    f32x4 rsum = {}, rsq = {}, cs0 = {}, cs1 = {}, cs2 = {};
    bf16x8 ones8 = {0x3f80, 0x3f80, 0x3f80, 0x3f80, 0x3f80, 0x3f80, 0x3f80, 0x3f80};
    PIN(ones8);
    if (tid < 16) lds_stu(L_RDY + tid * 4, 0u);
    const unsigned pv = L_PRIV + wid * PRIV, po = L_PRIV + (wid ^ 1) * PRIV;
    unsigned ad_rs = (g == (i16 >> 2)) ? L_RS + ((rt * 4 + hd) * 16 + i16) * 8 : pv + lane * 16;
    unsigned ad_cq = (i16 == 0) ? po + 1280 + g * 16 : pv + lane * 16, ad_cv = po + 1280 + 128 + i16 * 4;
    unsigned rd_rs = L_RS + ((rt * 4 + (g & 1) * 2) * 16 + i16) * 8, rd_cq = pv + 1280 + g * 16, rd_cv = pv + 1280 + 128 + i16 * 4;
    unsigned ad_mr = pv + 2048 + r32 * 4, rd_mr = pv + 2048 + (sw * 16 + 4 * g) * 4;
    const int ahead = hq * 4 + hd, tile = rb * 2 + rt;
    unsigned go_k = WS_KV + (ahead * 64 + tile) * 2048 + (lane ^ (sw * 16)) * 16, go_q = WS_Q + (ahead * 64 + tile) * 1024 + (lane ^ (sw * 16)) * 16;
    unsigned go_v = WS_KV + (ahead * 64 + tile) * 2048 + 1024 + ((g & 1) ^ sw) * 512 + (hh * 16 + i16) * 16;
    PIN(ad_rs); PIN(ad_cq); PIN(ad_cv); PIN(rd_rs); PIN(rd_cq); PIN(rd_cv); PIN(ad_mr); PIN(rd_mr); PIN(go_k); PIN(go_q); PIN(go_v);
    PIN(x0A); PIN(x1A); PIN(x0B); PIN(x1B); PIN(wAa); PIN(wBa);
    PIN(aq0); PIN(aq1); PIN(ak0); PIN(ak1); PIN(av0); PIN(av1); PIN(rsum); PIN(rsq); PIN(cs0); PIN(cs1); PIN(cs2);
    BARRIER();
    STAMP(1);
    AFrag fa, fb;
    fa.q = RDW(wAa, 0, 0); fa.x0 = RDX(x0A, 0); fa.x1 = RDX(x1A, 0); fa.k = RDW(wAa, 0, 1); fa.v = RDW(wAa, 0, 2);
    fb.q = RDW(wBa, 0, 0); fb.x0 = RDX(x0B, 0); fb.x1 = RDX(x1B, 0); fb.k = RDW(wBa, 0, 1); fb.v = RDW(wBa, 0, 2);
#define AMF(acc, a, b) asm volatile("v_mfma_f32_16x16x32_bf16 %0, %1, %2, %0" : "+v"(acc) : "v"(a), "v"(b))
#define A_CHUNK(so) do { \
      asm volatile("s_barrier" ::: "memory"); \
      SBAR(); \
      AMF(aq0, fa.q, fa.x0); AMF(aq1, fa.q, fa.x1); \
      AMF(cs0, fa.q, ones8); \
      SBAR(); fa.q = RDW(wAa, so, 0); SBAR(); \
      AMF(ak0, fa.k, fa.x0); AMF(ak1, fa.k, fa.x1); \
      AMF(cs1, fa.k, ones8); \
      SBAR(); fa.k = RDW(wAa, so, 1); SBAR(); \
      AMF(av0, fa.x0, fa.v); AMF(av1, fa.x1, fa.v); \
      SBAR(); fa.x1 = RDX(x1A, so); SBAR(); \
      AMF(cs2, ones8, fa.v); AMF(rsum, ones8, fa.x0); AMF(rsq, fa.x0, fa.x0); \
      SBAR(); fa.x0 = RDX(x0A, so); fa.v = RDW(wAa, so, 2); SBAR(); \
      AMF(aq0, fb.q, fb.x0); AMF(aq1, fb.q, fb.x1); \
      SBAR(); fb.q = RDW(wBa, so, 0); SBAR(); \
      AMF(ak0, fb.k, fb.x0); AMF(ak1, fb.k, fb.x1); \
      SBAR(); fb.k = RDW(wBa, so, 1); SBAR(); \
      AMF(av0, fb.x0, fb.v); AMF(av1, fb.x1, fb.v); \
      SBAR(); fb.x0 = RDX(x0B, so); fb.x1 = RDX(x1B, so); fb.v = RDW(wBa, so, 2); SBAR(); \
    } while (0)
#pragma unroll 1
    for (int c2 = 0; c2 < 3; ++c2) {
      A_CHUNK(32768);
      x0A ^= 65536u; x1A ^= 65536u; x0B ^= 65536u; x1B ^= 65536u; wAa ^= 65536u; wBa ^= 65536u;
      A_CHUNK(0);
    }
    A_CHUNK(32768);
    rsum = MFMA16(ones8, fa.x0, rsum); rsq = MFMA16(fa.x0, fa.x0, rsq);
    cs0 = MFMA16(fa.q, ones8, cs0); cs1 = MFMA16(fa.k, ones8, cs1); cs2 = MFMA16(ones8, fa.v, cs2);
    SBAR();
    aq0 = MFMA16(fa.q, fa.x0, aq0); aq1 = MFMA16(fa.q, fa.x1, aq1);
    SBAR();
    {
      const bool b0 = lane & 1, b1 = lane & 2;
      const float dlo = b0 ? rsq[1] : rsq[0], dhi = b0 ? rsq[3] : rsq[2];
      *(LAS f32x2*)(uintptr_t)ad_rs = f32x2{rsum[0], b1 ? dhi : dlo};
      lds_stf4(ad_cq, cs0); lds_stf4(ad_cq + 64, cs1); lds_stf(ad_cv, cs2[0]);
    }
    STAMP(2);
    BARRIER();
    {
      const f32x2 pa = *(const LAS f32x2*)(uintptr_t)rd_rs, pb = *(const LAS f32x2*)(uintptr_t)(rd_rs + 128);
      const f32x4 oq = lds_ldf4(rd_cq), ok = lds_ldf4(rd_cq + 64);
      const float ov = lds_ldf(rd_cv);
      SBAR();
      aq0 = MFMA16(fb.q, fb.x0, aq0); aq1 = MFMA16(fb.q, fb.x1, aq1);
      ak0 = MFMA16(fa.k, fa.x0, ak0); ak1 = MFMA16(fa.k, fa.x1, ak1);
      SBAR();
      float mb, rden;
      ln_stats(pa[0] + pb[0], pa[1] + pb[1], mb, rden);
      lds_stf(ad_mr, mb); lds_stf(ad_mr + 128, rden);
      const f32x4 cq = cs0 + oq, ck = cs1 + ok;
      const float cv = cs2[0] + ov;
      const auto sm = __builtin_amdgcn_permlane16_swap(__float_as_uint(mb), __float_as_uint(mb), false, false);
      const auto sd = __builtin_amdgcn_permlane16_swap(__float_as_uint(rden), __float_as_uint(rden), false, false);
      const float m0 = __uint_as_float(sw ? sm[1] : sm[0]), m1 = __uint_as_float(sw ? sm[0] : sm[1]);
      const float d0 = __uint_as_float(sw ? sd[1] : sd[0]), d1v = __uint_as_float(sw ? sd[0] : sd[1]);
      unsigned a0, a1, b0, b1;
      const float e0 = d0 * QSCALE, e1 = d1v * QSCALE;
      a0 = pack_bf16((aq0[0] - m0 * cq[0]) * e0, (aq0[1] - m0 * cq[1]) * e0); a1 = pack_bf16((aq0[2] - m0 * cq[2]) * e0, (aq0[3] - m0 * cq[3]) * e0);
      b0 = pack_bf16((aq1[0] - m1 * cq[0]) * e1, (aq1[1] - m1 * cq[1]) * e1); b1 = pack_bf16((aq1[2] - m1 * cq[2]) * e1, (aq1[3] - m1 * cq[3]) * e1);
      { auto r0 = __builtin_amdgcn_permlane16_swap(a0, b0, false, false); auto r1 = __builtin_amdgcn_permlane16_swap(a1, b1, false, false);
        ho_st(rs, go_q, u32x4{r0[0], r1[0], r0[1], r1[1]}); }
      SBAR();
      ak0 = MFMA16(fb.k, fb.x0, ak0); ak1 = MFMA16(fb.k, fb.x1, ak1);
      av0 = MFMA16(fa.x0, fa.v, av0); av1 = MFMA16(fa.x1, fa.v, av1);
      av0 = MFMA16(fb.x0, fb.v, av0); av1 = MFMA16(fb.x1, fb.v, av1);
      WAIT_LGKM0();
      const f32x4 mv0 = lds_ldf4(rd_mr), mv1 = lds_ldf4(rd_mr ^ 64), dv0 = lds_ldf4(rd_mr + 128), dv1 = lds_ldf4((rd_mr ^ 64) + 128);
      u32x4 pk, pvv;
      a0 = pack_bf16((ak0[0] - m0 * ck[0]) * d0, (ak0[1] - m0 * ck[1]) * d0); a1 = pack_bf16((ak0[2] - m0 * ck[2]) * d0, (ak0[3] - m0 * ck[3]) * d0);
      b0 = pack_bf16((ak1[0] - m1 * ck[0]) * d1v, (ak1[1] - m1 * ck[1]) * d1v); b1 = pack_bf16((ak1[2] - m1 * ck[2]) * d1v, (ak1[3] - m1 * ck[3]) * d1v);
      { auto r0 = __builtin_amdgcn_permlane16_swap(a0, b0, false, false); auto r1 = __builtin_amdgcn_permlane16_swap(a1, b1, false, false);
        pk = u32x4{r0[0], r1[0], r0[1], r1[1]}; }
      a0 = pack_bf16((av0[0] - mv0[0] * cv) * dv0[0], (av0[1] - mv0[1] * cv) * dv0[1]); a1 = pack_bf16((av0[2] - mv0[2] * cv) * dv0[2], (av0[3] - mv0[3] * cv) * dv0[3]);
      b0 = pack_bf16((av1[0] - mv1[0] * cv) * dv1[0], (av1[1] - mv1[1] * cv) * dv1[1]); b1 = pack_bf16((av1[2] - mv1[2] * cv) * dv1[2], (av1[3] - mv1[3] * cv) * dv1[3]);
      { auto r0 = __builtin_amdgcn_permlane16_swap(a0, b0, false, false); auto r1 = __builtin_amdgcn_permlane16_swap(a1, b1, false, false);
        pvv = u32x4{r0[0], r1[0], r0[1], r1[1]}; }
      tag = __builtin_amdgcn_readfirstlane(tag) + 1u;
      const u32x4 fl = {tag, ~tag, tag, ~tag};
      PIN(pk); PIN(pvv);
      STAMP(3);
      if (rb < 4) { ho_st(rs, go_k, pk); ho_st(rs, go_v, pvv); asm volatile("s_waitcnt vmcnt(2)" : "+v"(code_pf) :: "memory"); }
      else asm volatile("s_waitcnt vmcnt(0)" : "+v"(code_pf) :: "memory");
      if (lane < 8) ho_st(rs, WS_FQ + (ahead * 64 + tile) * 128 + lane * 16, fl);
      if (lane == 0) __hip_atomic_fetch_add((LAS unsigned*)(uintptr_t)(L_RDY + 36), 1u, __ATOMIC_RELAXED, __HIP_MEMORY_SCOPE_WORKGROUP);
      if (rb >= 4) {
        ho_st(rs, go_k, pk); ho_st(rs, go_v, pvv); WAIT_VM(0);
      } else WAIT_VM(1);
      if (lane < 8) ho_st(rs, WS_FA + (ahead * 64 + tile) * 128 + lane * 16, fl);
    }
    STAMP(4);
  }

  const int qg = wid & 3, kh = wid >> 2;
  u32x4 fw0 = {}, fw1 = {}, fw2 = {}, fw3 = {}, fw4 = {}, fw5 = {};
  const char* fb0 = isd1 ? (const char*)wg + wid * (64 * F * 2) + d1 * 32 : (const char*)wd + wid * (32 * D * 2) + sub * 32;
  const char* fb2 = isd1 ? (const char*)wu + wid * (64 * F * 2) + d1 * 32 : fb0 + 2 * (256 * D * 2);
  const unsigned fst = isd1 ? 32 * F * 2 : 256 * D * 2;
  const unsigned fs4 = isd1 ? 0u : 2 * fst, fs5 = (isd1 || wid >= 3) ? 0u : 3 * fst;
  unsigned fvo = (lane >> 1) * (isd1 ? F * 2 : D * 2) + (lane & 1) * 16;
  const int fit = (isd1 || isd2) ? 6 : -1;
  f32x16 oa = {}, ob = {};
  int sidx = (int)0xDDDDDDDDu;
  bf16x8 qa, qb2;
  float mref;
  {
    unsigned ka = kh * 32 * KVT + lane * 16;
    unsigned va0 = kh * 32 * KVT + (r32 < 16 ? 1024 + (hh * 16 + r32) * 16 : (r32 == 16 ? 2048 : 2064));
    unsigned va1 = va0 + (r32 < 16 ? 512 : 0);
    unsigned qad = L_STG + qg * 2048 + kh * 1024 + lane * 16;
    PIN(ka); PIN(va0); PIN(va1); PIN(qad); PIN(fvo); PIN(oa); PIN(ob); PIN(sidx);
    PIN(fw0); PIN(fw1); PIN(fw2); PIN(fw3); PIN(fw4); PIN(fw5);
    __builtin_amdgcn_s_setprio(2);
    wait_rdy(1u);
    STAMP(6);
    bf16x8 k0 = lds_ld128(ka), k1;
    qa = lds_ld128(qad); qb2 = lds_ld128(qad ^ 1024);
    bf16x8 v00 = lds_ld128(va0), v01 = lds_ld128(va1), v10, v11 = lds_ld128(va0);
    f32x16 ng, sA, sB;
    {
      const f32x16 z = {};
      sA = MFMA32(k0, qa, z); sB = MFMA32(k0, qb2, z);
      mref = max_halves(fmaxf(tile_max(sA), tile_max(sB)));
    }
#pragma unroll
    for (int j = 0; j < 16; ++j) { ng[j] = -mref; sA[j] -= mref; if (j >= 8) sB[j] = 0.f; }
    PIN(ng); PIN(sA);
    for (int it = 0; it < 8; ++it) {
      FSTAMP(32 + 2 * it);
      { unsigned tv_;
        asm volatile("s_cmp_lg_u32 %[it], %[fit]\n\ts_cbranch_scc1 .Lff%=\n\t"
          "global_load_dwordx4 %[a0], %[vo], %[b0]\n\tglobal_load_dwordx4 %[a2], %[vo], %[b2]\n\tv_add_u32 %[tv], %[st], %[vo]\n\t"
          "global_load_dwordx4 %[a1], %[tv], %[b0]\n\tglobal_load_dwordx4 %[a3], %[tv], %[b2]\n\tv_add_u32 %[tv], %[s4], %[vo]\n\t"
          "global_load_dwordx4 %[a4], %[tv], %[b2]\n\tv_add_u32 %[tv], %[s5], %[vo]\n\t"
          "global_load_dwordx4 %[a5], %[tv], %[b2]\n.Lff%=:\n\t"
          "s_cmp_lt_u32 %[it], 5\n\ts_cbranch_scc1 .Lpr%=\n\ts_setprio 1\n\ts_cmp_lt_u32 %[it], 7\n\ts_cbranch_scc1 .Lpr%=\n\ts_setprio 0\n.Lpr%=:"
          : [a0] "+v"(fw0), [a1] "+v"(fw1), [a2] "+v"(fw2), [a3] "+v"(fw3), [a4] "+v"(fw4), [a5] "+v"(fw5), [tv] "=&v"(tv_)
          : [it] "s"(it), [fit] "s"(fit), [vo] "v"(fvo), [b0] "s"(fb0), [b2] "s"(fb2), [st] "s"(fst), [s4] "s"(fs4), [s5] "s"(fs5) : "memory", "scc"); }
      SBAR();
      TWO_TILES(0);
      PIN(sA); PIN(sB);
      const unsigned need = (0x8042u >> (4 * it)) & 15u;
      if (need) {
        wait_rdy(need);
        if (it == 0 && fresh_lane() == 0) __hip_atomic_fetch_add((LAS unsigned*)(uintptr_t)(L_RDY + 16 + qg * 4), 1u, __ATOMIC_RELAXED, __HIP_MEMORY_SCOPE_WORKGROUP);
      }
      SBAR();
      TWO_TILES(2);
      ka += 4 * KVT; va0 += 4 * KVT; va1 += 4 * KVT;
    }
    ob = SMF(v11, hi8(sB), ob);
  }
  asm volatile("s_waitcnt vmcnt(0)" : "+v"(fw0), "+v"(fw1), "+v"(fw2), "+v"(fw3), "+v"(fw4), "+v"(fw5) :: "memory");
  {
  RELANE();
  const unsigned priv = L_PRIV + wid * PRIV;
  float ma = mref, mb = mref;
  float la = sum_halves(oa[8]), lb = sum_halves(ob[8]);
  {
    float t = 0.f;
#pragma unroll
    for (int j = 0; j < 8; ++j) t += fabsf(oa[j]) + fabsf(ob[j]);
    const bool ok = (la <= 1.0e18f) && (lb <= 1.0e18f) && (la >= 1.0e-18f) && (lb >= 1.0e-18f) && (t <= 3.0e38f);
    if (__builtin_expect(!__all(ok), 0)) {
      const unsigned kaddr = kh * 32 * KVT + lane * 16;
      const unsigned vaddr0 = kh * 32 * KVT + (r32 < 16 ? 1024 + (hh * 16 + r32) * 16 : (r32 == 16 ? 2048 : 2064));
      const unsigned vaddr1 = vaddr0 + (r32 < 16 ? 512 : 0);
      const f32x16 z = {};
      ma = -INFINITY; mb = -INFINITY;
      for (int t2 = 0; t2 < 32; ++t2) {
        const bf16x8 kf = lds_ld128(kaddr + t2 * KVT);
        ma = fmaxf(ma, tile_max(MFMA32(kf, qa, z)));
        mb = fmaxf(mb, tile_max(MFMA32(kf, qb2, z)));
      }
      ma = max_halves(ma); mb = max_halves(mb);
      oa = z; ob = z;
      for (int t2 = 0; t2 < 32; ++t2) {
        const bf16x8 kf = lds_ld128(kaddr + t2 * KVT), v0 = lds_ld128(vaddr0 + t2 * KVT), v1 = lds_ld128(vaddr1 + t2 * KVT);
        f32x16 sa = MFMA32(kf, qa, z), sb = MFMA32(kf, qb2, z);
#pragma unroll
        for (int j = 0; j < 16; ++j) { sa[j] = EX(sa[j] - ma); sb[j] = EX(sb[j] - mb); }
        oa = SMF(v0, lo8(sa), oa); oa = SMF(v1, hi8(sa), oa);
        ob = SMF(v0, lo8(sb), ob); ob = SMF(v1, hi8(sb), ob);
      }
      la = sum_halves(oa[8]); lb = sum_halves(ob[8]);
    }
  }
  STAMP(7);
  const int row = tid >> 4, c8 = tid & 15;
  const int goff = ((tt * 32 + row) * D + cbk * 128 + c8 * 8) * 2;
  const int po_probe = HO_O + (tt * H + wid * 4 + (lane & 3)) * 1024 + 1020;
  unsigned probe;
  {
    lds_stf4(priv + lane * 16, f32x4{ob[0], ob[1], ob[2], ob[3]}); lds_stf4(priv + 1024 + lane * 16, f32x4{ob[4], ob[5], ob[6], ob[7]});
    *(LAS f32x2*)(uintptr_t)(priv + 2048 + lane * 8) = f32x2{lb, mb};
    BARRIER();
    probe = __builtin_amdgcn_raw_buffer_load_b32(rs, po_probe, 0, 16);
    SBAR();
    const unsigned yp = L_PRIV + (wid ^ 4) * PRIV;
    const f32x2 y2 = *(const LAS f32x2*)(uintptr_t)(yp + 2048 + lane * 8);
    const f32x4 y0 = lds_ldf4(yp + lane * 16), y1 = lds_ldf4(yp + 1024 + lane * 16);
    const float M = fmaxf(ma, y2[1]);
    float f1 = EX(ma - M), f2 = EX(y2[1] - M);
    const float inv = RCP(la * f1 + y2[0] * f2);
    f1 *= inv; f2 *= inv;
    u32x4 po;
    po[0] = san(pack_bf16(oa[0] * f1 + y0[0] * f2, oa[1] * f1 + y0[1] * f2));
    po[1] = san(pack_bf16(oa[2] * f1 + y0[2] * f2, oa[3] * f1 + y0[3] * f2));
    po[2] = san(pack_bf16(oa[4] * f1 + y1[0] * f2, oa[5] * f1 + y1[1] * f2));
    po[3] = san(pack_bf16(oa[6] * f1 + y1[2] * f2, oa[7] * f1 + y1[3] * f2));
    ho_st(rs, HO_O + ((qb * 8 + qg * 2 + kh) * H + head) * 1024 + lane * 16, po);
  }
  STAMP(8);

  {
    const int oo = HO_O + (tt * H + wid * 4) * 1024 + lane * 16;
    for (unsigned spin = 0;; ++spin) {
      if (__builtin_amdgcn_ballot_w64(probe == SENT) == 0 || spin > SPIN_CAP) break;
      POLL_PAUSE(1);
      probe = __builtin_amdgcn_raw_buffer_load_b32(rs, po_probe, 0, 16);
    }
    u32x4 o0 = ho_ld(rs, oo), o1 = ho_ld(rs, oo + 1024), o2 = ho_ld(rs, oo + 2048), o3 = ho_ld(rs, oo + 3072);
    for (unsigned spin = 0;; ++spin) {
      const unsigned long long p0 = __builtin_amdgcn_ballot_w64(pend(o0)), p1 = __builtin_amdgcn_ballot_w64(pend(o1));
      const unsigned long long p2 = __builtin_amdgcn_ballot_w64(pend(o2)), p3 = __builtin_amdgcn_ballot_w64(pend(o3));
      if ((p0 | p1 | p2 | p3) == 0 || spin > SPIN_CAP) break;
      POLL_PAUSE(1);
      if (p0) o0 = ho_ld(rs, oo);
      if (p1) o1 = ho_ld(rs, oo + 1024);
      if (p2) o2 = ho_ld(rs, oo + 2048);
      if (p3) o3 = ho_ld(rs, oo + 3072);
    }
    const unsigned oa2 = wid * 4096 + lane * 16;
    lds_stu4(oa2, o0); lds_stu4(oa2 + 1024, o1); lds_stu4(oa2 + 2048, o2); lds_stu4(oa2 + 3072, o3);
    WAIT_LGKM0();
    if (lane == 0) __hip_atomic_fetch_or((LAS unsigned*)(uintptr_t)(L_RDY + 32), 1u << wid, __ATOMIC_RELAXED, __HIP_MEMORY_SCOPE_WORKGROUP);
  }
  STAMP(9);
  if (isd1) {
    const unsigned fa = F_OFF + wid * 2048 + lane * 16;
    lds_stu4(fa, fw0); lds_stu4(fa + 1024, fw1); lds_stu4(fa + 16384, fw2); lds_stu4(fa + 17408, fw3);
  } else if (isd2) {
    const unsigned fa = F_OFF + wid * 1024 + lane * 16;
    lds_stu4(fa, fw0); lds_stu4(fa + 8192, fw1); lds_stu4(fa + 16384, fw2); lds_stu4(fa + 24576, fw3); lds_stu4(fa + 32768, fw4); lds_stu4(fa + 40960, fw5);
  }
  WAIT_LGKM0();
  bf16x8 ff[6];
  {
    const unsigned ft = (8 * g + (i16 >> 2)) * 32 + (i16 & 3) * 8;
    const unsigned fl = F_OFF + wid * (isd1 ? 2048 : 1024) + ft, s1 = isd1 ? 1024 : 8192;
    ff[0] = cat8(tr_read(fl), tr_read(fl + 128)); ff[1] = cat8(tr_read(fl + s1), tr_read(fl + s1 + 128));
    ff[2] = cat8(tr_read(fl + 16384), tr_read(fl + 16384 + 128)); ff[3] = cat8(tr_read(fl + 16384 + s1), tr_read(fl + 16384 + s1 + 128));
    ff[4] = cat8(tr_read(fl + 32768), tr_read(fl + 32768 + 128)); ff[5] = cat8(tr_read(fl + 40960), tr_read(fl + 40960 + 128));
  }
  if (wid < 6) {
    const int rec = ((cbk + 1 + (wid >> 1)) & 3) * 4 + (wid & 1) * 2 + hh;
    u32x4 sv[1];
    wait_word(L_RDY + 40, 4u);
    __builtin_amdgcn_s_sleep(DLY_ST);
    poll_all<1>(rs, HO_ST + (tt * 16 + rec) * 512 + r32 * 16, 0, sv);
    lds_stu4(S_OFF + rec * 528 + r32 * 16, sv[0]);
    STAMP(10);
  }
  BARRIER();
  STAMP(11);
  const u32x4 hv = lds_ldu4(X_OFF + row * 272 + c8 * 16);
  u32x4 ov;
  {
    const f32x2 pr = *(const LAS f32x2*)(uintptr_t)(S_OFF + c8 * 528 + row * 16);
    f32x2 st;
    { float mbv, rden; ln_stats(row16_sum(pr[0]), row16_sum(pr[1]), mbv, rden); st = f32x2{mbv, rden}; }
    u32x4 lv;
#pragma unroll
    for (int j = 0; j < 4; ++j) {
      const float h0 = bf_lo(hv[j]), h1 = bf_hi(hv[j]);
      const unsigned d = pack_bf16(h0 - st[0], h1 - st[0]);
      lv[j] = pack_bf16(bf_lo(d) * st[1], bf_hi(d) * st[1]);
      ov[j] = pack_bf16(h0 + bf_lo(lv[j]), h1 + bf_hi(lv[j]));
    }
    lv = u32x4{san(lv[0]), san(lv[1]), san(lv[2]), san(lv[3])};
    PIN(lv);
    BARRIER();
    ho_st(rs, HO_LN + goff, lv);
  }
  BARRIER(); BARRIER(); BARRIER();
  STAMP(13);
  const unsigned long long tsel = __builtin_amdgcn_s_memtime();
  const unsigned mykey = lds_ldu(T_OFF + 640 + (lane & 7) * 4);
  const int myrow = 2047 - (int)(mykey & 0x7ffu);
  u32x4 ax[2];
  const int axo = HO_LN + (myrow * D + wid * 64 + g * 8) * 2;
  if (isd1) { ax[0] = ho_ld(rs, axo); ax[1] = ho_ld(rs, axo + 64); }
  SBAR();
  int sel = -1;
  {
    const int grow = tt * 32 + row;
#pragma unroll
    for (int c = 0; c < 8; ++c) if (__builtin_amdgcn_readlane(myrow, c) == grow) sel = c;
    if (sel >= 0) ho_st(rs, HO_HS + (sel * D + cbk * 128 + c8 * 8) * 2, u32x4{san(hv[0]), san(hv[1]), san(hv[2]), san(hv[3])});
  }
  if (isd1) {
    if (__builtin_amdgcn_ballot_w64(max(max4(ax[0]), max4(ax[1])) == SENT) != 0) poll_all<2>(rs, axo, 64, ax);
    f32x4 ag = {}, au = {};
    ag = MFMA16(BC8(ax[0]), ff[0], ag); au = MFMA16(BC8(ax[0]), ff[2], au);
    ag = MFMA16(BC8(ax[1]), ff[1], ag); au = MFMA16(BC8(ax[1]), ff[3], au);
    lds_stf4(R_OFF + wid * 1024 + lane * 16, hh ? au : ag);
    BARRIER();
    if (wid == 0) {
      f32x4 sum = lds_ldf4(R_OFF + lane * 16);
#pragma unroll
      for (int w2 = 1; w2 < 8; ++w2) sum += lds_ldf4(R_OFF + w2 * 1024 + lane * 16);
      const auto x0 = __builtin_amdgcn_permlane32_swap(__float_as_uint(sum[0]), __float_as_uint(sum[2]), false, false);
      const auto x1 = __builtin_amdgcn_permlane32_swap(__float_as_uint(sum[1]), __float_as_uint(sum[3]), false, false);
      const float g0 = rbf(__uint_as_float(x0[0])), u0 = rbf(__uint_as_float(x0[1])), g1 = rbf(__uint_as_float(x1[0])), u1 = rbf(__uint_as_float(x1[1]));
      const float a0f = rbf(g0 * sigmoid(g0)) * u0, a1f = rbf(g1 * sigmoid(g1)) * u1;
      const float n0 = dpp_f<DPP_X1>(a0f), n1 = dpp_f<DPP_X1>(a1f);
      const bool odd = lane & 1;
      const int tok = 4 * (g & 1) + 2 * hh + (odd ? 1 : 0);
      ho_st1(rs, HO_ACT + d1 * 256 + tok * 32 + (i16 >> 1) * 4, san(odd ? pack_bf16(n1, a1f) : pack_bf16(a0f, n0)));
    }
    STAMP(14);
  }
  if (sel < 0) __builtin_amdgcn_raw_buffer_store_b128(ov, __builtin_amdgcn_make_buffer_rsrc(out, 0, S * D * 2, 0x00020000), goff, 0, 16);
  if (b == 0 && tid == 0) __builtin_amdgcn_raw_buffer_store_b32(tag, rs, WS_EP, 0, 16);
  if (isd2) {
    const int n0 = sub * 16;
    unsigned hw = SENT, lw = SENT, fkey = 0;
    const bool odd = lane & 1;
    const int tok = 4 * (g & 1) + 2 * hh + (odd ? 1 : 0), cp = i16 >> 1;
    float sig = 0.f;
    fkey = lds_ldu(T_OFF + 640 + tok * 4);
    const int frow = 2047 - (int)(fkey & 0x7ffu);
    sig = rbf(sigmoid(key_score(fkey)));
    PIN(sig);
    u32x4 av[6];
    const int ao = HO_ACT + ((wid * 2 + (g >> 1)) * 8 + (lane & 7)) * 32 + (g & 1) * 16;
    if (wid == 0) {
      while (__builtin_amdgcn_s_memtime() - tsel < DLY_ACT / 2) __builtin_amdgcn_s_sleep(1);
      for (unsigned spin = 0;; ++spin) {
        hw = __builtin_amdgcn_raw_buffer_load_b32(rs, HO_HS + (tok * D + n0 + cp * 2) * 2, 0, 16);
        lw = __builtin_amdgcn_raw_buffer_load_b32(rs, HO_LN + (frow * D + n0 + cp * 2) * 2, 0, 16);
        if (__builtin_amdgcn_ballot_w64(max(hw, lw) == SENT) == 0 || spin > SPIN_CAP) break;
        POLL_PAUSE(1);
      }
    }
    while (__builtin_amdgcn_s_memtime() - tsel < DLY_ACT) __builtin_amdgcn_s_sleep(1);
    STAMP(12);
    poll_all<6>(rs, ao, 4096, av, wid < 3 ? 0 : 4096);
    STAMP(5);
    f32x4 y = {}, y2 = {};
    y = MFMA16(BC8(av[0]), ff[0], y); y2 = MFMA16(BC8(av[1]), ff[1], y2);
    y = MFMA16(BC8(av[2]), ff[2], y); y2 = MFMA16(BC8(av[3]), ff[3], y2);
    y = MFMA16(BC8(av[4]), ff[4], y);
    if (wid < 3) y2 = MFMA16(BC8(av[5]), ff[5], y2);
    lds_stf4(R_OFF + wid * 1024 + lane * 16, hh ? y2 : y);
    BARRIER();
    if (wid == 0) {
      f32x4 sum = lds_ldf4(R_OFF + lane * 16);
#pragma unroll
      for (int w2 = 1; w2 < 8; ++w2) sum += lds_ldf4(R_OFF + w2 * 1024 + lane * 16);
      const auto x0 = __builtin_amdgcn_permlane32_swap(__float_as_uint(sum[0]), __float_as_uint(sum[2]), false, false);
      const auto x1 = __builtin_amdgcn_permlane32_swap(__float_as_uint(sum[1]), __float_as_uint(sum[3]), false, false);
      const float t0 = __uint_as_float(x0[0]) + __uint_as_float(x0[1]), t1 = __uint_as_float(x1[0]) + __uint_as_float(x1[1]);
      const float n0f = dpp_f<DPP_X1>(t0), n1f = dpp_f<DPP_X1>(t1);
      const float ylo = rbf(rbf(odd ? n1f : t0) * sig), yhi = rbf(rbf(odd ? t1 : n0f) * sig);
      *(unsigned*)((char*)out + ((size_t)(2047 - (int)(fkey & 0x7ffu)) * D + n0 + cp * 2) * 2) =
          pack_bf16(bf_lo(hw) + rbf(bf_lo(lw) + ylo), bf_hi(hw) + rbf(bf_hi(lw) + yhi));
    }
  }
  }
  STAMP(15); STAMP_OUT(0);
}

extern "C" void kernel_launch(void* const* d_in, const int* in_sizes, int n_in, void* d_out, int out_size, void* d_ws, size_t ws_size,
                              hipStream_t stream) {
  (void)hipFuncSetAttribute((const void*)k_fused, hipFuncAttributeMaxDynamicSharedMemorySize, LDS_BYTES);
  k_fused<<<256, 768, LDS_BYTES, stream>>>((const uint16_t*)d_in[0], (const uint16_t*)d_in[1], (const uint16_t*)d_in[2], (const uint16_t*)d_in[4],
                                           (const uint16_t*)d_in[5], (const uint16_t*)d_in[6], (const uint16_t*)d_in[7], (char*)d_ws, (uint16_t*)d_out);
}
```
